```python
import torch

def _softmax(x, axis=-1):
    return torch.softmax(x, dim=axis)
def _gelu(x):
    return torch.nn.functional.gelu(x, approximate="tanh")
def _silu(x):
    return torch.nn.functional.silu(x)
def _sigmoid(x):
    return torch.sigmoid(x)
def _layernorm(x, axis=-1, eps=1e-5):
    m = x.mean(dim=axis, keepdim=True)
    v = x.var(dim=axis, keepdim=True, unbiased=False)
    return (x - m) / torch.sqrt(v + eps)
def _scatter_add(tgt, idx, src, axis=0):
    return tgt.clone().index_add_(axis, idx, src)
def _segment_sum(src, seg, n):
    out = torch.zeros((n,) + src.shape[1:], dtype=src.dtype)
    return out.index_add_(0, seg, src)
def _pad(x, pw):
    flat = tuple(v for pair in reversed(pw) for v in pair)
    return torch.nn.functional.pad(x, flat)

kernel_name = 'gated_mix__linear_attn_block__glu_gated_glu__prompt'

def setup_inputs():

    _w = (lambda a: (lambda t: t.to(torch.bfloat16) if t.is_floating_point() else t)(torch.as_tensor(a)))
    def _A_setup():
        B, S, D = 1, 2048, 640
        return {
            "x": _w((lambda *s: torch.randn(*s, dtype=torch.bfloat16) * 0.05)(B, S, D)),
            "wqkv": _w((lambda *s: torch.randn(*s, dtype=torch.bfloat16) * 0.8)(D, 3 * D)),
            "wo": _w((lambda *s: torch.randn(*s, dtype=torch.bfloat16) * 0.2)(D, D)),
        }
    _A_in = _A_setup()

    def _B_setup():
        B, S, D, F = 1, 2048, 640, 1536
        return {
            "x": _w((lambda *s: torch.randn(*s, dtype=torch.bfloat16) * 0.05)(B, S, D)),
            "w_gate": _w((lambda *s: torch.randn(*s, dtype=torch.bfloat16) * 0.4)(D, F)),
            "w_up": _w((lambda *s: torch.randn(*s, dtype=torch.bfloat16) * 0.05)(D, F)),
            "w_down": _w((lambda *s: torch.randn(*s, dtype=torch.bfloat16) * 0.05)(F, D)),
        }
    _B_in = _B_setup()

    return {
        "A_x": _A_in["x"],
        "A_wqkv": _A_in["wqkv"],
        "A_wo": _A_in["wo"],
        "B_x": _B_in["x"],
        "B_w_gate": _B_in["w_gate"],
        "B_w_up": _B_in["w_up"],
        "B_w_down": _B_in["w_down"],
        "gate_w": _w((lambda *s: torch.randn(*s, dtype=torch.bfloat16) * 0.05)(640) * 16.0),
    }

def reference(A_x, A_wqkv, A_wo, B_x, B_w_gate, B_w_up, B_w_down, gate_w):
    def _A_ref(x, wqkv, wo):
        B, S, D = x.shape
        H = 32; Dh = D // H
        qkv = (lambda x, *d: x.permute(*d))((x @ wqkv).reshape(B, S, 3, H, Dh), 2, 0, 3, 1, 4)
        q, k, v = qkv[0], qkv[1], qkv[2]
        phi = lambda t: torch.where(t > 0, t + 1, torch.exp(t)) + 1e-6
        pq, pk = phi(q), phi(k)
        kv = torch.einsum("bhsd,bhse->bhde", pk, v)
        z = torch.einsum("bhsd->bhd", pk)
        num = torch.einsum("bhqd,bhde->bhqe", pq, kv)
        den = torch.einsum("bhqd,bhd->bhq", pq, z)[..., None] + 1e-6
        o = num / den
        return (lambda x, *d: x.permute(*d))(o, 0, 2, 1, 3).reshape(B, S, D) @ wo

    def _B_ref(x, w_gate, w_up, w_down):
        g = torch.einsum("bsd,df->bsf", x, w_gate)
        u = torch.einsum("bsd,df->bsf", x, w_up)
        h = _sigmoid(g) * u
        return torch.einsum("bsf,fd->bsd", h, w_down)

    x = A_x
    g = _sigmoid((x * gate_w).sum(-1, keepdims=True))
    a = _A_ref(x, A_wqkv, A_wo)
    b = _B_ref(x, B_w_gate, B_w_up, B_w_down)
    return g * a + (1 - g) * b
```

```cpp
#include <hip/hip_runtime.h>
#include <hip/hip_bf16.h>
typedef unsigned short u16;
__device__ __forceinline__ float bf2f(u16 v){ return __uint_as_float(((unsigned)v)<<16); }
__device__ __forceinline__ u16 f2bf(float f){ __hip_bfloat16 h = __float2bfloat16(f); return *(u16*)&h; }
__device__ __forceinline__ float rbf(float f){ return bf2f(f2bf(f)); }
constexpr int S=2048,D=640,F=1536,H=32,DH=20;
__global__ void gemm(const u16*A,const u16*B,u16*C,int M,int N,int K){
  __shared__ float As[16][17], Bs[16][17];
  int tx=threadIdx.x&15, ty=threadIdx.x>>4; int row=blockIdx.y*16+ty, col=blockIdx.x*16+tx; float acc=0;
  for(int k0=0;k0<K;k0+=16){ As[ty][tx]=bf2f(A[row*K+k0+tx]); Bs[ty][tx]=bf2f(B[(k0+ty)*N+col]); __syncthreads();
    for(int k=0;k<16;k++) acc+=As[ty][k]*Bs[k][tx]; __syncthreads(); }
  C[row*N+col]=f2bf(acc);
}
__device__ float phi(float t){ float r = t>0? rbf(t+1.f) : rbf(__expf(t)); return rbf(r+1e-6f); }
__global__ void kvk(const u16*qkv, float*kv){
  int h=blockIdx.x; int t=threadIdx.x; if(t>=420) return; int d=t/21,e=t%21; float acc=0;
  for(int s=0;s<S;s++){ float pk=phi(bf2f(qkv[s*1920+640+h*20+d])); float v= e<20? bf2f(qkv[s*1920+1280+h*20+e]) : 1.f; acc+=pk*v; }
  kv[h*420+t]=rbf(acc);
}
__global__ void ok(const u16*qkv,const float*kv,u16*o){ int s=blockIdx.x; int c=threadIdx.x; int h=c/20,e=c%20; float num=0,den=0;
  for(int d=0;d<20;d++){ float pq=phi(bf2f(qkv[s*1920+h*20+d])); num+=pq*kv[h*420+d*21+e]; den+=pq*kv[h*420+d*21+20]; }
  num=rbf(num); den=rbf(rbf(den)+1e-6f); o[s*640+c]=f2bf(num/den);
}
__global__ void hk(const u16*g,const u16*u,u16*h){ int i=blockIdx.x*256+threadIdx.x; float sg=rbf(1.f/(1.f+__expf(-bf2f(g[i])))); h[i]=f2bf(sg*bf2f(u[i])); }
__global__ void fin(const u16*x,const u16*gw,const u16*a,const u16*b,u16*out){ int s=blockIdx.x; __shared__ float red[640]; int c=threadIdx.x;
  red[c]=rbf(bf2f(x[s*640+c])*bf2f(gw[c])); __syncthreads(); float sum=0; for(int i=0;i<640;i++) sum+=red[i]; sum=rbf(sum);
  float g=rbf(1.f/(1.f+__expf(-sum))); float r=rbf(g*bf2f(a[s*640+c]))+rbf(rbf(1.f-g)*bf2f(b[s*640+c])); out[s*640+c]=f2bf(r);
}
extern "C" void kernel_launch(void* const* d_in, const int* in_sizes, int n_in, void* d_out, int out_size, void* d_ws, size_t ws_size, hipStream_t stream) {
  const u16*x=(const u16*)d_in[0],*wqkv=(const u16*)d_in[1],*wo=(const u16*)d_in[2],*wg=(const u16*)d_in[4],*wu=(const u16*)d_in[5],*wd=(const u16*)d_in[6],*gw=(const u16*)d_in[7];
  char*ws=(char*)d_ws; u16*qkv=(u16*)ws; u16*g=(u16*)(ws+(16<<20)); u16*u=(u16*)(ws+(32<<20)); u16*h=(u16*)(ws+(48<<20)); u16*o=(u16*)(ws+(64<<20)); u16*a=(u16*)(ws+(80<<20)); u16*b=(u16*)(ws+(96<<20)); float*kv=(float*)(ws+(112<<20));
  gemm<<<dim3(1920/16,S/16),256,0,stream>>>(x,wqkv,qkv,S,1920,D);
  gemm<<<dim3(F/16,S/16),256,0,stream>>>(x,wg,g,S,F,D);
  gemm<<<dim3(F/16,S/16),256,0,stream>>>(x,wu,u,S,F,D);
  kvk<<<32,448,0,stream>>>(qkv,kv);
  ok<<<S,640,0,stream>>>(qkv,kv,o);
  hk<<<S*F/256,256,0,stream>>>(g,u,h);
  gemm<<<dim3(D/16,S/16),256,0,stream>>>(o,wo,a,S,D,D);
  gemm<<<dim3(D/16,S/16),256,0,stream>>>(h,wd,b,S,D,F);
  fin<<<S,640,0,stream>>>(x,gw,a,b,(u16*)d_out);
}
```

```cpp
#include <hip/hip_runtime.h>
#include <hip/hip_bf16.h>

typedef unsigned short u16;
typedef __bf16 bf16x8 __attribute__((ext_vector_type(8)));
typedef __bf16 bf16x4 __attribute__((ext_vector_type(4)));
typedef __bf16 bf16x2 __attribute__((ext_vector_type(2)));
typedef float f32x16 __attribute__((ext_vector_type(16)));
typedef float f32x4 __attribute__((ext_vector_type(4)));
typedef float f32x2 __attribute__((ext_vector_type(2)));
typedef unsigned u32x4 __attribute__((ext_vector_type(4)));
typedef unsigned u32x2 __attribute__((ext_vector_type(2)));
#define LAS __attribute__((address_space(3)))
#define GAS __attribute__((address_space(1)))
typedef LAS char* ldsp;

constexpr int S = 2048, D = 640, F = 1536;
constexpr float LOG2E = 1.4426950408889634f;
constexpr int KB_ALL = 68;
constexpr size_t A2_BYTES = (size_t)128 * KB_ALL * 1024;
constexpr size_t Q_OFF = A2_BYTES, Q_BYTES = (size_t)64 * 40 * 1024;
constexpr size_t KVP_OFF = Q_OFF + Q_BYTES, KVP_BYTES = (size_t)8 * 8 * 1680 * 4;
constexpr size_t G_OFF = KVP_OFF + KVP_BYTES, G_BYTES = S * 4;
constexpr size_t FLAG_OFF = G_OFF + G_BYTES;
constexpr int RSRC3 = 0x00020000;

struct Params {
  const u16 *x, *wqkv, *wo, *wg, *wu, *wd, *gw;
  u16* out;
  char* ws;
  unsigned long long* stamps;
};

#define STAMP(i) do {} while (0)

#define WAIT_VM(n) asm volatile("s_waitcnt vmcnt(" #n ")" ::: "memory")
#define WAIT_LGKM0() asm volatile("s_waitcnt lgkmcnt(0)" ::: "memory")
#define BARRIER() do { asm volatile("" ::: "memory"); __builtin_amdgcn_s_barrier(); asm volatile("" ::: "memory"); } while (0)
#define SCHED_FENCE() __builtin_amdgcn_sched_barrier(0)
#define SG_MFMA(n) __builtin_amdgcn_sched_group_barrier(0x8, n, 0)
#define SG_DS(n) __builtin_amdgcn_sched_group_barrier(0x100, n, 0)

__device__ __forceinline__ __amdgpu_buffer_rsrc_t mk_rsrc(const void* p, unsigned bytes) {
  return __builtin_amdgcn_make_buffer_rsrc((void*)p, 0, bytes, RSRC3);
}
__device__ __forceinline__ void dma16(__amdgpu_buffer_rsrc_t r, ldsp dst, unsigned voff, unsigned soff) {
  __builtin_amdgcn_raw_ptr_buffer_load_lds(r, dst, 16, voff, soff, 0, 0);
}
__device__ __forceinline__ bf16x8 lds_b128(ldsp a) { return *(LAS bf16x8*)a; }
__device__ __forceinline__ bf16x8 lds_tr2(ldsp a0, ldsp a1) {
  bf16x4 lo = __builtin_amdgcn_ds_read_tr16_b64_v4bf16((LAS bf16x4*)a0);
  bf16x4 hi = __builtin_amdgcn_ds_read_tr16_b64_v4bf16((LAS bf16x4*)a1);
  return __builtin_shufflevector(lo, hi, 0, 1, 2, 3, 4, 5, 6, 7);
}
__device__ __forceinline__ f32x16 mfma32(bf16x8 a, bf16x8 b, f32x16 c) { return __builtin_amdgcn_mfma_f32_32x32x16_bf16(a, b, c, 0, 0, 0); }
__device__ __forceinline__ f32x4 mfma16(bf16x8 a, bf16x8 b, f32x4 c) { return __builtin_amdgcn_mfma_f32_16x16x32_bf16(a, b, c, 0, 0, 0); }
__device__ __forceinline__ unsigned pk2(float a, float b) {
  bf16x2 v = {(__bf16)a, (__bf16)b};
  return __builtin_bit_cast(unsigned, v);
}
__device__ __forceinline__ bf16x8 pack8(const f32x16& c, int o) {
  u32x4 r = {pk2(c[o], c[o + 1]), pk2(c[o + 2], c[o + 3]), pk2(c[o + 4], c[o + 5]), pk2(c[o + 6], c[o + 7])};
  return __builtin_bit_cast(bf16x8, r);
}
__device__ __forceinline__ float phi(float t) {
  float e = __builtin_amdgcn_exp2f(t * LOG2E);
  return fmaxf(t, 1e-6f) + __builtin_amdgcn_fmed3f(e, 0.f, 1.f);
}
__device__ __forceinline__ float sigmoidf(float t) { return __builtin_amdgcn_rcpf(1.f + __builtin_amdgcn_exp2f(-LOG2E * t)); }

constexpr int K1_SLOT = 53248, K1_LDS = 3 * K1_SLOT;
constexpr int NST1 = 10;

template <bool TB>
__device__ __forceinline__ void k1_loader(const Params& p, ldsp lds, int lw, int row0, int colA, int colB) {
  const int l = threadIdx.x & 63;
  const __amdgpu_buffer_rsrc_t rx = mk_rsrc(p.x, S * D * 2);
  const __amdgpu_buffer_rsrc_t rw0 = TB ? mk_rsrc(p.wg, D * F * 2) : mk_rsrc(p.wqkv, D * 3 * D * 2);
  const __amdgpu_buffer_rsrc_t rw1 = TB ? mk_rsrc(p.wu, D * F * 2) : rw0;
  constexpr int NX = TB ? 5 : 8;
  constexpr int XB = TB ? 20480 : 32768;
  const int i0 = lw * NX;
  unsigned xv[2];
#pragma unroll
  for (int e = 0; e < 2; ++e) xv[e] = (l >> 3) * 1280 + (((l & 7) ^ ((((i0 + e) & 1) << 2) + (l >> 4))) << 4);
  const unsigned xs0 = (unsigned)(row0 + 8 * i0) * 1280;
  unsigned wv[5];
  unsigned ws0;
  if (TB) {
    wv[0] = (l >> 4) * 3072 + (((l & 15) ^ ((l >> 4) << 2)) << 4);
    ws0 = (unsigned)(16 * lw) * 3072 + colA * 2;
  } else {
#pragma unroll
    for (int j = 0; j < 5; ++j) {
      const int c = 64 * j + l, r = c / 20, cc = c % 20;
      wv[j] = r * 3840 + (cc < 10 ? colA * 2 + cc * 16 : colB * 2 + (cc - 10) * 16);
    }
    ws0 = (unsigned)(16 * lw) * 3840;
  }
  auto issue = [&](int st, int slot) {
    ldsp sb = lds + slot * K1_SLOT;
    if (TB) {
#pragma unroll
      for (int j = 0; j < 4; ++j) dma16(rw0, sb + XB + (4 * lw + j) * 1024, wv[0], ws0 + (unsigned)(st * 64 + 4 * j) * 3072);
#pragma unroll
      for (int j = 0; j < 4; ++j) dma16(rw1, sb + XB + 16384 + (4 * lw + j) * 1024, wv[0], ws0 + (unsigned)(st * 64 + 4 * j) * 3072);
    } else {
#pragma unroll
      for (int j = 0; j < 5; ++j) dma16(rw0, sb + XB + (5 * lw + j) * 1024, wv[j], ws0 + (unsigned)(st * 64) * 3840);
    }
#pragma unroll
    for (int j = 0; j < NX; ++j) dma16(rx, sb + (i0 + j) * 1024, xv[j & 1], xs0 + j * 8 * 1280 + st * 128);
  };
  issue(0, 0); issue(1, 1); issue(2, 2);
  WAIT_VM(26);
  BARRIER();
  int slot = 0;
  for (int j = 0; j < NST1 - 3; ++j) {
    WAIT_VM(13);
    BARRIER();
    issue(j + 3, slot);
    slot = slot == 2 ? 0 : slot + 1;
  }
  WAIT_VM(13); BARRIER();
  WAIT_VM(0);  BARRIER();
}

struct FragA { bf16x8 w[5], x[2]; };
struct FragB { bf16x8 w[2], x[5]; };

template <bool SWAP>
__device__ __forceinline__ void k1_mainA(ldsp lds, int w, f32x16 (&acc)[5][2]) {
  const int l = threadIdx.x & 63, l31 = l & 31, h = l >> 5;
  const int xr = 64 * w + l31;
  const int xa = xr * 128 + ((h ^ ((l31 >> 1) & 7)) << 4);
  const int wa = 32768 + (8 * h + ((l & 15) >> 2)) * 320 + ((l >> 4) & 1) * 32 + (l & 3) * 8;
  auto load = [&](FragA& f, ldsp sb, int ks) {
    ldsp xp = sb + (xa ^ (ks << 5));
    ldsp wp = sb + wa + ks * 16 * 320;
    f.x[0] = lds_b128(xp);
    f.w[0] = lds_tr2(wp, wp + 1280);
    f.x[1] = lds_b128(xp + 4096);
#pragma unroll
    for (int n = 1; n < 5; ++n) f.w[n] = lds_tr2(wp + n * 64, wp + n * 64 + 1280);
  };
  auto mma = [&](const FragA& f) {
#pragma unroll
    for (int n = 0; n < 5; ++n)
#pragma unroll
      for (int s = 0; s < 2; ++s) acc[n][s] = SWAP ? mfma32(f.x[s], f.w[n], acc[n][s]) : mfma32(f.w[n], f.x[s], acc[n][s]);
#pragma unroll
    for (int i = 0; i < 6; ++i) { SG_MFMA(1); SG_DS(2); }
    SG_MFMA(4);
    SCHED_FENCE();
  };
  BARRIER();
  FragA f0, f1;
  int so = 0;
  load(f0, lds, 0);
  SCHED_FENCE();
  for (int j = 0; j < NST1; ++j) {
    ldsp sb = lds + so;
    load(f1, sb, 1); mma(f0);
    load(f0, sb, 2); mma(f1);
    load(f1, sb, 3); mma(f0);
    so = so == 2 * K1_SLOT ? 0 : so + K1_SLOT;
    WAIT_LGKM0();
    BARRIER();
    SCHED_FENCE();
    load(f0, lds + so, 0); mma(f1);
  }
  WAIT_LGKM0();
}

__device__ __forceinline__ void k1_mainB(ldsp lds, int w, f32x16 (&acc)[2][5]) {
  const int l = threadIdx.x & 63, l31 = l & 31, h = l >> 5, q = (l & 15) >> 2;
  const int xa = l31 * 128 + ((h ^ ((l31 >> 1) & 7)) << 4);
  const int wa = 20480 + (8 * h + q) * 256 + ((w ^ q) << 6) + ((l >> 4) & 1) * 32 + (l & 3) * 8;
  auto load = [&](FragB& f, ldsp sb, int ks) {
    ldsp xp = sb + (xa ^ (ks << 5));
    ldsp wp = sb + wa + ks * 4096;
    f.w[0] = lds_tr2(wp, wp + 1024);
    f.x[0] = lds_b128(xp);
    f.w[1] = lds_tr2(wp + 16384, wp + 16384 + 1024);
#pragma unroll
    for (int s = 1; s < 5; ++s) f.x[s] = lds_b128(xp + s * 4096);
  };
  auto mma = [&](const FragB& f) {
#pragma unroll
    for (int s = 0; s < 5; ++s)
#pragma unroll
      for (int n = 0; n < 2; ++n) acc[n][s] = mfma32(f.w[n], f.x[s], acc[n][s]);
#pragma unroll
    for (int i = 0; i < 4; ++i) { SG_MFMA(1); SG_DS(2); }
    SG_MFMA(1); SG_DS(1);
    SG_MFMA(5);
    SCHED_FENCE();
  };
  BARRIER();
  FragB f0, f1;
  int so = 0;
  load(f0, lds, 0);
  SCHED_FENCE();
  for (int j = 0; j < NST1; ++j) {
    ldsp sb = lds + so;
    load(f1, sb, 1); mma(f0);
    load(f0, sb, 2); mma(f1);
    load(f1, sb, 3); mma(f0);
    so = so == 2 * K1_SLOT ? 0 : so + K1_SLOT;
    WAIT_LGKM0();
    BARRIER();
    SCHED_FENCE();
    load(f0, lds + so, 0); mma(f1);
  }
  WAIT_LGKM0();
}

__global__ __launch_bounds__(512, 2) void k1(Params p) {
  extern __shared__ __attribute__((aligned(16))) char smem[];
  ldsp lds = (ldsp)smem;
  constexpr int kern = 0; (void)kern;
  const int b = blockIdx.x, xcd = b & 7, idx = b >> 3, cg = xcd & 3, rg = xcd >> 2;
  const int w = __builtin_amdgcn_readfirstlane(threadIdx.x >> 6);
  const int l = threadIdx.x & 63, l31 = l & 31, h = l >> 5;
  STAMP(0);
  if (idx < 12) {
    const int rt = 4 * rg + idx / 3, ty = idx % 3;
    const int hg = 2 * cg + ty - 1;
    if (w >= 4) {
      k1_loader<false>(p, lds, w - 4, rt * 256, ty == 0 ? 160 * cg : 640 + 80 * hg, ty == 0 ? 160 * cg + 80 : 1280 + 80 * hg);
      STAMP(1);
      return;
    }
    f32x16 acc[5][2];
#pragma unroll
    for (int n = 0; n < 5; ++n) { acc[n][0] = f32x16{}; acc[n][1] = f32x16{}; }
    if (ty == 0) {
      k1_mainA<false>(lds, w, acc);
      STAMP(1);
      GAS char* qp = (GAS char*)(p.ws + Q_OFF) + ((size_t)(rt * 8 + 2 * w) * 40 + 10 * cg) * 1024 + l * 16;
#pragma unroll
      for (int s = 0; s < 2; ++s)
#pragma unroll
        for (int n = 0; n < 5; ++n) {
          *(GAS bf16x8*)(qp + (s * 40 + 2 * n) * 1024) = pack8(acc[n][s], 0);
          *(GAS bf16x8*)(qp + (s * 40 + 2 * n + 1) * 1024) = pack8(acc[n][s], 8);
        }
    } else {
      k1_mainA<true>(lds, w, acc);
      STAMP(1);
      f32x16 kv[7];
#pragma unroll
      for (int i = 0; i < 7; ++i) kv[i] = f32x16{};
#pragma unroll
      for (int s = 0; s < 2; ++s) {
        bf16x8 kf[3][2], vf[3][2];
#pragma unroll
        for (int n = 0; n < 3; ++n) {
          f32x16 t;
#pragma unroll
          for (int r = 0; r < 16; ++r) t[r] = phi(acc[n][s][r]);
          kf[n][0] = pack8(t, 0); kf[n][1] = pack8(t, 8);
        }
#pragma unroll
        for (int n = 0; n < 3; ++n) { vf[n][0] = pack8(acc[n + 2][s], 0); vf[n][1] = pack8(acc[n + 2][s], 8); }
        const u32x4 ones = {0x3F803F80u, 0x3F803F80u, 0x3F803F80u, 0x3F803F80u};
#pragma unroll
        for (int t = 0; t < 2; ++t) {
          if (l31 < 16) vf[0][t] = __builtin_bit_cast(bf16x8, ones);
          kv[0] = mfma32(kf[0][t], vf[0][t], kv[0]);
          kv[1] = mfma32(kf[0][t], vf[1][t], kv[1]);
          kv[2] = mfma32(kf[1][t], vf[0][t], kv[2]);
          kv[3] = mfma32(kf[1][t], vf[1][t], kv[3]);
          kv[4] = mfma32(kf[1][t], vf[2][t], kv[4]);
          kv[5] = mfma32(kf[2][t], vf[0][t], kv[5]);
          kv[6] = mfma32(kf[2][t], vf[2][t], kv[6]);
        }
      }
      BARRIER();
      ldsp rp = lds + w * 7 * 5120 + l * 80;
#pragma unroll
      for (int i = 0; i < 7; ++i)
#pragma unroll
        for (int g = 0; g < 4; ++g) *(LAS f32x4*)(rp + i * 5120 + g * 16) = f32x4{kv[i][4 * g], kv[i][4 * g + 1], kv[i][4 * g + 2], kv[i][4 * g + 3]};
      WAIT_LGKM0();
      BARRIER();
      GAS float* kp = (GAS float*)(p.ws + KVP_OFF) + (rt * 8 + hg) * 1680;
      for (int en = threadIdx.x; en < 1680; en += 256) {
        const int hd = en / 420, rem = en % 420, d = rem / 21, e = rem % 21;
        const int dk = 20 * hd + d, kt = dk >> 5, row = dk & 31;
        int vt, col;
        if (e == 20) { vt = 0; col = 0; } else { const int ev = 20 * hd + e + 16; vt = ev >> 5; col = ev & 31; }
        const int tile = kt == 0 ? vt : kt == 1 ? 2 + vt : (vt == 0 ? 5 : 6);
        const int off = tile * 5120 + (col + 32 * ((row >> 2) & 1)) * 80 + ((row & 3) + 4 * (row >> 3)) * 4;
        float s = 0.f;
#pragma unroll
        for (int ww = 0; ww < 4; ++ww) s += *(LAS float*)(lds + ww * 7 * 5120 + off);
        kp[en] = s;
      }
    }
  } else {
    const int j = idx - 12 + 20 * rg;
    if (j < 39) {
      const int rt = j / 3, ct = 3 * cg + j % 3;
      if (w >= 4) { k1_loader<true>(p, lds, w - 4, rt * 160, ct * 128, 0); STAMP(1); return; }
      f32x16 acc[2][5];
#pragma unroll
      for (int s = 0; s < 5; ++s) { acc[0][s] = f32x16{}; acc[1][s] = f32x16{}; }
      k1_mainB(lds, w, acc);
      STAMP(1);
      const __amdgpu_buffer_rsrc_t ra = mk_rsrc(p.ws, (unsigned)A2_BYTES);
      const unsigned vo = (unsigned)(((l >> 4) & 1) * KB_ALL * 1024 + h * 256 + (l & 15) * 16);
      const unsigned so = (unsigned)((rt * 10) * KB_ALL + ct * 4 + w) * 1024;
#pragma unroll
      for (int s = 0; s < 5; ++s) {
        f32x16 t;
#pragma unroll
        for (int r = 0; r < 16; ++r) t[r] = acc[1][s][r] * sigmoidf(acc[0][s][r]);
        __builtin_amdgcn_raw_buffer_store_b128(__builtin_bit_cast(u32x4, pack8(t, 0)), ra, vo + so + s * 2 * KB_ALL * 1024, 0, 0);
        __builtin_amdgcn_raw_buffer_store_b128(__builtin_bit_cast(u32x4, pack8(t, 8)), ra, vo + so + s * 2 * KB_ALL * 1024 + 512, 0, 0);
      }
    } else {
      if (cg == 0 && threadIdx.x < 32) *(GAS unsigned*)(p.ws + FLAG_OFF + threadIdx.x * 256) = 0u;
      const int l15 = l & 15;
      float gwf[5][8];
#pragma unroll
      for (int c = 0; c < 5; ++c) {
        const u32x4 v = *(const GAS u32x4*)((const GAS char*)p.gw + (l15 + 16 * c) * 16);
#pragma unroll
        for (int e = 0; e < 4; ++e) { gwf[c][2 * e] = __uint_as_float(v[e] << 16); gwf[c][2 * e + 1] = __uint_as_float(v[e] & 0xFFFF0000u); }
      }
      const int r0 = cg * 512 + w * 64 + (l >> 4);
      for (int it = 0; it < 16; it += 4) {
        u32x4 v[4][5];
#pragma unroll
        for (int u = 0; u < 4; ++u)
#pragma unroll
          for (int c = 0; c < 5; ++c) v[u][c] = *(const GAS u32x4*)((const GAS char*)p.x + (size_t)(r0 + 4 * (it + u)) * 1280 + (l15 + 16 * c) * 16);
#pragma unroll
        for (int u = 0; u < 4; ++u) {
          float s = 0.f;
#pragma unroll
          for (int c = 0; c < 5; ++c)
#pragma unroll
            for (int e = 0; e < 4; ++e) {
              s = fmaf(__uint_as_float(v[u][c][e] << 16), gwf[c][2 * e], s);
              s = fmaf(__uint_as_float(v[u][c][e] & 0xFFFF0000u), gwf[c][2 * e + 1], s);
            }
          s += __shfl_xor(s, 1); s += __shfl_xor(s, 2); s += __shfl_xor(s, 4); s += __shfl_xor(s, 8);
          if (l15 == 0) *(GAS float*)(p.ws + G_OFF + (size_t)(r0 + 4 * (it + u)) * 4) = sigmoidf(s);
        }
      }
    }
  }
  STAMP(2);
}

constexpr int K2_SLOT = 20480, K2_NS = 7, K2_LDS = K2_NS * K2_SLOT;
constexpr int NST2 = 34, NST2_H = 24;

__device__ __forceinline__ void o_producer(const Params& p, ldsp lds, int hg, int rq, int w) {
  const int tid = threadIdx.x, l = tid & 63, l31 = l & 31, h = l >> 5;
  constexpr int KS = 168;
  const int s32 = rq * 16 + w * 2;
  u32x4 qf[2][5];
#pragma unroll
  for (int s = 0; s < 2; ++s)
#pragma unroll
    for (int t = 0; t < 5; ++t) qf[s][t] = *(const GAS u32x4*)((const GAS char*)(p.ws + Q_OFF) + ((size_t)(s32 + s) * 40 + 5 * hg + t) * 1024 + l * 16);
  float gv[2];
#pragma unroll
  for (int s = 0; s < 2; ++s) gv[s] = *(const GAS float*)(p.ws + G_OFF + (size_t)((s32 + s) * 32 + l31) * 4);
  float part[4][8];
#pragma unroll
  for (int i = 0; i < 4; ++i) {
    const int en = min(tid + 512 * i, 1679);
#pragma unroll
    for (int r = 0; r < 8; ++r) part[i][r] = *(const GAS float*)(p.ws + KVP_OFF + (size_t)((r * 8 + hg) * 1680 + en) * 4);
  }
  for (int i = tid; i < 96 * KS / 8; i += 512) *(LAS u32x2*)(lds + i * 8) = u32x2{0u, 0u};
  WAIT_LGKM0();
  BARRIER();
#pragma unroll
  for (int i = 0; i < 4; ++i) {
    const int en = tid + 512 * i;
    float s = 0.f;
#pragma unroll
    for (int r = 0; r < 8; ++r) s += part[i][r];
    if (en < 1680) {
      const int hd = en / 420, rem = en % 420, d = rem / 21, e = rem % 21;
      const __bf16 v = (__bf16)s;
      if (e < 20) *(LAS __bf16*)(lds + (20 * hd + e) * KS + (20 * hd + d) * 2) = v;
      else { *(LAS __bf16*)(lds + (80 + hd) * KS + (20 * hd + d) * 2) = v; *(LAS __bf16*)(lds + (84 + hd) * KS + (20 * hd + d) * 2) = v; }
    }
  }
  bf16x8 pq[2][5];
#pragma unroll
  for (int s = 0; s < 2; ++s)
#pragma unroll
    for (int t = 0; t < 5; ++t) {
      u32x4 r;
#pragma unroll
      for (int e = 0; e < 4; ++e) r[e] = pk2(phi(__uint_as_float(qf[s][t][e] << 16)), phi(__uint_as_float(qf[s][t][e] & 0xFFFF0000u)));
      pq[s][t] = __builtin_bit_cast(bf16x8, r);
    }
  WAIT_LGKM0();
  BARRIER();
  f32x16 acc[2][3];
#pragma unroll
  for (int s = 0; s < 2; ++s)
#pragma unroll
    for (int et = 0; et < 3; ++et) acc[s][et] = f32x16{};
#pragma unroll
  for (int et = 0; et < 3; ++et)
#pragma unroll
    for (int t = 0; t < 5; ++t) {
      if ((et == 0 && t > 2) || (et == 1 && t == 0)) continue;
      ldsp ap = lds + (32 * et + l31) * KS + (16 * t + 4 * h) * 2;
      const bf16x4 lo = *(LAS bf16x4*)ap, hi = *(LAS bf16x4*)(ap + 16);
      const bf16x8 a = __builtin_shufflevector(lo, hi, 0, 1, 2, 3, 4, 5, 6, 7);
      acc[0][et] = mfma32(a, pq[0][t], acc[0][et]);
      acc[1][et] = mfma32(a, pq[1][t], acc[1][et]);
    }
  const __amdgpu_buffer_rsrc_t ra = mk_rsrc(p.ws, (unsigned)A2_BYTES);
  const unsigned vo = (unsigned)(((l >> 4) & 1) * KB_ALL * 1024 + h * 256 + (l & 15) * 16);
#pragma unroll
  for (int s = 0; s < 2; ++s) {
    f32x4 sc;
#pragma unroll
    for (int hd = 0; hd < 4; ++hd) sc[hd] = gv[s] * __builtin_amdgcn_rcpf(acc[s][2][8 + hd] + 1e-6f);
#pragma unroll
    for (int pc = 0; pc < 5; ++pc) {
      f32x16 t;
#pragma unroll
      for (int g = 0; g < 2; ++g) {
        const int e0 = 16 * pc + 8 * g;
        float fa = sc[e0 / 20], fb = sc[(e0 + 4) / 20];
        asm volatile("" : "+v"(fa), "+v"(fb));
        const float f = h ? fb : fa;
#pragma unroll
        for (int r = 0; r < 4; ++r) t[4 * g + r] = acc[s][pc >> 1][8 * (pc & 1) + 4 * g + r] * f;
      }
      const int k16 = 5 * hg + pc;
      const unsigned so = (unsigned)(((s32 + s) * 2 * KB_ALL + 48 + (k16 >> 1)) * 1024 + (k16 & 1) * 512);
      __builtin_amdgcn_raw_buffer_store_b128(__builtin_bit_cast(u32x4, pack8(t, 0)), ra, vo + so, 0, 16);
    }
  }
  WAIT_VM(0);
  BARRIER();
  if (tid == 0) __hip_atomic_store((GAS unsigned*)(p.ws + FLAG_OFF + (rq * 8 + hg) * 256), 1u, __ATOMIC_RELAXED, __HIP_MEMORY_SCOPE_AGENT);
}

__device__ __forceinline__ void k2_loader(const Params& p, ldsp lds, int lw, int rt, int ct) {
  const int l = threadIdx.x & 63;
  const __amdgpu_buffer_rsrc_t ra = mk_rsrc(p.ws, (unsigned)A2_BYTES);
  const __amdgpu_buffer_rsrc_t rd = mk_rsrc(p.wd, F * D * 2);
  const __amdgpu_buffer_rsrc_t ro = mk_rsrc(p.wo, D * D * 2);
  const unsigned av = l * 16;
  const unsigned wv = (l >> 3) * 1280 + (((l & 7) ^ ((l >> 4) << 1)) << 4);
  unsigned as[3];
#pragma unroll
  for (int j = 0; j < 3; ++j) { const int f = 3 * lw + j; as[j] = (unsigned)((rt * 6 + (f >> 1)) * KB_ALL + (f & 1)) * 1024; }
  const unsigned ws0 = (unsigned)(16 * lw) * 1280 + ct * 128;
  auto issue = [&](int st, int slot, bool opart) {
    ldsp sb = lds + slot * K2_SLOT;
    const unsigned wo_ = ws0 + (unsigned)(opart ? st - NST2_H : st) * 64 * 1280;
#pragma unroll
    for (int j = 0; j < 2; ++j) dma16(opart ? ro : rd, sb + 12288 + (2 * lw + j) * 1024, wv, wo_ + j * 8 * 1280);
#pragma unroll
    for (int j = 0; j < 3; ++j) {
      if (opart) __builtin_amdgcn_raw_ptr_buffer_load_lds(ra, sb + (3 * lw + j) * 1024, 16, av, as[j] + st * 2048, 0, 16);
      else dma16(ra, sb + (3 * lw + j) * 1024, av, as[j] + st * 2048);
    }
  };
#pragma unroll
  for (int s = 0; s < K2_NS; ++s) issue(s, s, false);
  WAIT_VM(30);
  BARRIER();
  int slot = 0;
  for (int j = 0; j < NST2_H - K2_NS; ++j) {
    WAIT_VM(25);
    BARRIER();
    issue(j + K2_NS, slot, false);
    slot = slot == K2_NS - 1 ? 0 : slot + 1;
  }
  for (int j = NST2_H - K2_NS; j < NST2 - K2_NS; ++j) {
    WAIT_VM(25);
    BARRIER();
    issue(j + K2_NS, slot, true);
    slot = slot == K2_NS - 1 ? 0 : slot + 1;
  }
  WAIT_VM(25); BARRIER();
  WAIT_VM(20); BARRIER();
  WAIT_VM(15); BARRIER();
  WAIT_VM(10); BARRIER();
  WAIT_VM(5);  BARRIER();
  WAIT_VM(0);  BARRIER();
}

struct Frag2 { bf16x8 w[2], a[3]; };

__global__ __launch_bounds__(512, 2) void k2(Params p) {
  extern __shared__ __attribute__((aligned(16))) char smem[];
  ldsp lds = (ldsp)smem;
  constexpr int kern = 1; (void)kern;
  const int b = blockIdx.x, xcd = b & 7, idx = b >> 3, cg = xcd & 1, rg = xcd >> 1;
  const int w = __builtin_amdgcn_readfirstlane(threadIdx.x >> 6);
  const int l = threadIdx.x & 63;
  STAMP(0);
  if (idx < 4) { o_producer(p, lds, cg * 4 + idx, rg, w); STAMP(2); return; }
  const int ti = idx - 4;
  if (ti >= 28 - (rg & 1)) return;
  const int t = rg * 27 + ((rg + 1) >> 1) + ti, rt = t / 5, ct = cg * 5 + t % 5;
  if (w >= 4) { k2_loader(p, lds, w - 4, rt, ct); STAMP(1); return; }
  const int wn = w & 1, ws = w >> 1, kg = l >> 4, q = (l & 15) >> 2;
  float gi[3];
#pragma unroll
  for (int s = 0; s < 3; ++s) gi[s] = 1.f - *(const GAS float*)(p.ws + G_OFF + (size_t)min(rt * 96 + ws * 48 + s * 16 + (l & 15), S - 1) * 4);
  const int aa = ws * 6144 + l * 16;
  const int wrow = 16 * (kg >> 1) + 4 * (kg & 1) + q;
  int wa[2];
#pragma unroll
  for (int n = 0; n < 2; ++n) wa[n] = 12288 + wrow * 128 + (((2 * wn + n) ^ ((wrow >> 1) & 3)) << 5) + (l & 3) * 8;
  f32x4 acc[2][3];
#pragma unroll
  for (int n = 0; n < 2; ++n)
#pragma unroll
    for (int s = 0; s < 3; ++s) acc[n][s] = f32x4{};
  auto load = [&](Frag2& f, ldsp sb, int kb) {
#pragma unroll
    for (int n = 0; n < 2; ++n) f.w[n] = lds_tr2(sb + wa[n] + kb * 4096, sb + wa[n] + kb * 4096 + 1024);
#pragma unroll
    for (int s = 0; s < 3; ++s) f.a[s] = lds_b128(sb + aa + s * 2048 + kb * 1024);
  };
  auto mma = [&](const Frag2& f) {
#pragma unroll
    for (int s = 0; s < 3; ++s)
#pragma unroll
      for (int n = 0; n < 2; ++n) acc[n][s] = mfma16(f.w[n], f.a[s], acc[n][s]);
    SCHED_FENCE();
  };
  GAS unsigned* fp = (GAS unsigned*)(p.ws + FLAG_OFF + ((((l & 8) ? min(rt * 96 + 95, S - 1) : rt * 96) >> 9) * 8 + (l & 7)) * 256);
  unsigned fv = 1u;
  BARRIER();
  Frag2 f0, f1;
  int so = 0;
  load(f0, lds, 0);
  SCHED_FENCE();
  for (int j = 0; j < NST2; ++j) {
    load(f1, lds + so, 1); mma(f0);
    so = so == (K2_NS - 1) * K2_SLOT ? 0 : so + K2_SLOT;
    if (w == 0) {
      if (j == NST2_H - K2_NS - 3) fv = __hip_atomic_load(fp, __ATOMIC_RELAXED, __HIP_MEMORY_SCOPE_AGENT);
      if (j == NST2_H - K2_NS) {
        for (int spin = 0; !__all(fv == 1u) && spin < (1 << 20); ++spin) {
          __builtin_amdgcn_s_sleep(1);
          fv = __hip_atomic_load(fp, __ATOMIC_RELAXED, __HIP_MEMORY_SCOPE_AGENT);
        }
      }
    }
    WAIT_LGKM0();
    BARRIER();
    SCHED_FENCE();
    load(f0, lds + so, 0); mma(f1);
    if (j == NST2_H - 1) {
#pragma unroll
      for (int n = 0; n < 2; ++n)
#pragma unroll
        for (int s = 0; s < 3; ++s) acc[n][s] *= gi[s];
    }
  }
  WAIT_LGKM0();
  STAMP(1);
  const __amdgpu_buffer_rsrc_t rout = mk_rsrc(p.out, S * D * 2);
  const unsigned vo = (unsigned)((rt * 96 + ws * 48 + (l & 15)) * 1280 + (ct * 64 + wn * 32 + 4 * kg) * 2);
#pragma unroll
  for (int s = 0; s < 3; ++s)
#pragma unroll
    for (int n = 0; n < 2; ++n) {
      const u32x2 v = {pk2(acc[n][s][0], acc[n][s][1]), pk2(acc[n][s][2], acc[n][s][3])};
      __builtin_amdgcn_raw_buffer_store_b64(v, rout, vo + s * 16 * 1280 + n * 32, 0, 0);
    }
  STAMP(2);
}

#define STAMP_PTR nullptr
extern "C" void kernel_launch(void* const* d_in, const int* in_sizes, int n_in, void* d_out, int out_size, void* d_ws, size_t ws_size,
                              hipStream_t stream) {
  Params p;
  p.x = (const u16*)d_in[0]; p.wqkv = (const u16*)d_in[1]; p.wo = (const u16*)d_in[2];
  p.wg = (const u16*)d_in[4]; p.wu = (const u16*)d_in[5]; p.wd = (const u16*)d_in[6]; p.gw = (const u16*)d_in[7];
  p.out = (u16*)d_out; p.ws = (char*)d_ws; p.stamps = STAMP_PTR;
  (void)hipFuncSetAttribute((const void*)k1, hipFuncAttributeMaxDynamicSharedMemorySize, K1_LDS);
  (void)hipFuncSetAttribute((const void*)k2, hipFuncAttributeMaxDynamicSharedMemorySize, K2_LDS);
  hipLaunchKernelGGL(k1, dim3(256), dim3(512), K1_LDS, stream, p);
  hipLaunchKernelGGL(k2, dim3(256), dim3(512), K2_LDS, stream, p);
}
```

```cpp
#include <hip/hip_runtime.h>
#include <hip/hip_bf16.h>

typedef unsigned short u16;
typedef unsigned long long u64;
typedef __bf16 bf16x8 __attribute__((ext_vector_type(8)));
typedef __bf16 bf16x4 __attribute__((ext_vector_type(4)));
typedef __bf16 bf16x2 __attribute__((ext_vector_type(2)));
typedef float f32x16 __attribute__((ext_vector_type(16)));
typedef float f32x4 __attribute__((ext_vector_type(4)));
typedef unsigned u32x4 __attribute__((ext_vector_type(4)));
typedef unsigned u32x2 __attribute__((ext_vector_type(2)));
#define LAS __attribute__((address_space(3)))
#define GAS __attribute__((address_space(1)))
typedef LAS char* ldsp;
typedef __amdgpu_buffer_rsrc_t rsrc_t;

constexpr int S = 2048, D = 640, F = 1536;
constexpr float LOG2E = 1.4426950408889634f;
constexpr int KB_ALL = 68;
constexpr unsigned A2_BYTES = 128u * KB_ALL * 1024;
constexpr unsigned KVP_OFF = A2_BYTES, KVP_BYTES = 8 * 8 * 6720;
constexpr unsigned G_OFF = KVP_OFF + KVP_BYTES, G_BYTES = S * 4;
constexpr unsigned FLAG_OFF = G_OFF + G_BYTES;
constexpr unsigned EPOCH_OFF = FLAG_OFF + 256 * 128;
constexpr unsigned WS_USED = EPOCH_OFF + 128;
constexpr int FL_H = 0, FL_KV = 156, FL_O = 220;
constexpr int SC1 = 16;
constexpr int SLOT = 53248, MISC = 3 * SLOT, LDS_BYTES = 163840;
constexpr int M_DUMMY = MISC, M_CNT = MISC + 256, M_Z = MISC + 512  , M_G = MISC + 2560;
constexpr int NST1 = 10;
constexpr int SPIN_MAX = 1 << 16;
constexpr unsigned CODE_BYTES = 40000;
#define ROT ((cg * 5 / 2 + rg * 5) % 10)
#define W2_ST 4

struct Params {
  const u16 *x, *wqkv, *wo, *wg, *wu, *wd, *gw;
  u16* out;
  char* ws;
  u64* stamps;
};

#define FST(i) do {} while (0)
#define FDUMP(n) do {} while (0)
#define STAMP(i) do {} while (0)

#define WAIT_VM(n) asm volatile("s_waitcnt vmcnt(" #n ")" ::: "memory")
#define WAIT_LGKM0() asm volatile("s_waitcnt lgkmcnt(0)" ::: "memory")
#define BARRIER() do { asm volatile("" ::: "memory"); __builtin_amdgcn_s_barrier(); asm volatile("" ::: "memory"); } while (0)
#define SCHED_FENCE() __builtin_amdgcn_sched_barrier(0)
#define SG_MFMA(n) __builtin_amdgcn_sched_group_barrier(0x8, n, 0)
#define SG_DS(n) __builtin_amdgcn_sched_group_barrier(0x100, n, 0)
#define SG_VM(n) __builtin_amdgcn_sched_group_barrier(0x20, n, 0)

__device__ __forceinline__ rsrc_t mk_rsrc(const void* p, unsigned bytes) { return __builtin_amdgcn_make_buffer_rsrc((void*)p, 0, bytes, 0x00020000); }
__device__ __forceinline__ void dma16(rsrc_t r, ldsp dst, unsigned voff, unsigned soff) { __builtin_amdgcn_raw_ptr_buffer_load_lds(r, dst, 16, voff, soff, 0, 0); }
__device__ __forceinline__ bf16x8 lds_b128(ldsp a) { return *(LAS bf16x8*)a; }
__device__ __forceinline__ bf16x8 lds_tr2(ldsp a0, ldsp a1) {
  bf16x4 lo = __builtin_amdgcn_ds_read_tr16_b64_v4bf16((LAS bf16x4*)a0);
  bf16x4 hi = __builtin_amdgcn_ds_read_tr16_b64_v4bf16((LAS bf16x4*)a1);
  return __builtin_shufflevector(lo, hi, 0, 1, 2, 3, 4, 5, 6, 7);
}
__device__ __forceinline__ f32x16 mfma32(bf16x8 a, bf16x8 b, f32x16 c) { return __builtin_amdgcn_mfma_f32_32x32x16_bf16(a, b, c, 0, 0, 0); }
__device__ __forceinline__ f32x4 mfma16(bf16x8 a, bf16x8 b, f32x4 c) { return __builtin_amdgcn_mfma_f32_16x16x32_bf16(a, b, c, 0, 0, 0); }
__device__ __forceinline__ unsigned pk2(float a, float b) {
  bf16x2 v = {(__bf16)a, (__bf16)b};
  return __builtin_bit_cast(unsigned, v);
}
__device__ __forceinline__ bf16x8 pack8(const f32x16& c, int o) {
  u32x4 r = {pk2(c[o], c[o + 1]), pk2(c[o + 2], c[o + 3]), pk2(c[o + 4], c[o + 5]), pk2(c[o + 6], c[o + 7])};
  return __builtin_bit_cast(bf16x8, r);
}
__device__ __forceinline__ bf16x8 pack44(const f32x4& a, const f32x4& b) {
  u32x4 r = {pk2(a[0], a[1]), pk2(a[2], a[3]), pk2(b[0], b[1]), pk2(b[2], b[3])};
  return __builtin_bit_cast(bf16x8, r);
}
__device__ __forceinline__ float phi(float t) {
  const float e = __builtin_amdgcn_exp2f(t * LOG2E);
  return __builtin_amdgcn_fmed3f(t, 1e-6f, __builtin_inff()) + __builtin_amdgcn_fmed3f(e, 0.f, 1.f);
}
__device__ __forceinline__ float sigmoidf(float t) { return __builtin_amdgcn_rcpf(1.f + __builtin_amdgcn_exp2f(-LOG2E * t)); }
__device__ __forceinline__ void st128_sc1(rsrc_t r, unsigned off, u32x4 v) { __builtin_amdgcn_raw_buffer_store_b128(v, r, off, 0, SC1); }

__device__ __forceinline__ u64 make_tag(u64 e) { return ((e ^ 0x9E3779B97F4A7C15ull) * 0xBF58476D1CE4E5B9ull) | 1ull; }
__device__ __forceinline__ GAS u64* flag_ptr(const Params& p, int f) { return (GAS u64*)(p.ws + FLAG_OFF + (size_t)f * 128); }
__device__ __forceinline__ u64 ld_flag(GAS u64* f) { return __hip_atomic_load(f, __ATOMIC_RELAXED, __HIP_MEMORY_SCOPE_AGENT); }
__device__ __forceinline__ void poll(GAS u64* f, u64 tag) {
  for (int spin = 0; spin < SPIN_MAX; ++spin) {
    if (__all(ld_flag(f) == tag)) break;
    __builtin_amdgcn_s_sleep(2);
  }
  __builtin_amdgcn_fence(__ATOMIC_ACQUIRE, "wavefront");
}
__device__ __forceinline__ void publish(const Params& p, ldsp lds, int f, u64 tag) {
  WAIT_VM(0);
  if ((threadIdx.x & 63) == 0) {
    const unsigned old = __hip_atomic_fetch_add((LAS unsigned*)(lds + M_CNT), 1u, __ATOMIC_RELAXED, __HIP_MEMORY_SCOPE_WORKGROUP);
    if (old == 3u) __hip_atomic_store(flag_ptr(p, f), tag, __ATOMIC_RELAXED, __HIP_MEMORY_SCOPE_AGENT);
  }
}

enum { T_Q = 0, T_KV = 1, T_GLU = 2 };

__device__ __forceinline__ float p1_loader(const Params& p, ldsp lds, int lw, int ty, int rt, int ct, int k0) {
  const bool tb = ty == T_GLU;
  const int l = threadIdx.x & 63;
  const rsrc_t rx = mk_rsrc(p.x, S * D * 2);
  const rsrc_t r0 = mk_rsrc(tb ? p.wg : p.wqkv, tb ? D * F * 2 : D * 3 * D * 2);
  const rsrc_t r1 = mk_rsrc(tb ? p.wu : p.wqkv, tb ? D * F * 2 : D * 3 * D * 2);
  const rsrc_t r2 = mk_rsrc(tb ? p.wu : p.x, tb ? D * F * 2 : S * D * 2);
  const int i8 = tb ? 5 * lw : 8 * lw + 3;
  const unsigned xe = (l >> 3) * 1280 + (((l & 7) ^ (l >> 4)) << 4);
  const unsigned xa = (xe ^ ((i8 & 1) << 6)) + (unsigned)((tb ? 160 : 256) * rt + 8 * i8) * 1280, xb = (xa ^ 64) + 8 * 1280;
  unsigned v[8];
  {
    const int r = l >> 4;
    const unsigned g0 = (16 * lw + r) * 3072 + ct * 256 + (((l & 15) ^ (r << 1)) << 4);
    const int colA = ty == T_Q ? 320 * ct : 1280 + 160 * ct, colB = ty == T_Q ? 320 * ct + 160 : 2560 + 160 * ct;
#pragma unroll
    for (int j = 0; j < 5; ++j) {
      const int c = 64 * j + l, rr = (c * 3277) >> 16;
      int cc = c - 20 * rr;
      if (ty == T_KV) cc ^= (rr >> 2) & 2;
      const unsigned a = (16 * lw + rr) * 3840 + (cc < 10 ? colA + cc * 16 : colB + (cc - 10) * 16);
      v[j] = tb ? (g0 ^ ((j & 2) << 6)) + (j & 3) * 4 * 3072 : a;
    }
    v[5] = tb ? v[1] : xb - 4 * 8 * 1280;
    v[6] = tb ? v[2] : xa - 2 * 8 * 1280;
    v[7] = tb ? v[3] : xb - 2 * 8 * 1280;
  }
  const int d0 = tb ? 20480 + 4096 * lw : 32768 + 5120 * lw, d4 = tb ? d0 + 16384 : d0 + 4096;
  const int d8 = i8 * 1024, d5 = tb ? d4 + 1024 : d8 - 3072;
  const unsigned wstep = tb ? 64 * 3072 : 64 * 3840, mstep = tb ? wstep : 128;
  auto issue = [&](int st, int so) {
    ldsp sb = lds + so;
#pragma unroll
    for (int j = 0; j < 4; ++j) dma16(r0, sb + d0 + j * 1024, v[j], st * wstep);
    dma16(r1, sb + d4, v[4], st * wstep);
#pragma unroll
    for (int j = 0; j < 3; ++j) dma16(r2, sb + d5 + j * 1024, v[5 + j], st * mstep);
#pragma unroll
    for (int j = 0; j < 5; ++j) dma16(rx, sb + d8 + j * 1024, (j & 1) ? xb : xa, st * 128 + (j >> 1) * 16 * 1280);
  };
  float gs = 0.f;
  auto gate = [&](int st, int so) {
    if (ty != T_Q) return;
    const unsigned a = (unsigned)(size_t)(lds + so) + (64 * lw + l) * 128 + (((l >> 1) & 7) << 4);
    const unsigned ga = (unsigned)(size_t)(lds + M_Z) + st * 128;
    u32x4 xq[8], gw[8];
    asm volatile(
        "ds_read_b128 %0, %16\n\tds_read_b128 %1, %17\n\tds_read_b128 %2, %18\n\tds_read_b128 %3, %19\n\t"
        "ds_read_b128 %4, %20\n\tds_read_b128 %5, %21\n\tds_read_b128 %6, %22\n\tds_read_b128 %7, %23\n\t"
        "ds_read_b128 %8, %24\n\tds_read_b128 %9, %24 offset:16\n\tds_read_b128 %10, %24 offset:32\n\tds_read_b128 %11, %24 offset:48\n\t"
        "ds_read_b128 %12, %24 offset:64\n\tds_read_b128 %13, %24 offset:80\n\tds_read_b128 %14, %24 offset:96\n\tds_read_b128 %15, %24 offset:112\n\t"
        "s_waitcnt lgkmcnt(0)"
        : "=&v"(xq[0]), "=&v"(xq[1]), "=&v"(xq[2]), "=&v"(xq[3]), "=&v"(xq[4]), "=&v"(xq[5]), "=&v"(xq[6]), "=&v"(xq[7]),
          "=&v"(gw[0]), "=&v"(gw[1]), "=&v"(gw[2]), "=&v"(gw[3]), "=&v"(gw[4]), "=&v"(gw[5]), "=&v"(gw[6]), "=&v"(gw[7])
        : "v"(a), "v"(a ^ 16), "v"(a ^ 32), "v"(a ^ 48), "v"(a ^ 64), "v"(a ^ 80), "v"(a ^ 96), "v"(a ^ 112), "v"(ga)
        : "memory");
#pragma unroll
    for (int c = 0; c < 8; ++c)
#pragma unroll
      for (int e = 0; e < 4; ++e) {
        const unsigned xw = xq[c][e], ge = gw[c][e];
        gs = __builtin_amdgcn_fdot2_f32_bf16(__builtin_bit_cast(bf16x2, xw), __builtin_bit_cast(bf16x2, ge), gs, false);
      }
  };
  FST(0);
  auto rot = [&](int j) { const int t = k0 + j; return t >= NST1 ? t - NST1 : t; };
  issue(k0, 0);
  FST(1);
  if (ty == T_Q && lw < 2) dma16(mk_rsrc(p.gw, D * 2), lds + M_Z + lw * 1024, l * 16, lw * 1024);
  issue(rot(1), SLOT);
  FST(2);
  WAIT_VM(13);
  FST(3);
  int so = 2 * SLOT, sc = 0;
#pragma unroll 1
  for (int j = -1; j < NST1 - 3; ++j) {
    BARRIER();
    issue(rot(j + 3), so);
    gate(rot(j + 1), sc);
    so = so == 2 * SLOT ? 0 : so + SLOT;
    sc = sc == 2 * SLOT ? 0 : sc + SLOT;
    FST(6 + 2 * j);
    WAIT_VM(13);
    FST(7 + 2 * j);
  }
  BARRIER();
  gate(rot(8), 2 * SLOT);
  WAIT_VM(0);
  BARRIER();
  gate(rot(9), 0);
  return gs;
}

struct Warm {
  const GAS char* a2;
  unsigned sink;
  __device__ __forceinline__ Warm(const Params& p, ldsp lds, int w, int xcd, int idx) {
    const int l = threadIdx.x & 63, blk = xcd * 32 + idx;
    sink = (unsigned)(size_t)(lds + M_DUMMY);
    a2 = (const GAS char*)p.gw;
    if (w == 0 && l < 60) a2 = (const GAS char*)p.wd + (blk * 60 + l) * 128;
    if (w == 1 && l < 25) a2 = (const GAS char*)p.wo + (blk * 25 + l) * 128;
  }
  __device__ __forceinline__ void touch(const GAS char* t) {
    unsigned keep;
    asm volatile("s_mov_b32 %0, m0\n\ts_mov_b32 m0, %2\n\ts_nop 0\n\tglobal_load_lds_dword %1, off\n\ts_mov_b32 m0, %0" : "=&s"(keep) : "v"(t), "s"(sink) : "memory");
  }
};

struct FragQ { bf16x8 w[5], x[2]; };
__device__ __forceinline__ void p1_main_q(ldsp lds, int w, Warm& wm, f32x16 (&acc)[5][2]) {
  const int l = threadIdx.x & 63, l31 = l & 31, h = l >> 5;
  const int xa = (64 * w + l31) * 128 + ((h ^ ((l31 >> 1) & 7)) << 4);
  const int wa = 32768 + (8 * h + ((l & 15) >> 2)) * 320 + ((l >> 4) & 1) * 32 + (l & 3) * 8;
  auto load = [&](FragQ& f, ldsp sb, int ks) {
    ldsp xp = sb + (xa ^ (ks << 5));
    ldsp wp = sb + wa + ks * 16 * 320;
    f.x[0] = lds_b128(xp);
    f.w[0] = lds_tr2(wp, wp + 1280);
    f.x[1] = lds_b128(xp + 4096);
#pragma unroll
    for (int n = 1; n < 5; ++n) f.w[n] = lds_tr2(wp + n * 64, wp + n * 64 + 1280);
  };
  auto mma = [&](const FragQ& f) {
#pragma unroll
    for (int n = 0; n < 5; ++n)
#pragma unroll
      for (int s = 0; s < 2; ++s) acc[n][s] = mfma32(f.w[n], f.x[s], acc[n][s]);
#pragma unroll
    for (int i = 0; i < 6; ++i) { SG_MFMA(1); SG_DS(2); }
    SG_MFMA(4);
    SCHED_FENCE();
  };
  FST(0);
  BARRIER();
  FST(1);
  FragQ f0, f1;
  int so = 0;
  load(f0, lds, 0);
  SCHED_FENCE();
#pragma unroll 1
  for (int j = 0; j < NST1; ++j) {
    ldsp sb = lds + so;
    load(f1, sb, 1); mma(f0);
    load(f0, sb, 2); mma(f1);
    load(f1, sb, 3); mma(f0);
    so = so == 2 * SLOT ? 0 : so + SLOT;
    WAIT_LGKM0();
    BARRIER();
    FST(2 + j);
    if (j == W2_ST) wm.touch(wm.a2);
    SCHED_FENCE();
    load(f0, lds + so, 0); mma(f1);
  }
  WAIT_LGKM0();
}

template <int TY>
__device__ __forceinline__ void p1_main16(ldsp lds, int w, Warm& wm, f32x4 (&acc)[4][10]) {
  constexpr bool TB = TY == T_GLU;
  const int l = threadIdx.x & 63, l15 = l & 15, g = l >> 4, q = l15 >> 2;
  const int xa = ((TB ? 0 : 64 * w) + l15) * 128 + ((g ^ ((l15 >> 1) & 7)) << 4);
  int wa0, wa1;
  if (TB) {
    wa0 = 20480 + (8 * g + q) * 256 + (((2 * w) ^ (q + 4 * (g & 1))) << 5) + (l & 3) * 8;
    wa1 = wa0 ^ 32;
  } else {
    wa0 = 32768 + (8 * g + q) * 320 + (l & 3) * 8 + 32 * (g & 1);
    wa1 = wa0 - 64 * (g & 1);
  }
  auto ldx = [&](ldsp sb, int u, int t) { return lds_b128(sb + (xa ^ (u << 6)) + t * 2048); };
  auto ldw = [&](ldsp sb, int u, int t) {
    ldsp a = sb + ((t & 1) ? wa1 : wa0) + (TB ? u * 8192 + (t >> 1) * 16384 : u * 10240 + t * 32);
    return lds_tr2(a, a + (TB ? 1024 : 1280));
  };
  auto lda = [&](ldsp sb, int u, int i) { return TB ? ldw(sb, u, i) : ldx(sb, u, i); };
  auto ldb = [&](ldsp sb, int u, int j) { return TB ? ldx(sb, u, j) : ldw(sb, u, j); };
  auto mma = [&](const bf16x8 (&a)[4], const bf16x8& b, int j) {
#pragma unroll
    for (int i = 0; i < 4; ++i) acc[i][j] = mfma16(a[i], b, acc[i][j]);
    SG_MFMA(1); SG_DS(2); SG_MFMA(1); SG_DS(2); SG_MFMA(1); SG_DS(2); SG_MFMA(1);
    SCHED_FENCE();
  };
  FST(0);
  BARRIER();
  FST(1);
  bf16x8 a0[4], a1[4], b0[10], b1[10];
  int so = 0;
#pragma unroll
  for (int i = 0; i < 4; ++i) a0[i] = lda(lds, 0, i);
#pragma unroll
  for (int j = 0; j < 5; ++j) b0[j] = ldb(lds, 0, j);
  SCHED_FENCE();
#pragma unroll 1
  for (int st = 0; st < NST1; ++st) {
    ldsp sb = lds + so;
#pragma unroll
    for (int j = 0; j < 10; ++j) {
      if (j < 5) b0[j + 5] = ldb(sb, 0, j + 5); else b1[j - 5] = ldb(sb, 1, j - 5);
      if (j >= 2 && j < 6) a1[j - 2] = lda(sb, 1, j - 2);
      mma(a0, b0[j], j);
    }
#pragma unroll
    for (int j = 0; j < 6; ++j) {
      if (j < 5) b1[j + 5] = ldb(sb, 1, j + 5);
      mma(a1, b1[j], j);
    }
    so = so == 2 * SLOT ? 0 : so + SLOT;
    sb = lds + so;
    WAIT_LGKM0();
    BARRIER();
    FST(2 + st);
    if (st == W2_ST) wm.touch(wm.a2);
    SCHED_FENCE();
    a0[0] = lda(sb, 0, 0); b0[0] = ldb(sb, 0, 0); a0[1] = lda(sb, 0, 1); mma(a1, b1[6], 6);
    a0[2] = lda(sb, 0, 2); a0[3] = lda(sb, 0, 3); b0[1] = ldb(sb, 0, 1); mma(a1, b1[7], 7);
    b0[2] = ldb(sb, 0, 2); b0[3] = ldb(sb, 0, 3); mma(a1, b1[8], 8);
    b0[4] = ldb(sb, 0, 4); mma(a1, b1[9], 9);
  }
  WAIT_LGKM0();
}

__device__ __forceinline__ constexpr int w2_off(int s) { return (SLOT + (s % 39) * 4096) % (3 * SLOT); }

struct W2Loader {
  rsrc_t rd, ro;
  unsigned wv, ws0;
  ldsp dst;
  __device__ __forceinline__ W2Loader(const Params& p, ldsp lds, int lw, int ct) {
    const int l = threadIdx.x & 63;
    rd = mk_rsrc(p.wd, F * D * 2);
    ro = mk_rsrc(p.wo, D * D * 2);
    wv = (l >> 3) * 1280 + (((l & 7) ^ ((l >> 4) << 1)) << 4);
    ws0 = (unsigned)(8 * lw) * 1280 + ct * 128;
    dst = lds + lw * 1024;
  }
  template <int S0, int S1> __device__ __forceinline__ void fill() {
#pragma unroll
    for (int s = S0; s < S1; ++s) dma16(s < 48 ? rd : ro, dst + w2_off(s), wv, ws0 + (unsigned)(s < 48 ? s : s - 48) * 32 * 1280);
  }
};

__device__ __forceinline__ void p2_loader(const Params& p, W2Loader& L, int lw, int rt, u64 tag) {
  const int l = threadIdx.x & 63;
  const int rlo = rt * 96, rhi = min(rlo + 95, S - 1);
  if (lw == 0) poll(flag_ptr(p, FL_H + ((l & 16) ? rhi : rlo) / 160 * 12 + min(l & 15, 11)), tag);
  WAIT_VM(0);
  BARRIER();
  BARRIER();
  L.fill<39, 55>();
  BARRIER();
  GAS u64* fo = flag_ptr(p, FL_O + (((l & 4) ? rhi : rlo) >> 8) * 4 + (l & 3));
  u64 fv;
  asm volatile("global_load_dwordx2 %0, %1, off sc1" : "=v"(fv) : "v"(fo) : "memory");
  L.fill<55, 68>();
  asm volatile("s_waitcnt vmcnt(13)" : "+v"(fv)::"memory");
  if (lw == 0 && !__all(fv == tag)) poll(fo, tag);
  BARRIER();
  WAIT_VM(0);
  BARRIER();
}

__device__ __forceinline__ void p2_main(const Params& p, ldsp lds, int w, int rt, int ct) {
  constexpr int AW = 8;
  const int l = threadIdx.x & 63, l15 = l & 15, kg = l >> 4, q = l15 >> 2;
  const int sg = w & 1, kh = w >> 1;
  const rsrc_t ra = mk_rsrc(p.ws, A2_BYTES);
  const unsigned av = (unsigned)(((rt * 6 + sg * 3) * KB_ALL + kh) * 1024 + l * 16);
  const int rh = 4 * kg + q, ro = 16 * (kg >> 1) + 4 * (kg & 1) + q;
  const int wh = rh * 128 + (((rh >> 1) & 3) << 5) + (l & 3) * 8 + kh * 4096;
  const int wo = ro * 128 + (((ro >> 1) & 3) << 5) + (l & 3) * 8 + kh * 4096;
  f32x4 acc[4][3];
#pragma unroll
  for (int n = 0; n < 4; ++n)
#pragma unroll
    for (int s = 0; s < 3; ++s) acc[n][s] = f32x4{};
  u32x4 a[34][3];
  bf16x8 wf[2][4];
  float gi[3];
  auto lda = [&](int t) {
#pragma unroll
    for (int s = 0; s < 3; ++s) a[t][s] = __builtin_amdgcn_raw_buffer_load_b128(ra, av + s * KB_ALL * 1024, t * 2048, SC1);
  };
  ldsp wb[4];
  auto ldw = [&](int t) {
    const int o0 = w2_off(2 * t), o1 = w2_off(2 * t + 1) - 4096;
    const int r0 = o0 & ~0x7FFF, rp = t ? w2_off(2 * t - 2) & ~0x7FFF : -1;
    const bool split = o0 != o1, was = t && w2_off(2 * t - 2) != w2_off(2 * t - 1) - 4096;
    if (r0 != rp || t == 24 || split || was) {
#pragma unroll
      for (int n = 0; n < 4; ++n) {
        wb[n] = lds + (((t < 24 ? wh : wo) ^ (n << 5)) + r0 + (split && kh ? o1 - o0 : 0));
        asm volatile("" : "+v"(wb[n]));
      }
    }
#pragma unroll
    for (int n = 0; n < 4; ++n) wf[t & 1][n] = lds_tr2(wb[n] + (o0 - r0), wb[n] + (o0 - r0) + (t < 24 ? 2048 : 1024));
  };
  BARRIER();
  STAMP(4);
#pragma unroll
  for (int t = 0; t < AW; ++t) lda(t);
  ldw(0);
  SCHED_FENCE();
#pragma unroll
  for (int t = 0; t < 34; ++t) {
    if (t == 8 || t == 15 || t == 19 || t == 27) {
      WAIT_LGKM0();
      FST(t == 8 ? 12 : t == 15 ? 14 : t == 19 ? 16 : 18);
      BARRIER();
      FST(t == 8 ? 13 : t == 15 ? 15 : t == 19 ? 17 : 19);
      SCHED_FENCE();
      if (t == 19) {
        const rsrc_t rg = mk_rsrc(p.ws + G_OFF, G_BYTES);
#pragma unroll
        for (int s = 0; s < 3; ++s) gi[s] = __builtin_bit_cast(float, __builtin_amdgcn_raw_buffer_load_b32(rg, (rt * 96 + sg * 48 + s * 16 + l15) * 4, 0, SC1));
#pragma unroll
        for (int u = 24; u < 19 + AW; ++u) lda(u);
      }
      if (t == 19 || t == 27) ldw(t);
    }
    if (t + AW < 24 || (t >= 19 && t + AW < 34)) lda(t + AW);
    if (t + 1 < 34 && t + 1 != 19 && t + 1 != 27) ldw(t + 1);
    if (t == 24) {
#pragma unroll
      for (int n = 0; n < 4; ++n)
#pragma unroll
        for (int s = 0; s < 3; ++s) acc[n][s] *= 1.f - gi[s];
    }
#pragma unroll
    for (int s = 0; s < 3; ++s)
#pragma unroll
      for (int n = 0; n < 4; ++n) acc[n][s] = mfma16(wf[t & 1][n], __builtin_bit_cast(bf16x8, a[t][s]), acc[n][s]);
    SG_MFMA(1); SG_DS(2); SG_MFMA(1); SG_DS(2); SG_MFMA(1); SG_DS(2); SG_MFMA(1); SG_DS(2);
    SG_MFMA(1); SG_VM(1); SG_MFMA(1); SG_VM(1); SG_MFMA(1); SG_VM(1); SG_MFMA(5);
    SCHED_FENCE();
  }
  STAMP(5);
  ldsp xb = lds + w2_off(30);
  {
    ldsp wp = xb + w * 6144 + l * 16;
#pragma unroll
    for (int n = 0; n < 2; ++n)
#pragma unroll
      for (int s = 0; s < 3; ++s) *(LAS f32x4*)(wp + (n * 3 + s) * 1024) = kh ? acc[n][s] : acc[n + 2][s];
  }
  WAIT_LGKM0();
  BARRIER();
  const rsrc_t rout = mk_rsrc(p.out, S * D * 2);
  const unsigned vo = (unsigned)((rt * 96 + sg * 48 + l15) * 1280 + (ct * 64 + kh * 32 + 4 * kg) * 2);
  ldsp rp = xb + (w ^ 2) * 6144 + l * 16;
#pragma unroll
  for (int s = 0; s < 3; ++s)
#pragma unroll
    for (int n = 0; n < 2; ++n) {
      const f32x4 v = (kh ? acc[n + 2][s] : acc[n][s]) + *(LAS f32x4*)(rp + (n * 3 + s) * 1024);
      __builtin_amdgcn_raw_buffer_store_b64(u32x2{pk2(v[0], v[1]), pk2(v[2], v[3])}, rout, vo + s * 16 * 1280 + n * 32, 0, 0);
    }
}

constexpr int KS = 168, KVT = SLOT, KVT_SZ = 96 * KS;

__global__ __launch_bounds__(512, 2) void fused(Params p) {
  extern __shared__ __attribute__((aligned(16))) char smem[];
  ldsp lds = (ldsp)smem;
  asm volatile("" ::"s"(p.x), "s"(p.wqkv), "s"(p.wg), "s"(p.wu));
  u64 pc;
  asm volatile("s_getpc_b64 %0" : "=s"(pc));
  constexpr unsigned rest = CODE_BYTES;
  const unsigned b = blockIdx.x, xcd = b & 7, idx = b >> 3, cg = xcd & 3, rg = xcd >> 2;
  const int w = __builtin_amdgcn_readfirstlane(threadIdx.x >> 6), lw = w - 4;
  const int l = threadIdx.x & 63;
  STAMP(0);
  const bool ta = idx < 12;
  const unsigned jj = ta ? idx : idx - 12 + 20 * rg, j3 = (jj * 43) >> 7, jr = jj - 3 * j3;
  const bool spare = jj == 39;
  const int ty = ta ? (jr == 0 ? T_Q : T_KV) : T_GLU;
  const int rt = ta ? 4 * rg + j3 : j3;
  const int ct = ta ? (jr == 0 ? cg : 2 * cg + jr - 1) : 3 * cg + jr;
  const unsigned t2 = (xcd < 4 ? 28 * xcd : 4 + 27 * xcd) + (ta ? 2 * j3 + jr - 1 : idx - 4);
  const int rt2 = (t2 * 205) >> 11, ct2 = t2 - 10 * rt2;
  auto get_tag = [&]() {
    u64 e;
    asm volatile("s_load_dwordx2 %0, %1, 0x0\n\ts_waitcnt lgkmcnt(0)" : "=s"(e) : "s"(p.ws + EPOCH_OFF) : "memory");
    return make_tag(e);
  };

  if (__builtin_expect(w >= 4, 1)) {
    if (__builtin_expect(spare, 0)) {
      if (cg == 0 && lw == 0) {
        const u64 tag = get_tag();
#pragma unroll 1
        for (int i = 0; i < 4; ++i) poll(flag_ptr(p, min(i * 64 + l, 251)), tag);
        if (l == 0) __hip_atomic_store((GAS u64*)(p.ws + EPOCH_OFF), tag, __ATOMIC_RELAXED, __HIP_MEMORY_SCOPE_AGENT);
      }
      return;
    }
    const float gs = p1_loader(p, lds, lw, ty, rt, ct, ROT);
    const u64 tag = get_tag();
    if (ty == T_Q) {
      const int tl = lw * 64 + l;
      for (int i = tl; i < 2 * KVT_SZ / 16; i += 256) *(LAS u32x4*)(lds + KVT + i * 16) = u32x4{0u, 0u, 0u, 0u};
      const float gv = sigmoidf(gs);
      *(LAS float*)(lds + M_G + tl * 4) = gv;
      if (lw == ct) __builtin_amdgcn_raw_buffer_store_b32(__builtin_bit_cast(unsigned, gv), mk_rsrc(p.ws + G_OFF, G_BYTES), (rt * 256 + tl) * 4, 0, SC1);
      WAIT_LGKM0();
      BARRIER();
      poll(flag_ptr(p, FL_KV + (l & 7) * 8 + 2 * ct + ((l >> 3) & 1)), tag);
      STAMP(3);
      const rsrc_t rk = mk_rsrc(p.ws + KVP_OFF, KVP_BYTES);
      u32x4 pv[4][8];
#pragma unroll
      for (int i = 0; i < 4; ++i) {
        const int en = min(tl + 256 * i, 839);
#pragma unroll
        for (int r = 0; r < 8; ++r) pv[i][r] = __builtin_amdgcn_raw_buffer_load_b128(rk, 2 * ct * 6720 + en * 16, r * 8 * 6720, SC1);
      }
#pragma unroll
      for (int i = 0; i < 4; ++i) {
        const int en = tl + 256 * i;
        f32x4 s = __builtin_bit_cast(f32x4, pv[i][0]);
#pragma unroll
        for (int r = 1; r < 8; ++r) s += __builtin_bit_cast(f32x4, pv[i][r]);
        if (en < 840) {
          const int hg = en >= 420, e2 = en - 420 * hg, row = e2 / 5, dq = e2 % 5, hd = row < 80 ? row / 20 : row - 80;
          ldsp dp = lds + KVT + hg * KVT_SZ + row * KS + (20 * hd + 4 * dq) * 2;
          const u32x2 v = {pk2(s[0], s[1]), pk2(s[2], s[3])};
          *(LAS u32x2*)dp = v;
          if (row >= 80) *(LAS u32x2*)(dp + 4 * KS) = v;
        }
      }
      WAIT_VM(0);
      WAIT_LGKM0();
      BARRIER();
      STAMP(4);
      FDUMP(20);
      return;
    }
    W2Loader L(p, lds, lw, ct2);
    L.fill<0, 26>();
    BARRIER();
    if (ty == T_KV) { BARRIER(); BARRIER(); }
    L.fill<26, 39>();
    p2_loader(p, L, lw, rt2, tag);
    FDUMP(20);
    return;
  }

  Warm wm(p, lds, w, xcd, idx);
  if (idx < 2) {
    const unsigned o = (idx * 256 + w * 64 + l) * 128;
    wm.touch((const GAS char*)pc + (o < rest ? o : 0u));
  }
  if (spare) { wm.touch(wm.a2); return; }
  if (threadIdx.x == 0) *(LAS unsigned*)(lds + M_CNT) = 0u;
  const u64 tag = get_tag();
  const rsrc_t ra = mk_rsrc(p.ws, A2_BYTES);
  WAIT_LGKM0();
  if (ty == T_Q) {
    const int l31 = l & 31, h = l >> 5;
    bf16x8 pq[2][10];
    {
      f32x16 acc[5][2];
#pragma unroll
      for (int n = 0; n < 5; ++n) { acc[n][0] = f32x16{}; acc[n][1] = f32x16{}; }
      p1_main_q(lds, w, wm, acc);
      STAMP(1);
#pragma unroll
      for (int s = 0; s < 2; ++s)
#pragma unroll
        for (int n = 0; n < 5; ++n) {
          f32x16 t;
#pragma unroll
          for (int r = 0; r < 16; ++r) t[r] = phi(acc[n][s][r]);
          pq[s][2 * n] = pack8(t, 0); pq[s][2 * n + 1] = pack8(t, 8);
          asm volatile("" : "+v"(pq[s][2 * n]), "+v"(pq[s][2 * n + 1]));
        }
    }
    STAMP(2);
    BARRIER();
    STAMP(4);
    const unsigned vo = (unsigned)((((rt * 16 + w * 4) + ((l >> 4) & 1)) * KB_ALL + 48 + 5 * ct) * 1024 + h * 256 + (l & 15) * 16);
#pragma unroll
    for (int hg = 0; hg < 2; ++hg) {
      f32x16 acc[2][3];
#pragma unroll
      for (int s = 0; s < 2; ++s)
#pragma unroll
        for (int et = 0; et < 3; ++et) acc[s][et] = f32x16{};
#pragma unroll
      for (int et = 2; et >= 0; --et)
#pragma unroll
        for (int t = 0; t < 5; ++t) {
          if ((et == 0 && t > 2) || (et == 1 && t == 0)) continue;
          ldsp ap = lds + KVT + hg * KVT_SZ + (32 * et + l31) * KS + (16 * t + 4 * h) * 2;
          const bf16x4 lo = *(LAS bf16x4*)ap, hi = *(LAS bf16x4*)(ap + 16);
          const bf16x8 a = __builtin_shufflevector(lo, hi, 0, 1, 2, 3, 4, 5, 6, 7);
          acc[0][et] = mfma32(a, pq[0][5 * hg + t], acc[0][et]);
          acc[1][et] = mfma32(a, pq[1][5 * hg + t], acc[1][et]);
        }
#pragma unroll
      for (int s = 0; s < 2; ++s) {
        const float gv = *(LAS float*)(lds + M_G + (64 * w + 32 * s + l31) * 4);
        f32x4 sc;
#pragma unroll
        for (int hd = 0; hd < 4; ++hd) sc[hd] = gv * __builtin_amdgcn_rcpf(acc[s][2][8 + hd] + 1e-6f);
#pragma unroll
        for (int pc = 0; pc < 5; ++pc) {
          f32x16 t;
#pragma unroll
          for (int gq = 0; gq < 2; ++gq) {
            const int e0 = 16 * pc + 8 * gq;
            float fa = sc[e0 / 20], fb = sc[(e0 + 4) / 20];
            if (e0 / 20 != (e0 + 4) / 20) { asm volatile("" : "+v"(fa), "+v"(fb)); fa = h ? fb : fa; }
#pragma unroll
            for (int r = 0; r < 4; ++r) t[4 * gq + r] = acc[s][pc >> 1][8 * (pc & 1) + 4 * gq + r] * fa;
          }
          const int k16 = 5 * hg + pc;
          st128_sc1(ra, vo + (s * 2 * KB_ALL + (k16 >> 1)) * 1024 + (k16 & 1) * 512, __builtin_bit_cast(u32x4, pack8(t, 0)));
        }
      }
    }
    publish(p, lds, FL_O + rt * 4 + ct, tag);
    STAMP(6);
    FDUMP(12);
    return;
  }
  {
    f32x4 acc[4][10];
#pragma unroll
    for (int i = 0; i < 4; ++i)
#pragma unroll
      for (int j = 0; j < 10; ++j) acc[i][j] = f32x4{};
    if (ty == T_GLU) {
      p1_main16<T_GLU>(lds, w, wm, acc);
      STAMP(1);
      const unsigned vo = (unsigned)((rt * 10 * KB_ALL + ct * 4 + w) * 1024 + l * 16);
#pragma unroll
      for (int j = 0; j < 10; ++j) {
        f32x4 t0, t1;
#pragma unroll
        for (int r = 0; r < 4; ++r) { t0[r] = acc[2][j][r] * sigmoidf(acc[0][j][r]); t1[r] = acc[3][j][r] * sigmoidf(acc[1][j][r]); }
        st128_sc1(ra, vo + j * KB_ALL * 1024, __builtin_bit_cast(u32x4, pack44(t0, t1)));
      }
      STAMP(2);
      publish(p, lds, FL_H + rt * 12 + ct, tag);
    } else {
      p1_main16<T_KV>(lds, w, wm, acc);
      STAMP(1);
      f32x4 kv[13], z[5];
#pragma unroll
      for (int i = 0; i < 13; ++i) kv[i] = f32x4{};
#pragma unroll
      for (int i = 0; i < 5; ++i) z[i] = f32x4{};
      const bf16x8 ones = __builtin_bit_cast(bf16x8, u32x4{0x3F803F80u, 0x3F803F80u, 0x3F803F80u, 0x3F803F80u});
#pragma unroll
      for (int u = 0; u < 2; ++u) {
        bf16x8 vf[5];
#pragma unroll
        for (int j = 0; j < 5; ++j) vf[j] = pack44(acc[2 * u][5 + j], acc[2 * u + 1][5 + j]);
#pragma unroll
        for (int i = 0; i < 5; ++i) {
          f32x4 t0, t1;
#pragma unroll
          for (int r = 0; r < 4; ++r) { t0[r] = phi(acc[2 * u][i][r]); t1[r] = phi(acc[2 * u + 1][i][r]); }
          const bf16x8 kf = pack44(t0, t1);
          if (i > 0) kv[3 * i - 1] = mfma16(kf, vf[i - 1], kv[3 * i - 1]);
          kv[3 * i] = mfma16(kf, vf[i], kv[3 * i]);
          if (i < 4) kv[3 * i + 1] = mfma16(kf, vf[i + 1], kv[3 * i + 1]);
          z[i] = mfma16(kf, ones, z[i]);
        }
      }
#pragma unroll
      for (int i = 0; i < 13; ++i) *(LAS f32x4*)(lds + w * 13312 + i * 1024 + l * 16) = kv[i];
      if ((l & 15) == 0) {
#pragma unroll
        for (int i = 0; i < 5; ++i) *(LAS f32x4*)(lds + M_Z + w * 320 + i * 64 + (l >> 4) * 16) = z[i];
      }
      WAIT_LGKM0();
      BARRIER();
      u32x4 res[2];
#pragma unroll
      for (int i = 0; i < 2; ++i) {
        const int en = min((int)threadIdx.x + 256 * i, 419), row = en / 5, dq = en % 5;
        const int hd = row < 80 ? row / 20 : row - 80, d0 = 20 * hd + 4 * dq, ti = d0 >> 4;
        const int off = row < 80 ? (2 * ti + (row >> 4)) * 1024 + (((d0 & 15) >> 2) * 16 + (row & 15)) * 16 : M_Z + d0 * 4;
        const int str = row < 80 ? 13312 : 320;
        f32x4 s = *(LAS f32x4*)(lds + off);
#pragma unroll
        for (int ww = 1; ww < 4; ++ww) s += *(LAS f32x4*)(lds + off + ww * str);
        res[i] = __builtin_bit_cast(u32x4, s);
      }
      const rsrc_t rk = mk_rsrc(p.ws + KVP_OFF + (rt * 8 + ct) * 6720, 6720);
      st128_sc1(rk, threadIdx.x * 16, res[0]);
      st128_sc1(rk, threadIdx.x * 16 + 4096, res[1]);
      WAIT_LGKM0();
      BARRIER();
      STAMP(2);
      publish(p, lds, FL_KV + rt * 8 + ct, tag);
    }
  }
  STAMP(3);
  p2_main(p, lds, w, rt2, ct2);
  STAMP(6);
  FDUMP(20);
}

#define STAMP_PTR nullptr
extern "C" void kernel_launch(void* const* d_in, const int* in_sizes, int n_in, void* d_out, int out_size, void* d_ws, size_t ws_size,
                              hipStream_t stream) {
  Params p;
  p.x = (const u16*)d_in[0]; p.wqkv = (const u16*)d_in[1]; p.wo = (const u16*)d_in[2];
  p.wg = (const u16*)d_in[4]; p.wu = (const u16*)d_in[5]; p.wd = (const u16*)d_in[6]; p.gw = (const u16*)d_in[7];
  p.out = (u16*)d_out; p.ws = (char*)d_ws; p.stamps = STAMP_PTR;
  (void)hipFuncSetAttribute((const void*)fused, hipFuncAttributeMaxDynamicSharedMemorySize, LDS_BYTES);
  hipLaunchKernelGGL(fused, dim3(256), dim3(512), LDS_BYTES, stream, p);
}
```

```cpp
#define DIAG 3
#define DIAG_THR 2900
#include <hip/hip_runtime.h>
#include <hip/hip_bf16.h>

typedef unsigned short u16;
typedef unsigned long long u64;
typedef __bf16 bf16x8 __attribute__((ext_vector_type(8)));
typedef __bf16 bf16x4 __attribute__((ext_vector_type(4)));
typedef __bf16 bf16x2 __attribute__((ext_vector_type(2)));
typedef float f32x16 __attribute__((ext_vector_type(16)));
typedef float f32x4 __attribute__((ext_vector_type(4)));
typedef unsigned u32x4 __attribute__((ext_vector_type(4)));
typedef unsigned u32x2 __attribute__((ext_vector_type(2)));
#define LAS __attribute__((address_space(3)))
#define GAS __attribute__((address_space(1)))
typedef LAS char* ldsp;
typedef __amdgpu_buffer_rsrc_t rsrc_t;

constexpr int S = 2048, D = 640, F = 1536;
constexpr float LOG2E = 1.4426950408889634f;
constexpr int KB_ALL = 68;
constexpr unsigned A2_BYTES = 128u * KB_ALL * 1024;
constexpr unsigned KVP_OFF = A2_BYTES, KVP_BYTES = 8 * 8 * 6720;
constexpr unsigned G_OFF = KVP_OFF + KVP_BYTES, G_BYTES = S * 4;
constexpr unsigned FLAG_OFF = G_OFF + G_BYTES;
constexpr unsigned EPOCH_OFF = FLAG_OFF + 256 * 128;
constexpr unsigned WS_USED = EPOCH_OFF + 128;
constexpr int FL_H = 0, FL_KV = 156, FL_O = 220;
constexpr int SC1 = 16;
constexpr int SLOT = 53248, MISC = 3 * SLOT, LDS_BYTES = 163840;
constexpr int M_DUMMY = MISC, M_CNT = MISC + 256, M_Z = MISC + 512  , M_G = MISC + 2560;
constexpr int NST1 = 10;
constexpr int SPIN_MAX = 1 << 16;
constexpr unsigned CODE_BYTES = 40000;
#define A2AUX SC1
#define ROT ((cg * 5 / 2 + rg * 5) % 10)
#define W2_ST 4

struct Params {
  const u16 *x, *wqkv, *wo, *wg, *wu, *wd, *gw;
  u16* out;
  char* ws;
  u64* stamps;
};

#define FST(i) do {} while (0)
#define FDUMP(n) do {} while (0)
#define STAMP(i) do {} while (0)

#define DT(v) asm volatile("s_memrealtime %0\n\ts_waitcnt lgkmcnt(0)" : "=s"(v)::"memory")
struct Diag { u64 t0, t1, t2, t3, t4; };
#define WAIT_VM(n) asm volatile("s_waitcnt vmcnt(" #n ")" ::: "memory")
#define WAIT_LGKM0() asm volatile("s_waitcnt lgkmcnt(0)" ::: "memory")
#define BARRIER() do { asm volatile("" ::: "memory"); __builtin_amdgcn_s_barrier(); asm volatile("" ::: "memory"); } while (0)
#define SCHED_FENCE() __builtin_amdgcn_sched_barrier(0)
#define SG_MFMA(n) __builtin_amdgcn_sched_group_barrier(0x8, n, 0)
#define SG_DS(n) __builtin_amdgcn_sched_group_barrier(0x100, n, 0)
#define SG_VM(n) __builtin_amdgcn_sched_group_barrier(0x20, n, 0)

__device__ __forceinline__ rsrc_t mk_rsrc(const void* p, unsigned bytes) { return __builtin_amdgcn_make_buffer_rsrc((void*)p, 0, bytes, 0x00020000); }
__device__ __forceinline__ void dma16(rsrc_t r, ldsp dst, unsigned voff, unsigned soff) { __builtin_amdgcn_raw_ptr_buffer_load_lds(r, dst, 16, voff, soff, 0, 0); }
__device__ __forceinline__ bf16x8 lds_b128(ldsp a) { return *(LAS bf16x8*)a; }
__device__ __forceinline__ bf16x8 lds_tr2(ldsp a0, ldsp a1) {
  bf16x4 lo = __builtin_amdgcn_ds_read_tr16_b64_v4bf16((LAS bf16x4*)a0);
  bf16x4 hi = __builtin_amdgcn_ds_read_tr16_b64_v4bf16((LAS bf16x4*)a1);
  return __builtin_shufflevector(lo, hi, 0, 1, 2, 3, 4, 5, 6, 7);
}
__device__ __forceinline__ f32x16 mfma32(bf16x8 a, bf16x8 b, f32x16 c) { return __builtin_amdgcn_mfma_f32_32x32x16_bf16(a, b, c, 0, 0, 0); }
__device__ __forceinline__ f32x4 mfma16(bf16x8 a, bf16x8 b, f32x4 c) { return __builtin_amdgcn_mfma_f32_16x16x32_bf16(a, b, c, 0, 0, 0); }
__device__ __forceinline__ unsigned pk2(float a, float b) {
  bf16x2 v = {(__bf16)a, (__bf16)b};
  return __builtin_bit_cast(unsigned, v);
}
__device__ __forceinline__ bf16x8 pack8(const f32x16& c, int o) {
  u32x4 r = {pk2(c[o], c[o + 1]), pk2(c[o + 2], c[o + 3]), pk2(c[o + 4], c[o + 5]), pk2(c[o + 6], c[o + 7])};
  return __builtin_bit_cast(bf16x8, r);
}
__device__ __forceinline__ bf16x8 pack44(const f32x4& a, const f32x4& b) {
  u32x4 r = {pk2(a[0], a[1]), pk2(a[2], a[3]), pk2(b[0], b[1]), pk2(b[2], b[3])};
  return __builtin_bit_cast(bf16x8, r);
}
__device__ __forceinline__ float phi(float t) {
  const float e = __builtin_amdgcn_exp2f(t * LOG2E);
  return __builtin_amdgcn_fmed3f(t, 1e-6f, __builtin_inff()) + __builtin_amdgcn_fmed3f(e, 0.f, 1.f);
}
__device__ __forceinline__ float sigmoidf(float t) { return __builtin_amdgcn_rcpf(1.f + __builtin_amdgcn_exp2f(-LOG2E * t)); }
__device__ __forceinline__ void st128_sc1(rsrc_t r, unsigned off, u32x4 v) { __builtin_amdgcn_raw_buffer_store_b128(v, r, off, 0, SC1); }

__device__ __forceinline__ u64 make_tag(u64 e) { return ((e ^ 0x9E3779B97F4A7C15ull) * 0xBF58476D1CE4E5B9ull) | 1ull; }
__device__ __forceinline__ GAS u64* flag_ptr(const Params& p, int f) { return (GAS u64*)(p.ws + FLAG_OFF + (size_t)f * 128); }
__device__ __forceinline__ u64 ld_flag(GAS u64* f) { return __hip_atomic_load(f, __ATOMIC_RELAXED, __HIP_MEMORY_SCOPE_AGENT); }
__device__ __forceinline__ void poll(GAS u64* f, u64 tag) {
  for (int spin = 0; spin < SPIN_MAX; ++spin) {
    if (__all(ld_flag(f) == tag)) break;
    __builtin_amdgcn_s_sleep(2);
  }
  __builtin_amdgcn_fence(__ATOMIC_ACQUIRE, "wavefront");
}
__device__ __forceinline__ void publish(const Params& p, ldsp lds, int f, u64 tag) {
  WAIT_VM(0);
  if ((threadIdx.x & 63) == 0) {
    const unsigned old = __hip_atomic_fetch_add((LAS unsigned*)(lds + M_CNT), 1u, __ATOMIC_RELAXED, __HIP_MEMORY_SCOPE_WORKGROUP);
    if (old == 3u) __hip_atomic_store(flag_ptr(p, f), tag, __ATOMIC_RELAXED, __HIP_MEMORY_SCOPE_AGENT);
  }
}

enum { T_Q = 0, T_KV = 1, T_GLU = 2 };

__device__ __forceinline__ float p1_loader(const Params& p, ldsp lds, int lw, int ty, int rt, int ct, int k0) {
  const bool tb = ty == T_GLU;
  const int l = threadIdx.x & 63;
  const rsrc_t rx = mk_rsrc(p.x, S * D * 2);
  const rsrc_t r0 = mk_rsrc(tb ? p.wg : p.wqkv, tb ? D * F * 2 : D * 3 * D * 2);
  const rsrc_t r1 = mk_rsrc(tb ? p.wu : p.wqkv, tb ? D * F * 2 : D * 3 * D * 2);
  const rsrc_t r2 = mk_rsrc(tb ? p.wu : p.x, tb ? D * F * 2 : S * D * 2);
  const int i8 = tb ? 5 * lw : 8 * lw + 3;
  const unsigned xe = (l >> 3) * 1280 + (((l & 7) ^ (l >> 4)) << 4);
  const unsigned xa = (xe ^ ((i8 & 1) << 6)) + (unsigned)((tb ? 160 : 256) * rt + 8 * i8) * 1280, xb = (xa ^ 64) + 8 * 1280;
  unsigned v[8];
  {
    const int r = l >> 4;
    const unsigned g0 = (16 * lw + r) * 3072 + ct * 256 + (((l & 15) ^ (r << 1)) << 4);
    const int colA = ty == T_Q ? 320 * ct : 1280 + 160 * ct, colB = ty == T_Q ? 320 * ct + 160 : 2560 + 160 * ct;
#pragma unroll
    for (int j = 0; j < 5; ++j) {
      const int c = 64 * j + l, rr = (c * 3277) >> 16;
      int cc = c - 20 * rr;
      if (ty == T_KV) cc ^= (rr >> 2) & 2;
      const unsigned a = (16 * lw + rr) * 3840 + (cc < 10 ? colA + cc * 16 : colB + (cc - 10) * 16);
      v[j] = tb ? (g0 ^ ((j & 2) << 6)) + (j & 3) * 4 * 3072 : a;
    }
    v[5] = tb ? v[1] : xb - 4 * 8 * 1280;
    v[6] = tb ? v[2] : xa - 2 * 8 * 1280;
    v[7] = tb ? v[3] : xb - 2 * 8 * 1280;
  }
  const int d0 = tb ? 20480 + 4096 * lw : 32768 + 5120 * lw, d4 = tb ? d0 + 16384 : d0 + 4096;
  const int d8 = i8 * 1024, d5 = tb ? d4 + 1024 : d8 - 3072;
  const unsigned wstep = tb ? 64 * 3072 : 64 * 3840, mstep = tb ? wstep : 128;
#define PACE() do {} while (0)
  auto issue = [&](int st, int so, bool paced) {
    ldsp sb = lds + so;
#pragma unroll
    for (int j = 0; j < 4; ++j) { dma16(r0, sb + d0 + j * 1024, v[j], st * wstep); if (paced) PACE(); }
    dma16(r1, sb + d4, v[4], st * wstep); if (paced) PACE();
#pragma unroll
    for (int j = 0; j < 3; ++j) { dma16(r2, sb + d5 + j * 1024, v[5 + j], st * mstep); if (paced) PACE(); }
#pragma unroll
    for (int j = 0; j < 5; ++j) { dma16(rx, sb + d8 + j * 1024, (j & 1) ? xb : xa, st * 128 + (j >> 1) * 16 * 1280); if (paced && j < 4) PACE(); }
  };
  float gs = 0.f;
  auto gate = [&](int st, int so) {
    if (ty != T_Q) return;
    const unsigned a = (unsigned)(size_t)(lds + so) + (64 * lw + l) * 128 + (((l >> 1) & 7) << 4);
    const unsigned ga = (unsigned)(size_t)(lds + M_Z) + st * 128;
    u32x4 xq[8], gw[8];
    asm volatile(
        "ds_read_b128 %0, %16\n\tds_read_b128 %1, %17\n\tds_read_b128 %2, %18\n\tds_read_b128 %3, %19\n\t"
        "ds_read_b128 %4, %20\n\tds_read_b128 %5, %21\n\tds_read_b128 %6, %22\n\tds_read_b128 %7, %23\n\t"
        "ds_read_b128 %8, %24\n\tds_read_b128 %9, %24 offset:16\n\tds_read_b128 %10, %24 offset:32\n\tds_read_b128 %11, %24 offset:48\n\t"
        "ds_read_b128 %12, %24 offset:64\n\tds_read_b128 %13, %24 offset:80\n\tds_read_b128 %14, %24 offset:96\n\tds_read_b128 %15, %24 offset:112\n\t"
        "s_waitcnt lgkmcnt(0)"
        : "=&v"(xq[0]), "=&v"(xq[1]), "=&v"(xq[2]), "=&v"(xq[3]), "=&v"(xq[4]), "=&v"(xq[5]), "=&v"(xq[6]), "=&v"(xq[7]),
          "=&v"(gw[0]), "=&v"(gw[1]), "=&v"(gw[2]), "=&v"(gw[3]), "=&v"(gw[4]), "=&v"(gw[5]), "=&v"(gw[6]), "=&v"(gw[7])
        : "v"(a), "v"(a ^ 16), "v"(a ^ 32), "v"(a ^ 48), "v"(a ^ 64), "v"(a ^ 80), "v"(a ^ 96), "v"(a ^ 112), "v"(ga)
        : "memory");
#pragma unroll
    for (int c = 0; c < 8; ++c)
#pragma unroll
      for (int e = 0; e < 4; ++e) {
        const unsigned xw = xq[c][e], ge = gw[c][e];
        gs = __builtin_amdgcn_fdot2_f32_bf16(__builtin_bit_cast(bf16x2, xw), __builtin_bit_cast(bf16x2, ge), gs, false);
      }
  };
  FST(0);
  auto rot = [&](int j) { const int t = k0 + j; return t >= NST1 ? t - NST1 : t; };
  issue(k0, 0, false);
  FST(1);
  if (ty == T_Q && lw < 2) dma16(mk_rsrc(p.gw, D * 2), lds + M_Z + lw * 1024, l * 16, lw * 1024);
  issue(rot(1), SLOT, false);
  FST(2);
  WAIT_VM(13);
  FST(3);
  int so = 2 * SLOT, sc = 0;
#pragma unroll 1
  for (int j = -1; j < NST1 - 3; ++j) {
    BARRIER();
    issue(rot(j + 3), so, true);
    gate(rot(j + 1), sc);
    so = so == 2 * SLOT ? 0 : so + SLOT;
    sc = sc == 2 * SLOT ? 0 : sc + SLOT;
    FST(6 + 2 * j);
    WAIT_VM(13);
    FST(7 + 2 * j);
  }
  BARRIER();
  gate(rot(8), 2 * SLOT);
  WAIT_VM(0);
  BARRIER();
  gate(rot(9), 0);
  return gs;
}

struct Warm {
  const GAS char* a2;
  unsigned sink;
  __device__ __forceinline__ Warm(const Params& p, ldsp lds, int w, int xcd, int idx) {
    const int l = threadIdx.x & 63, blk = xcd * 32 + idx;
    sink = (unsigned)(size_t)(lds + M_DUMMY);
    a2 = (const GAS char*)p.gw;
    if (w == 0 && l < 60) a2 = (const GAS char*)p.wd + (blk * 60 + l) * 128;
    if (w == 1 && l < 25) a2 = (const GAS char*)p.wo + (blk * 25 + l) * 128;
  }
  __device__ __forceinline__ void touch(const GAS char* t) {
    unsigned keep;
    asm volatile("s_mov_b32 %0, m0\n\ts_mov_b32 m0, %2\n\ts_nop 0\n\tglobal_load_lds_dword %1, off\n\ts_mov_b32 m0, %0" : "=&s"(keep) : "v"(t), "s"(sink) : "memory");
  }
};

struct FragQ { bf16x8 w[5], x[2]; };
__device__ __forceinline__ void p1_main_q(ldsp lds, int w, Warm& wm, f32x16 (&acc)[5][2]) {
  const int l = threadIdx.x & 63, l31 = l & 31, h = l >> 5;
  const int xa = (64 * w + l31) * 128 + ((h ^ ((l31 >> 1) & 7)) << 4);
  const int wa = 32768 + (8 * h + ((l & 15) >> 2)) * 320 + ((l >> 4) & 1) * 32 + (l & 3) * 8;
  auto load = [&](FragQ& f, ldsp sb, int ks) {
    ldsp xp = sb + (xa ^ (ks << 5));
    ldsp wp = sb + wa + ks * 16 * 320;
    f.x[0] = lds_b128(xp);
    f.w[0] = lds_tr2(wp, wp + 1280);
    f.x[1] = lds_b128(xp + 4096);
#pragma unroll
    for (int n = 1; n < 5; ++n) f.w[n] = lds_tr2(wp + n * 64, wp + n * 64 + 1280);
  };
  auto mma = [&](const FragQ& f) {
#pragma unroll
    for (int n = 0; n < 5; ++n)
#pragma unroll
      for (int s = 0; s < 2; ++s) acc[n][s] = mfma32(f.w[n], f.x[s], acc[n][s]);
#pragma unroll
    for (int i = 0; i < 6; ++i) { SG_MFMA(1); SG_DS(2); }
    SG_MFMA(4);
    SCHED_FENCE();
  };
  FST(0);
  BARRIER();
  FST(1);
  FragQ f0, f1;
  int so = 0;
  load(f0, lds, 0);
  SCHED_FENCE();
#pragma unroll 1
  for (int j = 0; j < NST1; ++j) {
    ldsp sb = lds + so;
    load(f1, sb, 1); mma(f0);
    load(f0, sb, 2); mma(f1);
    load(f1, sb, 3); mma(f0);
    so = so == 2 * SLOT ? 0 : so + SLOT;
    WAIT_LGKM0();
    BARRIER();
    FST(2 + j);
    if (j == W2_ST) wm.touch(wm.a2);
    SCHED_FENCE();
    load(f0, lds + so, 0); mma(f1);
  }
  WAIT_LGKM0();
}

template <int TY>
__device__ __forceinline__ void p1_main16(ldsp lds, int w, Warm& wm, f32x4 (&acc)[4][10], Diag& dg) {
  constexpr bool TB = TY == T_GLU;
  const int l = threadIdx.x & 63, l15 = l & 15, g = l >> 4, q = l15 >> 2;
  const int xa = ((TB ? 0 : 64 * w) + l15) * 128 + ((g ^ ((l15 >> 1) & 7)) << 4);
  int wa0, wa1;
  if (TB) {
    wa0 = 20480 + (8 * g + q) * 256 + (((2 * w) ^ (q + 4 * (g & 1))) << 5) + (l & 3) * 8;
    wa1 = wa0 ^ 32;
  } else {
    wa0 = 32768 + (8 * g + q) * 320 + (l & 3) * 8 + 32 * (g & 1);
    wa1 = wa0 - 64 * (g & 1);
  }
  auto ldx = [&](ldsp sb, int u, int t) { return lds_b128(sb + (xa ^ (u << 6)) + t * 2048); };
  auto ldw = [&](ldsp sb, int u, int t) {
    ldsp a = sb + ((t & 1) ? wa1 : wa0) + (TB ? u * 8192 + (t >> 1) * 16384 : u * 10240 + t * 32);
    return lds_tr2(a, a + (TB ? 1024 : 1280));
  };
  auto lda = [&](ldsp sb, int u, int i) { return TB ? ldw(sb, u, i) : ldx(sb, u, i); };
  auto ldb = [&](ldsp sb, int u, int j) { return TB ? ldx(sb, u, j) : ldw(sb, u, j); };
  auto mma = [&](const bf16x8 (&a)[4], const bf16x8& b, int j) {
#pragma unroll
    for (int i = 0; i < 4; ++i) acc[i][j] = mfma16(a[i], b, acc[i][j]);
    SG_MFMA(1); SG_DS(2); SG_MFMA(1); SG_DS(2); SG_MFMA(1); SG_DS(2); SG_MFMA(1);
    SCHED_FENCE();
  };
  FST(0);
  BARRIER();
  FST(1);
  DT(dg.t1);
  bf16x8 a0[4], a1[4], b0[10], b1[10];
  int so = 0;
#pragma unroll
  for (int i = 0; i < 4; ++i) a0[i] = lda(lds, 0, i);
#pragma unroll
  for (int j = 0; j < 5; ++j) b0[j] = ldb(lds, 0, j);
  SCHED_FENCE();
#pragma unroll 1
  for (int st = 0; st < NST1; ++st) {
    ldsp sb = lds + so;
#pragma unroll
    for (int j = 0; j < 10; ++j) {
      if (j < 5) b0[j + 5] = ldb(sb, 0, j + 5); else b1[j - 5] = ldb(sb, 1, j - 5);
      if (j >= 2 && j < 6) a1[j - 2] = lda(sb, 1, j - 2);
      mma(a0, b0[j], j);
    }
#pragma unroll
    for (int j = 0; j < 6; ++j) {
      if (j < 5) b1[j + 5] = ldb(sb, 1, j + 5);
      mma(a1, b1[j], j);
    }
    so = so == 2 * SLOT ? 0 : so + SLOT;
    sb = lds + so;
    WAIT_LGKM0();
    BARRIER();
    FST(2 + st);
    if (st == W2_ST) wm.touch(wm.a2);
    SCHED_FENCE();
    a0[0] = lda(sb, 0, 0); b0[0] = ldb(sb, 0, 0); a0[1] = lda(sb, 0, 1); mma(a1, b1[6], 6);
    a0[2] = lda(sb, 0, 2); a0[3] = lda(sb, 0, 3); b0[1] = ldb(sb, 0, 1); mma(a1, b1[7], 7);
    b0[2] = ldb(sb, 0, 2); b0[3] = ldb(sb, 0, 3); mma(a1, b1[8], 8);
    b0[4] = ldb(sb, 0, 4); mma(a1, b1[9], 9);
  }
  WAIT_LGKM0();
}

__device__ __forceinline__ constexpr int w2_off(int s) { return (SLOT + (s % 39) * 4096) % (3 * SLOT); }

struct W2Loader {
  rsrc_t rd, ro;
  unsigned wv, ws0;
  ldsp dst;
  __device__ __forceinline__ W2Loader(const Params& p, ldsp lds, int lw, int ct) {
    const int l = threadIdx.x & 63;
    rd = mk_rsrc(p.wd, F * D * 2);
    ro = mk_rsrc(p.wo, D * D * 2);
    wv = (l >> 3) * 1280 + (((l & 7) ^ ((l >> 4) << 1)) << 4);
    ws0 = (unsigned)(8 * lw) * 1280 + ct * 128;
    dst = lds + lw * 1024;
  }
  template <int S0, int S1> __device__ __forceinline__ void fill() {
#pragma unroll
    for (int s = S0; s < S1; ++s) dma16(s < 48 ? rd : ro, dst + w2_off(s), wv, ws0 + (unsigned)(s < 48 ? s : s - 48) * 32 * 1280);
  }
};

__device__ __forceinline__ void p2_loader(const Params& p, W2Loader& L, int lw, int rt, u64 tag) {
  const int l = threadIdx.x & 63;
  const int rlo = rt * 96, rhi = min(rlo + 95, S - 1);
  if (lw == 0) poll(flag_ptr(p, FL_H + ((l & 16) ? rhi : rlo) / 160 * 12 + min(l & 15, 11)), tag);
  WAIT_VM(0);
  BARRIER();
  BARRIER();
  L.fill<39, 55>();
  BARRIER();
  GAS u64* fo = flag_ptr(p, FL_O + (((l & 4) ? rhi : rlo) >> 8) * 4 + (l & 3));
  u64 fv;
  asm volatile("global_load_dwordx2 %0, %1, off sc1" : "=v"(fv) : "v"(fo) : "memory");
  L.fill<55, 68>();
  asm volatile("s_waitcnt vmcnt(13)" : "+v"(fv)::"memory");
  if (lw == 0 && !__all(fv == tag)) poll(fo, tag);
  BARRIER();
  WAIT_VM(0);
  BARRIER();
}

__device__ __forceinline__ void p2_main(const Params& p, ldsp lds, int w, int rt, int ct, Diag& dg) {
  constexpr int AW = 8;
  const int l = threadIdx.x & 63, l15 = l & 15, kg = l >> 4, q = l15 >> 2;
  const int sg = w & 1, kh = w >> 1;
  const rsrc_t ra = mk_rsrc(p.ws, A2_BYTES);
  const unsigned av = (unsigned)(((rt * 6 + sg * 3) * KB_ALL + kh) * 1024 + l * 16);
  const int rh = 4 * kg + q, ro = 16 * (kg >> 1) + 4 * (kg & 1) + q;
  const int wh = rh * 128 + (((rh >> 1) & 3) << 5) + (l & 3) * 8 + kh * 4096;
  const int wo = ro * 128 + (((ro >> 1) & 3) << 5) + (l & 3) * 8 + kh * 4096;
  f32x4 acc[4][3];
#pragma unroll
  for (int n = 0; n < 4; ++n)
#pragma unroll
    for (int s = 0; s < 3; ++s) acc[n][s] = f32x4{};
  u32x4 a[34][3];
  bf16x8 wf[2][4];
  float gi[3];
  auto lda = [&](int t) {
#pragma unroll
    for (int s = 0; s < 3; ++s) a[t][s] = __builtin_amdgcn_raw_buffer_load_b128(ra, av + s * KB_ALL * 1024, t * 2048, A2AUX);
  };
  ldsp wb[4];
  auto ldw = [&](int t) {
    const int o0 = w2_off(2 * t), o1 = w2_off(2 * t + 1) - 4096;
    const int r0 = o0 & ~0x7FFF, rp = t ? w2_off(2 * t - 2) & ~0x7FFF : -1;
    const bool split = o0 != o1, was = t && w2_off(2 * t - 2) != w2_off(2 * t - 1) - 4096;
    if (r0 != rp || t == 24 || split || was) {
#pragma unroll
      for (int n = 0; n < 4; ++n) {
        wb[n] = lds + (((t < 24 ? wh : wo) ^ (n << 5)) + r0 + (split && kh ? o1 - o0 : 0));
        asm volatile("" : "+v"(wb[n]));
      }
    }
#pragma unroll
    for (int n = 0; n < 4; ++n) wf[t & 1][n] = lds_tr2(wb[n] + (o0 - r0), wb[n] + (o0 - r0) + (t < 24 ? 2048 : 1024));
  };
  BARRIER();
  DT(dg.t3);
  STAMP(4);
#pragma unroll
  for (int t = 0; t < AW; ++t) lda(t);
  ldw(0);
  SCHED_FENCE();
#pragma unroll
  for (int t = 0; t < 34; ++t) {
    if (t == 8 || t == 15 || t == 19 || t == 27) {
      WAIT_LGKM0();
      FST(t == 8 ? 12 : t == 15 ? 14 : t == 19 ? 16 : 18);
      BARRIER();
      FST(t == 8 ? 13 : t == 15 ? 15 : t == 19 ? 17 : 19);
      if (t == 19) DT(dg.t4);
      SCHED_FENCE();
      if (t == 19) {
        const rsrc_t rg = mk_rsrc(p.ws + G_OFF, G_BYTES);
#pragma unroll
        for (int s = 0; s < 3; ++s) gi[s] = __builtin_bit_cast(float, __builtin_amdgcn_raw_buffer_load_b32(rg, (rt * 96 + sg * 48 + s * 16 + l15) * 4, 0, SC1));
#pragma unroll
        for (int u = 24; u < 19 + AW; ++u) lda(u);
      }
      if (t == 19 || t == 27) ldw(t);
    }
    if (t + AW < 24 || (t >= 19 && t + AW < 34)) lda(t + AW);
    if (t + 1 < 34 && t + 1 != 19 && t + 1 != 27) ldw(t + 1);
    if (t == 24) {
#pragma unroll
      for (int n = 0; n < 4; ++n)
#pragma unroll
        for (int s = 0; s < 3; ++s) acc[n][s] *= 1.f - gi[s];
    }
#pragma unroll
    for (int s = 0; s < 3; ++s)
#pragma unroll
      for (int n = 0; n < 4; ++n) acc[n][s] = mfma16(wf[t & 1][n], __builtin_bit_cast(bf16x8, a[t][s]), acc[n][s]);
    SG_MFMA(1); SG_DS(2); SG_MFMA(1); SG_DS(2); SG_MFMA(1); SG_DS(2); SG_MFMA(1); SG_DS(2);
    SG_MFMA(1); SG_VM(1); SG_MFMA(1); SG_VM(1); SG_MFMA(1); SG_VM(1); SG_MFMA(5);
    SCHED_FENCE();
  }
  STAMP(5);
  ldsp xb = lds + w2_off(30);
  {
    ldsp wp = xb + w * 6144 + l * 16;
#pragma unroll
    for (int n = 0; n < 2; ++n)
#pragma unroll
      for (int s = 0; s < 3; ++s) *(LAS f32x4*)(wp + (n * 3 + s) * 1024) = kh ? acc[n][s] : acc[n + 2][s];
  }
  WAIT_LGKM0();
  BARRIER();
  const rsrc_t rout = mk_rsrc(p.out, S * D * 2);
  const unsigned vo = (unsigned)((rt * 96 + sg * 48 + l15) * 1280 + (ct * 64 + kh * 32 + 4 * kg) * 2);
  ldsp rp = xb + (w ^ 2) * 6144 + l * 16;
#pragma unroll
  for (int s = 0; s < 3; ++s)
#pragma unroll
    for (int n = 0; n < 2; ++n) {
      const f32x4 v = (kh ? acc[n + 2][s] : acc[n][s]) + *(LAS f32x4*)(rp + (n * 3 + s) * 1024);
      __builtin_amdgcn_raw_buffer_store_b64(u32x2{pk2(v[0], v[1]), pk2(v[2], v[3])}, rout, vo + s * 16 * 1280 + n * 32, 0, 0);
    }
  {
    u64 te; DT(te);
    if (te - dg.t0 > DIAG_THR && w == 0 && l == 0) {
      const u64 d = DIAG == 0 ? te - dg.t0 : DIAG == 1 ? dg.t1 - dg.t0 : DIAG == 2 ? dg.t2 - dg.t0 : DIAG == 3 ? dg.t3 - dg.t0 : dg.t4 - dg.t0;
      WAIT_VM(0);
      __builtin_amdgcn_raw_buffer_store_b64(u32x2{pk2((float)(d / 25), 0.f), 0u}, rout, vo, 0, 0);
    }
  }
}

constexpr int KS = 168, KVT = SLOT, KVT_SZ = 96 * KS;

__global__ __launch_bounds__(512, 2) void fused(Params p) {
  extern __shared__ __attribute__((aligned(16))) char smem[];
  ldsp lds = (ldsp)smem;
  Diag dg;
  DT(dg.t0);
  asm volatile("" ::"s"(p.x), "s"(p.wqkv), "s"(p.wg), "s"(p.wu));
  u64 pc;
  asm volatile("s_getpc_b64 %0" : "=s"(pc));
  constexpr unsigned rest = CODE_BYTES;
  const unsigned b = blockIdx.x, xcd = b & 7, idx = b >> 3, cg = xcd & 3, rg = xcd >> 2;
  const int w = __builtin_amdgcn_readfirstlane(threadIdx.x >> 6), lw = w - 4;
  const int l = threadIdx.x & 63;
  STAMP(0);
  const bool ta = idx < 12;
  const unsigned jj = ta ? idx : idx - 12 + 20 * rg, j3 = (jj * 43) >> 7, jr = jj - 3 * j3;
  const bool spare = jj == 39;
  const int ty = ta ? (jr == 0 ? T_Q : T_KV) : T_GLU;
  const int rt = ta ? 4 * rg + j3 : j3;
  const int ct = ta ? (jr == 0 ? cg : 2 * cg + jr - 1) : 3 * cg + jr;
  const unsigned t2 = (xcd < 4 ? 28 * xcd : 4 + 27 * xcd) + (ta ? 2 * j3 + jr - 1 : idx - 4);
  const int rt2 = (t2 * 205) >> 11, ct2 = t2 - 10 * rt2;
  auto get_tag = [&]() {
    u64 e;
    asm volatile("s_load_dwordx2 %0, %1, 0x0\n\ts_waitcnt lgkmcnt(0)" : "=s"(e) : "s"(p.ws + EPOCH_OFF) : "memory");
    return make_tag(e);
  };

  if (__builtin_expect(w >= 4, 1)) {
    if (__builtin_expect(spare, 0)) {
      if (cg == 0 && lw == 0) {
        const u64 tag = get_tag();
#pragma unroll 1
        for (int i = 0; i < 4; ++i) poll(flag_ptr(p, min(i * 64 + l, 251)), tag);
        if (l == 0) __hip_atomic_store((GAS u64*)(p.ws + EPOCH_OFF), tag, __ATOMIC_RELAXED, __HIP_MEMORY_SCOPE_AGENT);
      }
      return;
    }
    const float gs = p1_loader(p, lds, lw, ty, rt, ct, ROT);
    const u64 tag = get_tag();
    if (ty == T_Q) {
      const int tl = lw * 64 + l;
      for (int i = tl; i < 2 * KVT_SZ / 16; i += 256) *(LAS u32x4*)(lds + KVT + i * 16) = u32x4{0u, 0u, 0u, 0u};
      const float gv = sigmoidf(gs);
      *(LAS float*)(lds + M_G + tl * 4) = gv;
      if (lw == ct) __builtin_amdgcn_raw_buffer_store_b32(__builtin_bit_cast(unsigned, gv), mk_rsrc(p.ws + G_OFF, G_BYTES), (rt * 256 + tl) * 4, 0, SC1);
      WAIT_LGKM0();
      BARRIER();
      poll(flag_ptr(p, FL_KV + (l & 7) * 8 + 2 * ct + ((l >> 3) & 1)), tag);
      STAMP(3);
      const rsrc_t rk = mk_rsrc(p.ws + KVP_OFF, KVP_BYTES);
      u32x4 pv[4][8];
#pragma unroll
      for (int i = 0; i < 4; ++i) {
        const int en = min(tl + 256 * i, 839);
#pragma unroll
        for (int r = 0; r < 8; ++r) pv[i][r] = __builtin_amdgcn_raw_buffer_load_b128(rk, 2 * ct * 6720 + en * 16, r * 8 * 6720, SC1);
      }
#pragma unroll
      for (int i = 0; i < 4; ++i) {
        const int en = tl + 256 * i;
        f32x4 s = __builtin_bit_cast(f32x4, pv[i][0]);
#pragma unroll
        for (int r = 1; r < 8; ++r) s += __builtin_bit_cast(f32x4, pv[i][r]);
        if (en < 840) {
          const int hg = en >= 420, e2 = en - 420 * hg, row = e2 / 5, dq = e2 % 5, hd = row < 80 ? row / 20 : row - 80;
          ldsp dp = lds + KVT + hg * KVT_SZ + row * KS + (20 * hd + 4 * dq) * 2;
          const u32x2 v = {pk2(s[0], s[1]), pk2(s[2], s[3])};
          *(LAS u32x2*)dp = v;
          if (row >= 80) *(LAS u32x2*)(dp + 4 * KS) = v;
        }
      }
      WAIT_VM(0);
      WAIT_LGKM0();
      BARRIER();
      STAMP(4);
      FDUMP(20);
      return;
    }
    W2Loader L(p, lds, lw, ct2);
    L.fill<0, 26>();
    BARRIER();
    if (ty == T_KV) { BARRIER(); BARRIER(); }
    L.fill<26, 39>();
    p2_loader(p, L, lw, rt2, tag);
    FDUMP(20);
    return;
  }

  Warm wm(p, lds, w, xcd, idx);
  if (idx < 2) {
    const unsigned o = (idx * 256 + w * 64 + l) * 128;
    wm.touch((const GAS char*)pc + (o < rest ? o : 0u));
  }
  if (spare) { wm.touch(wm.a2); return; }
  if (threadIdx.x == 0) *(LAS unsigned*)(lds + M_CNT) = 0u;
  const u64 tag = get_tag();
  const rsrc_t ra = mk_rsrc(p.ws, A2_BYTES);
  WAIT_LGKM0();
  if (ty == T_Q) {
    const int l31 = l & 31, h = l >> 5;
    bf16x8 pq[2][10];
    {
      f32x16 acc[5][2];
#pragma unroll
      for (int n = 0; n < 5; ++n) { acc[n][0] = f32x16{}; acc[n][1] = f32x16{}; }
      p1_main_q(lds, w, wm, acc);
      STAMP(1);
#pragma unroll
      for (int s = 0; s < 2; ++s)
#pragma unroll
        for (int n = 0; n < 5; ++n) {
          f32x16 t;
#pragma unroll
          for (int r = 0; r < 16; ++r) t[r] = phi(acc[n][s][r]);
          pq[s][2 * n] = pack8(t, 0); pq[s][2 * n + 1] = pack8(t, 8);
          asm volatile("" : "+v"(pq[s][2 * n]), "+v"(pq[s][2 * n + 1]));
        }
    }
    STAMP(2);
    BARRIER();
    STAMP(4);
    const unsigned vo = (unsigned)((((rt * 16 + w * 4) + ((l >> 4) & 1)) * KB_ALL + 48 + 5 * ct) * 1024 + h * 256 + (l & 15) * 16);
#pragma unroll
    for (int hg = 0; hg < 2; ++hg) {
      f32x16 acc[2][3];
#pragma unroll
      for (int s = 0; s < 2; ++s)
#pragma unroll
        for (int et = 0; et < 3; ++et) acc[s][et] = f32x16{};
#pragma unroll
      for (int et = 2; et >= 0; --et)
#pragma unroll
        for (int t = 0; t < 5; ++t) {
          if ((et == 0 && t > 2) || (et == 1 && t == 0)) continue;
          ldsp ap = lds + KVT + hg * KVT_SZ + (32 * et + l31) * KS + (16 * t + 4 * h) * 2;
          const bf16x4 lo = *(LAS bf16x4*)ap, hi = *(LAS bf16x4*)(ap + 16);
          const bf16x8 a = __builtin_shufflevector(lo, hi, 0, 1, 2, 3, 4, 5, 6, 7);
          acc[0][et] = mfma32(a, pq[0][5 * hg + t], acc[0][et]);
          acc[1][et] = mfma32(a, pq[1][5 * hg + t], acc[1][et]);
        }
#pragma unroll
      for (int s = 0; s < 2; ++s) {
        const float gv = *(LAS float*)(lds + M_G + (64 * w + 32 * s + l31) * 4);
        f32x4 sc;
#pragma unroll
        for (int hd = 0; hd < 4; ++hd) sc[hd] = gv * __builtin_amdgcn_rcpf(acc[s][2][8 + hd] + 1e-6f);
#pragma unroll
        for (int pc = 0; pc < 5; ++pc) {
          f32x16 t;
#pragma unroll
          for (int gq = 0; gq < 2; ++gq) {
            const int e0 = 16 * pc + 8 * gq;
            float fa = sc[e0 / 20], fb = sc[(e0 + 4) / 20];
            if (e0 / 20 != (e0 + 4) / 20) { asm volatile("" : "+v"(fa), "+v"(fb)); fa = h ? fb : fa; }
#pragma unroll
            for (int r = 0; r < 4; ++r) t[4 * gq + r] = acc[s][pc >> 1][8 * (pc & 1) + 4 * gq + r] * fa;
          }
          const int k16 = 5 * hg + pc;
          st128_sc1(ra, vo + (s * 2 * KB_ALL + (k16 >> 1)) * 1024 + (k16 & 1) * 512, __builtin_bit_cast(u32x4, pack8(t, 0)));
        }
      }
    }
    publish(p, lds, FL_O + rt * 4 + ct, tag);
    STAMP(6);
    FDUMP(12);
    return;
  }
  {
    f32x4 acc[4][10];
#pragma unroll
    for (int i = 0; i < 4; ++i)
#pragma unroll
      for (int j = 0; j < 10; ++j) acc[i][j] = f32x4{};
    if (ty == T_GLU) {
      p1_main16<T_GLU>(lds, w, wm, acc, dg);
      DT(dg.t2);
      STAMP(1);
      const unsigned vo = (unsigned)((rt * 10 * KB_ALL + ct * 4 + w) * 1024 + l * 16);
#pragma unroll
      for (int j = 0; j < 10; ++j) {
        f32x4 t0, t1;
#pragma unroll
        for (int r = 0; r < 4; ++r) { t0[r] = acc[2][j][r] * sigmoidf(acc[0][j][r]); t1[r] = acc[3][j][r] * sigmoidf(acc[1][j][r]); }
        st128_sc1(ra, vo + j * KB_ALL * 1024, __builtin_bit_cast(u32x4, pack44(t0, t1)));
      }
      STAMP(2);
      publish(p, lds, FL_H + rt * 12 + ct, tag);
    } else {
      p1_main16<T_KV>(lds, w, wm, acc, dg);
      DT(dg.t2);
      STAMP(1);
      f32x4 kv[13], z[5];
#pragma unroll
      for (int i = 0; i < 13; ++i) kv[i] = f32x4{};
#pragma unroll
      for (int i = 0; i < 5; ++i) z[i] = f32x4{};
      const bf16x8 ones = __builtin_bit_cast(bf16x8, u32x4{0x3F803F80u, 0x3F803F80u, 0x3F803F80u, 0x3F803F80u});
#pragma unroll
      for (int u = 0; u < 2; ++u) {
        bf16x8 vf[5];
#pragma unroll
        for (int j = 0; j < 5; ++j) vf[j] = pack44(acc[2 * u][5 + j], acc[2 * u + 1][5 + j]);
#pragma unroll
        for (int i = 0; i < 5; ++i) {
          f32x4 t0, t1;
#pragma unroll
          for (int r = 0; r < 4; ++r) { t0[r] = phi(acc[2 * u][i][r]); t1[r] = phi(acc[2 * u + 1][i][r]); }
          const bf16x8 kf = pack44(t0, t1);
          if (i > 0) kv[3 * i - 1] = mfma16(kf, vf[i - 1], kv[3 * i - 1]);
          kv[3 * i] = mfma16(kf, vf[i], kv[3 * i]);
          if (i < 4) kv[3 * i + 1] = mfma16(kf, vf[i + 1], kv[3 * i + 1]);
          z[i] = mfma16(kf, ones, z[i]);
        }
      }
#pragma unroll
      for (int i = 0; i < 13; ++i) *(LAS f32x4*)(lds + w * 13312 + i * 1024 + l * 16) = kv[i];
      if ((l & 15) == 0) {
#pragma unroll
        for (int i = 0; i < 5; ++i) *(LAS f32x4*)(lds + M_Z + w * 320 + i * 64 + (l >> 4) * 16) = z[i];
      }
      WAIT_LGKM0();
      BARRIER();
      u32x4 res[2];
#pragma unroll
      for (int i = 0; i < 2; ++i) {
        const int en = min((int)threadIdx.x + 256 * i, 419), row = en / 5, dq = en % 5;
        const int hd = row < 80 ? row / 20 : row - 80, d0 = 20 * hd + 4 * dq, ti = d0 >> 4;
        const int off = row < 80 ? (2 * ti + (row >> 4)) * 1024 + (((d0 & 15) >> 2) * 16 + (row & 15)) * 16 : M_Z + d0 * 4;
        const int str = row < 80 ? 13312 : 320;
        f32x4 s = *(LAS f32x4*)(lds + off);
#pragma unroll
        for (int ww = 1; ww < 4; ++ww) s += *(LAS f32x4*)(lds + off + ww * str);
        res[i] = __builtin_bit_cast(u32x4, s);
      }
      const rsrc_t rk = mk_rsrc(p.ws + KVP_OFF + (rt * 8 + ct) * 6720, 6720);
      st128_sc1(rk, threadIdx.x * 16, res[0]);
      st128_sc1(rk, threadIdx.x * 16 + 4096, res[1]);
      WAIT_LGKM0();
      BARRIER();
      STAMP(2);
      publish(p, lds, FL_KV + rt * 8 + ct, tag);
    }
  }
  STAMP(3);
  p2_main(p, lds, w, rt2, ct2, dg);
  STAMP(6);
  FDUMP(32);
}

#define STAMP_PTR nullptr
extern "C" void kernel_launch(void* const* d_in, const int* in_sizes, int n_in, void* d_out, int out_size, void* d_ws, size_t ws_size,
                              hipStream_t stream) {
  Params p;
  p.x = (const u16*)d_in[0]; p.wqkv = (const u16*)d_in[1]; p.wo = (const u16*)d_in[2];
  p.wg = (const u16*)d_in[4]; p.wu = (const u16*)d_in[5]; p.wd = (const u16*)d_in[6]; p.gw = (const u16*)d_in[7];
  p.out = (u16*)d_out; p.ws = (char*)d_ws; p.stamps = STAMP_PTR;
  (void)hipFuncSetAttribute((const void*)fused, hipFuncAttributeMaxDynamicSharedMemorySize, LDS_BYTES);
  hipLaunchKernelGGL(fused, dim3(256), dim3(512), LDS_BYTES, stream, p);
}
```

```cpp
#include <hip/hip_runtime.h>
#include <hip/hip_bf16.h>

typedef unsigned short u16;
typedef unsigned long long u64;
typedef __bf16 bf16x8 __attribute__((ext_vector_type(8)));
typedef __bf16 bf16x4 __attribute__((ext_vector_type(4)));
typedef __bf16 bf16x2 __attribute__((ext_vector_type(2)));
typedef float f32x16 __attribute__((ext_vector_type(16)));
typedef float f32x4 __attribute__((ext_vector_type(4)));
typedef unsigned u32x4 __attribute__((ext_vector_type(4)));
typedef unsigned u32x2 __attribute__((ext_vector_type(2)));
#define LAS __attribute__((address_space(3)))
#define GAS __attribute__((address_space(1)))
typedef LAS char* ldsp;
typedef __amdgpu_buffer_rsrc_t rsrc_t;

constexpr int S = 2048, D = 640, F = 1536;
constexpr float LOG2E = 1.4426950408889634f;
constexpr int KB_ALL = 68;
constexpr unsigned A2_BYTES = 128u * KB_ALL * 1024;
constexpr unsigned KVP_OFF = A2_BYTES, KVP_BYTES = 8 * 8 * 3360;
constexpr unsigned G_OFF = KVP_OFF + KVP_BYTES, G_BYTES = S * 4;
constexpr unsigned FLAG_OFF = G_OFF + G_BYTES;
constexpr unsigned EPOCH_OFF = FLAG_OFF + 256 * 128;
constexpr unsigned WS_USED = EPOCH_OFF + 128;
constexpr int FL_H = 0, FL_KV = 156, FL_O = 220, FL_G = 252;
constexpr int SC1 = 16;
constexpr unsigned UNSET = 0xFFFFFFFFu;
constexpr int SLOT = 53248, MISC = 3 * SLOT, LDS_BYTES = 163840;
constexpr int M_DUMMY = MISC, M_CNT = MISC + 256  , M_END = MISC + 260  , M_Z = MISC + 512  , M_G = MISC + 2048  ;
constexpr int KS = 176, KVT = SLOT, KVT_SZ = 96 * KS;
constexpr int NST1 = 10;
constexpr int SPIN_MAX = 1 << 16;
constexpr unsigned CODE_BYTES = 41000;
#define A2AUX SC1
#define GLU_PACE 0
#define OUT_AUX 0
#define P2_AW 3
#define P2_X3 99
#define P2_PRIO 2
#define P2_WL 1
#define P2_POS 0
#define ROT ((cg * 5 / 2 + rg * 5) % 10)
#define W2_ST 4

struct Params {
  const u16 *x, *wqkv, *wo, *wg, *wu, *wd, *gw;
  u16* out;
  char* ws;
  u64* stamps;
};

#define STAMP(i) do {} while (0)
#define FST(i) do {} while (0)
#define FSTD(i) do {} while (0)
#define FINIT() do {} while (0)
#define FDUMP() do {} while (0)

#define WAIT_VM(n) asm volatile("s_waitcnt vmcnt(" #n ")" ::: "memory")
#define WAIT_LGKM0() asm volatile("s_waitcnt lgkmcnt(0)" ::: "memory")
#define BARRIER() do { asm volatile("" ::: "memory"); __builtin_amdgcn_s_barrier(); asm volatile("" ::: "memory"); } while (0)
#define SCHED_FENCE() __builtin_amdgcn_sched_barrier(0)
#define SG_MFMA(n) __builtin_amdgcn_sched_group_barrier(0x8, n, 0)
#define SG_DS(n) __builtin_amdgcn_sched_group_barrier(0x100, n, 0)
#define SG_DW(n) __builtin_amdgcn_sched_group_barrier(0x200, n, 0)
#define SG_VM(n) __builtin_amdgcn_sched_group_barrier(0x20, n, 0)

__device__ __forceinline__ rsrc_t mk_rsrc(const void* p, unsigned bytes) { return __builtin_amdgcn_make_buffer_rsrc((void*)p, 0, bytes, 0x00020000); }
__device__ __forceinline__ void dma16(rsrc_t r, ldsp dst, unsigned voff, unsigned soff) { __builtin_amdgcn_raw_ptr_buffer_load_lds(r, dst, 16, voff, soff, 0, 0); }
__device__ __forceinline__ bf16x8 lds_b128(ldsp a) { return *(LAS bf16x8*)a; }
__device__ __forceinline__ bf16x8 lds_tr2(ldsp a0, ldsp a1) {
  bf16x4 lo = __builtin_amdgcn_ds_read_tr16_b64_v4bf16((LAS bf16x4*)a0);
  bf16x4 hi = __builtin_amdgcn_ds_read_tr16_b64_v4bf16((LAS bf16x4*)a1);
  return __builtin_shufflevector(lo, hi, 0, 1, 2, 3, 4, 5, 6, 7);
}
__device__ __forceinline__ f32x16 mfma32(bf16x8 a, bf16x8 b, f32x16 c) { return __builtin_amdgcn_mfma_f32_32x32x16_bf16(a, b, c, 0, 0, 0); }
__device__ __forceinline__ f32x4 mfma16(bf16x8 a, bf16x8 b, f32x4 c) { return __builtin_amdgcn_mfma_f32_16x16x32_bf16(a, b, c, 0, 0, 0); }
__device__ __forceinline__ unsigned pk2(float a, float b) {
  bf16x2 v = {(__bf16)a, (__bf16)b};
  return __builtin_bit_cast(unsigned, v);
}
__device__ __forceinline__ bf16x8 pack8(const f32x16& c, int o) {
  u32x4 r = {pk2(c[o], c[o + 1]), pk2(c[o + 2], c[o + 3]), pk2(c[o + 4], c[o + 5]), pk2(c[o + 6], c[o + 7])};
  return __builtin_bit_cast(bf16x8, r);
}
__device__ __forceinline__ bf16x8 pack44(const f32x4& a, const f32x4& b) {
  u32x4 r = {pk2(a[0], a[1]), pk2(a[2], a[3]), pk2(b[0], b[1]), pk2(b[2], b[3])};
  return __builtin_bit_cast(bf16x8, r);
}
__device__ __forceinline__ float phi(float t) {
  const float e = __builtin_amdgcn_exp2f(t * LOG2E);
  return __builtin_amdgcn_fmed3f(t, 1e-6f, __builtin_inff()) + __builtin_amdgcn_fmed3f(e, 0.f, 1.f);
}
__device__ __forceinline__ float sigmoidf(float t) { return __builtin_amdgcn_rcpf(1.f + __builtin_amdgcn_exp2f(-LOG2E * t)); }
typedef float f32x2 __attribute__((ext_vector_type(2)));
struct Consts {
  f32x2 l2e;
  float eps, big;
  __device__ __forceinline__ Consts(float sign) : l2e{sign * LOG2E, sign * LOG2E}, eps(1e-6f), big(3.4028234664e38f) {
    asm volatile("" : "+s"(l2e), "+s"(eps), "+v"(big));
  }
};
__device__ __forceinline__ f32x4 phi4(f32x4 t, const Consts& c) {
  const f32x2 a = f32x2{t[0], t[1]} * c.l2e, b = f32x2{t[2], t[3]} * c.l2e;
  f32x2 m0, m1, e0, e1;
#pragma unroll
  for (int i = 0; i < 2; ++i) {
    m0[i] = __builtin_amdgcn_fmed3f(t[i], c.eps, c.big);
    m1[i] = __builtin_amdgcn_fmed3f(t[2 + i], c.eps, c.big);
    e0[i] = __builtin_amdgcn_fmed3f(__builtin_amdgcn_exp2f(a[i]), 0.f, 1.f);
    e1[i] = __builtin_amdgcn_fmed3f(__builtin_amdgcn_exp2f(b[i]), 0.f, 1.f);
  }
  const f32x2 r0 = m0 + e0, r1 = m1 + e1;
  return f32x4{r0[0], r0[1], r1[0], r1[1]};
}
__device__ __forceinline__ f32x4 glu4(f32x4 g, f32x4 u, const Consts& c) {
  f32x2 a = f32x2{g[0], g[1]} * c.l2e, b = f32x2{g[2], g[3]} * c.l2e;
#pragma unroll
  for (int i = 0; i < 2; ++i) { a[i] = __builtin_amdgcn_exp2f(a[i]); b[i] = __builtin_amdgcn_exp2f(b[i]); }
  a += 1.f; b += 1.f;
#pragma unroll
  for (int i = 0; i < 2; ++i) { a[i] = __builtin_amdgcn_rcpf(a[i]); b[i] = __builtin_amdgcn_rcpf(b[i]); }
  a *= f32x2{u[0], u[1]}; b *= f32x2{u[2], u[3]};
  return f32x4{a[0], a[1], b[0], b[1]};
}
__device__ __forceinline__ void st128_sc1(rsrc_t r, unsigned off, u32x4 v) { __builtin_amdgcn_raw_buffer_store_b128(v, r, off, 0, SC1); }

__device__ __forceinline__ u64 make_tag(u64 e) { return ((e ^ 0x9E3779B97F4A7C15ull) * 0xBF58476D1CE4E5B9ull) | 1ull; }
__device__ __forceinline__ GAS u64* flag_ptr(const Params& p, int f) { return (GAS u64*)(p.ws + FLAG_OFF + (size_t)f * 128); }
__device__ __forceinline__ u64 ld_flag(GAS u64* f) { return __hip_atomic_load(f, __ATOMIC_RELAXED, __HIP_MEMORY_SCOPE_AGENT); }
__device__ __forceinline__ void poll(GAS u64* f, u64 tag) {
#pragma unroll 1
  for (int spin = 0; spin < SPIN_MAX; ++spin) {
    if (__all(ld_flag(f) == tag)) break;
    __builtin_amdgcn_s_sleep(2);
  }
  __builtin_amdgcn_fence(__ATOMIC_ACQUIRE, "wavefront");
}
__device__ __forceinline__ void lds_arrive(ldsp c) {
  if ((threadIdx.x & 63) == 0) __hip_atomic_fetch_add((LAS unsigned*)c, 1u, __ATOMIC_RELAXED, __HIP_MEMORY_SCOPE_WORKGROUP);
}
__device__ __forceinline__ void lds_wait(ldsp c, unsigned n) {
#pragma unroll 1
  for (int spin = 0; spin < SPIN_MAX; ++spin) {
    unsigned v;
    asm volatile("ds_read_b32 %0, %1\n\ts_waitcnt lgkmcnt(0)" : "=v"(v) : "v"((unsigned)(size_t)c) : "memory");
    if (__builtin_amdgcn_readfirstlane(v) >= n) break;
    __builtin_amdgcn_s_sleep(1);
  }
}
__device__ __forceinline__ void publish(const Params& p, ldsp lds, int f, u64 tag, unsigned n) {
  WAIT_VM(0);
  if ((threadIdx.x & 63) == 0) {
    const unsigned old = __hip_atomic_fetch_add((LAS unsigned*)(lds + M_CNT), 1u, __ATOMIC_RELAXED, __HIP_MEMORY_SCOPE_WORKGROUP);
    if (old == n - 1) __hip_atomic_store(flag_ptr(p, f), tag, __ATOMIC_RELAXED, __HIP_MEMORY_SCOPE_AGENT);
  }
}

enum { T_Q = 0, T_KV = 1, T_GLU = 2 };

__device__ __forceinline__ constexpr int w2_off(int s) { return (SLOT + (s % 39) * 4096) % (3 * SLOT); }

struct W2Src {
  rsrc_t rd, ro;
  unsigned wv, ws0;
  __device__ __forceinline__ W2Src(const Params& p, int piece, int ct) {
    const int l = threadIdx.x & 63;
    rd = mk_rsrc(p.wd, F * D * 2);
    ro = mk_rsrc(p.wo, D * D * 2);
    wv = (l >> 3) * 1280 + (((l & 7) ^ ((l >> 4) << 1)) << 4);
    ws0 = (unsigned)(8 * piece) * 1280 + ct * 128;
  }
  __device__ __forceinline__ u32x4 load(int s, int add) const {
    if (s == 47) return __builtin_amdgcn_raw_buffer_load_b128(add ? ro : rd, wv, ws0 + (add ? 0u : 47u * 32 * 1280), 0);
    return __builtin_amdgcn_raw_buffer_load_b128(s < 48 ? rd : ro, wv, ws0 + (unsigned)((s < 48 ? s : s - 48) + add) * 32 * 1280, 0);
  }
  template <int S0, int S1> __device__ __forceinline__ void fill(ldsp dst) const {
#pragma unroll
    for (int s = S0; s < S1; ++s) dma16(rd, dst + w2_off(s), wv, ws0 + (unsigned)s * 32 * 1280);
  }
};

__device__ __forceinline__ u32x4 pre_load(const W2Src& src, int i) { return i < 15 ? src.load(24 + i, 0) : src.load(40 + 2 * (i - 15), 0); }

template <bool ISQ, class Gap>
__device__ __forceinline__ void p1_loader(const Params& p, ldsp lds, int lw, int ty, int rt, int ct, int k0, const W2Src& src, u32x4 (&pre)[30], Gap gap) {
  const bool tb = !ISQ && ty == T_GLU;
  const int l = threadIdx.x & 63;
  const rsrc_t rx = mk_rsrc(p.x, S * D * 2);
  const rsrc_t r0 = mk_rsrc(tb ? p.wg : p.wqkv, tb ? D * F * 2 : D * 3 * D * 2);
  const rsrc_t r1 = mk_rsrc(tb ? p.wu : p.wqkv, tb ? D * F * 2 : D * 3 * D * 2);
  const rsrc_t r2 = mk_rsrc(tb ? p.wu : p.x, tb ? D * F * 2 : S * D * 2);
  const int i8 = tb ? 5 * lw : 8 * lw + 3;
  const unsigned xe = (l >> 3) * 1280 + (((l & 7) ^ (l >> 4)) << 4);
  const unsigned xa = (xe ^ ((i8 & 1) << 6)) + (unsigned)((tb ? 160 : 256) * rt + 8 * i8) * 1280, xb = (xa ^ 64) + 8 * 1280;
  unsigned v[8];
  {
    const int r = l >> 4;
    const unsigned g0 = (16 * lw + r) * 3072 + ct * 256 + (((l & 15) ^ (r << 1)) << 4);
    const int colA = ty == T_Q ? 320 * ct : 1280 + 160 * ct, colB = ty == T_Q ? 320 * ct + 160 : 2560 + 160 * ct;
#pragma unroll
    for (int j = 0; j < 5; ++j) {
      const int c = 64 * j + l, rr = (c * 3277) >> 16;
      int cc = c - 20 * rr;
      if (ty == T_KV) cc ^= (rr >> 2) & 2;
      const unsigned a = (16 * lw + rr) * 3840 + (cc < 10 ? colA + cc * 16 : colB + (cc - 10) * 16);
      v[j] = tb ? (g0 ^ ((j & 2) << 6)) + (j & 3) * 4 * 3072 : a;
    }
    v[5] = tb ? v[1] : xb - 4 * 8 * 1280;
    v[6] = tb ? v[2] : xa - 2 * 8 * 1280;
    v[7] = tb ? v[3] : xb - 2 * 8 * 1280;
  }
  const int d0 = tb ? 20480 + 4096 * lw : 32768 + 5120 * lw, d4 = tb ? d0 + 16384 : d0 + 4096;
  const int d8 = i8 * 1024, d5 = tb ? d4 + 1024 : d8 - 3072;
  const unsigned wstep = tb ? 64 * 3072 : 64 * 3840, mstep = tb ? wstep : 128;
#define PACE() do {} while (0)
  auto issue = [&](int st, int so, bool paced) {
    ldsp sb = lds + so;
#pragma unroll
    for (int j = 0; j < 4; ++j) { dma16(r0, sb + d0 + j * 1024, v[j], st * wstep); if (paced) PACE(); }
    dma16(r1, sb + d4, v[4], st * wstep); if (paced) PACE();
#pragma unroll
    for (int j = 0; j < 3; ++j) { dma16(r2, sb + d5 + j * 1024, v[5 + j], st * mstep); if (paced) PACE(); }
#pragma unroll
    for (int j = 0; j < 5; ++j) { dma16(rx, sb + d8 + j * 1024, (j & 1) ? xb : xa, st * 128 + (j >> 1) * 16 * 1280); if (paced && j < 4) PACE(); }
  };
  FST(0);
  auto rot = [&](int j) { const int t = k0 + j; return t >= NST1 ? t - NST1 : t; };
  issue(k0, 0, false);
  FST(1);
  issue(rot(1), SLOT, false);
  FST(2);
  WAIT_VM(13);
  FST(3);
  if (tb) {
#pragma unroll
    for (int j = -1; j < NST1 - 3; ++j) {
      BARRIER();
      issue(rot(j + 3), ((j + 3) % 3) * SLOT, true);
#pragma unroll
      for (int i = 4 * (j + 1); i < min(4 * (j + 2), 30); ++i) pre[i] = pre_load(src, i);
      SCHED_FENCE();
      if (j == -1) WAIT_VM(17); else if (j < NST1 - 4) WAIT_VM(21); else WAIT_VM(19);
    }
    BARRIER();
    gap();
    WAIT_VM(2);
  } else {
    int so = 2 * SLOT;
#pragma unroll 1
    for (int j = -1; j < NST1 - 3; ++j) {
      BARRIER();
      issue(rot(j + 3), so, true);
      so = so == 2 * SLOT ? 0 : so + SLOT;

      WAIT_VM(13);

    }
    BARRIER();
    gap();
    WAIT_VM(0);
  }
  BARRIER();
}

struct Warm {
  unsigned sink;
  __device__ __forceinline__ Warm(ldsp lds) : sink((unsigned)(size_t)(lds + M_DUMMY)) {}
  __device__ __forceinline__ void touch(const GAS char* t) {
    unsigned keep;
    asm volatile("s_mov_b32 %0, m0\n\ts_mov_b32 m0, %2\n\ts_nop 0\n\tglobal_load_lds_dword %1, off\n\ts_mov_b32 m0, %0" : "=&s"(keep) : "v"(t), "s"(sink) : "memory");
  }
};

struct FragQ { bf16x8 w[5], x[2]; };
__device__ __forceinline__ void p1_main_q(ldsp lds, int w, f32x16 (&acc)[5][2]) {
  const int l = threadIdx.x & 63, l31 = l & 31, h = l >> 5;
  const int xa = (64 * w + l31) * 128 + ((h ^ ((l31 >> 1) & 7)) << 4);
  const int wa = 32768 + (8 * h + ((l & 15) >> 2)) * 320 + ((l >> 4) & 1) * 32 + (l & 3) * 8;
  auto load = [&](FragQ& f, ldsp sb, int ks) {
    ldsp xp = sb + (xa ^ (ks << 5));
    ldsp wp = sb + wa + ks * 16 * 320;
    f.x[0] = lds_b128(xp);
    f.w[0] = lds_tr2(wp, wp + 1280);
    f.x[1] = lds_b128(xp + 4096);
#pragma unroll
    for (int n = 1; n < 5; ++n) f.w[n] = lds_tr2(wp + n * 64, wp + n * 64 + 1280);
  };
  auto mma = [&](const FragQ& f) {
#pragma unroll
    for (int n = 0; n < 5; ++n)
#pragma unroll
      for (int s = 0; s < 2; ++s) acc[n][s] = mfma32(f.w[n], f.x[s], acc[n][s]);
#pragma unroll
    for (int i = 0; i < 6; ++i) { SG_MFMA(1); SG_DS(2); }
    SG_MFMA(4);
    SCHED_FENCE();
  };
  FST(0);
  BARRIER();
  FST(1);
  FragQ f0, f1;
  int so = 0;
  load(f0, lds, 0);
  SCHED_FENCE();
#pragma unroll 1
  for (int j = 0; j < NST1; ++j) {
    ldsp sb = lds + so;
    load(f1, sb, 1); mma(f0);
    load(f0, sb, 2); mma(f1);
    load(f1, sb, 3); mma(f0);
    so = so == 2 * SLOT ? 0 : so + SLOT;
    WAIT_LGKM0();
    if (j < NST1 - 1) BARRIER();
    FSTD(2 + j);
    SCHED_FENCE();
    load(f0, lds + so, 0); mma(f1);
  }
  WAIT_LGKM0();
}

template <int TY>
__device__ __forceinline__ void p1_main16(ldsp lds, int w, f32x4 (&acc)[4][10]) {
  constexpr bool TB = TY == T_GLU;
  const int l = threadIdx.x & 63, l15 = l & 15, g = l >> 4, q = l15 >> 2;
  const int xa = ((TB ? 0 : 64 * w) + l15) * 128 + ((g ^ ((l15 >> 1) & 7)) << 4);
  int wa0, wa1;
  if (TB) {
    wa0 = 20480 + (8 * g + q) * 256 + (((2 * w) ^ (q + 4 * (g & 1))) << 5) + (l & 3) * 8;
    wa1 = wa0 ^ 32;
  } else {
    wa0 = 32768 + (8 * g + q) * 320 + (l & 3) * 8 + 32 * (g & 1);
    wa1 = wa0 - 64 * (g & 1);
  }
  auto ldx = [&](ldsp sb, int u, int t) { return lds_b128(sb + (xa ^ (u << 6)) + t * 2048); };
  auto ldw = [&](ldsp sb, int u, int t) {
    ldsp a = sb + ((t & 1) ? wa1 : wa0) + (TB ? u * 8192 + (t >> 1) * 16384 : u * 10240 + t * 32);
    return lds_tr2(a, a + (TB ? 1024 : 1280));
  };
  auto lda = [&](ldsp sb, int u, int i) { return TB ? ldw(sb, u, i) : ldx(sb, u, i); };
  auto ldb = [&](ldsp sb, int u, int j) { return TB ? ldx(sb, u, j) : ldw(sb, u, j); };
  auto mma = [&](const bf16x8 (&a)[4], const bf16x8& b, int j) {
#pragma unroll
    for (int i = 0; i < 4; ++i) acc[i][j] = mfma16(a[i], b, acc[i][j]);
    SG_MFMA(1); SG_DS(2); SG_MFMA(1); SG_DS(2); SG_MFMA(1); SG_DS(2); SG_MFMA(1);
    SCHED_FENCE();
  };
  FST(0);
  BARRIER();
  FST(1);
  bf16x8 a0[4], a1[4], b0[10], b1[10];
  int so = 0;
#pragma unroll
  for (int i = 0; i < 4; ++i) a0[i] = lda(lds, 0, i);
#pragma unroll
  for (int j = 0; j < 5; ++j) b0[j] = ldb(lds, 0, j);
  SCHED_FENCE();
#pragma unroll 1
  for (int st = 0; st < NST1; ++st) {
    ldsp sb = lds + so;
#pragma unroll
    for (int j = 0; j < 10; ++j) {
      if (j < 5) b0[j + 5] = ldb(sb, 0, j + 5); else b1[j - 5] = ldb(sb, 1, j - 5);
      if (j >= 2 && j < 6) a1[j - 2] = lda(sb, 1, j - 2);
      mma(a0, b0[j], j);
    }
#pragma unroll
    for (int j = 0; j < 6; ++j) {
      if (j < 5) b1[j + 5] = ldb(sb, 1, j + 5);
      mma(a1, b1[j], j);
    }
    so = so == 2 * SLOT ? 0 : so + SLOT;
    sb = lds + so;
    WAIT_LGKM0();
    if (st < NST1 - 1) BARRIER(); else lds_arrive(lds + M_END);
    FSTD(2 + st);
    SCHED_FENCE();
    a0[0] = lda(sb, 0, 0); b0[0] = ldb(sb, 0, 0); a0[1] = lda(sb, 0, 1); mma(a1, b1[6], 6);
    a0[2] = lda(sb, 0, 2); a0[3] = lda(sb, 0, 3); b0[1] = ldb(sb, 0, 1); mma(a1, b1[7], 7);
    b0[2] = ldb(sb, 0, 2); b0[3] = ldb(sb, 0, 3); mma(a1, b1[8], 8);
    b0[4] = ldb(sb, 0, 4); mma(a1, b1[9], 9);
  }
  WAIT_LGKM0();
}

__device__ __forceinline__ void p2_main(const Params& p, ldsp lds, int w, int rt, int ct, u64 tag, const u32x4 (&held)[15]) {
  constexpr int WL = P2_WL, X1 = 5 - WL, X2 = WL == 2 ? 6 : 8, X3 = P2_X3, AW = P2_AW, NR = 17, RH = 12, GATE = 10;
  const int l = threadIdx.x & 63, l15 = l & 15, kg = l >> 4, q = l15 >> 2;
  const int sg = w & 1, kq = w >> 1, hf = w >> 2;
  const int rlo = rt * 96, rhi = min(rlo + 95, S - 1);
  const rsrc_t ra = mk_rsrc(p.ws, A2_BYTES);
  const unsigned av = (unsigned)(((rt * 6 + sg * 3) * KB_ALL + kq) * 1024 + l * 16);
  const int rh = 4 * kg + q, ro = 16 * (kg >> 1) + 4 * (kg & 1) + q;
  const int wh = rh * 128 + (((rh >> 1) & 3) << 5) + (l & 3) * 8;
  const int wo = ro * 128 + (((ro >> 1) & 3) << 5) + (l & 3) * 8;
  ldsp hdst = lds + ((w & 3) * 1024 + hf * 4096 + l * 16);
  f32x4 acc[4][3];
#pragma unroll
  for (int n = 0; n < 4; ++n)
#pragma unroll
    for (int s = 0; s < 3; ++s) acc[n][s] = f32x4{};
  u32x4 a[NR][3];
  bf16x8 wf[WL + 1][4];
  float gi[3];
  auto lda = [&](int t) {
#pragma unroll
    for (int s = 0; s < 3; ++s) {
      a[t][s] = __builtin_amdgcn_raw_buffer_load_b128(ra, av + s * KB_ALL * 1024, t * 4096, A2AUX);
    }
  };
  ldsp wb[4];
  auto ldw = [&](int t) {
    const int o0 = w2_off(4 * t), r0 = o0 & ~0x7FFF;
    auto straight = [](int u) { return w2_off(4 * u + 3) == w2_off(4 * u) + 3 * 4096; };
    if (t == 0 || t == RH || r0 != (w2_off(4 * t - 4) & ~0x7FFF) || !straight(t) || !straight(t - 1)) {
      int rel = kq * 4096;
      if (!straight(t)) rel = kq == 0 ? 0 : kq == 1 ? w2_off(4 * t + 1) - o0 : kq == 2 ? w2_off(4 * t + 2) - o0 : w2_off(4 * t + 3) - o0;
#pragma unroll
      for (int n = 0; n < 4; ++n) {
        wb[n] = lds + (((t < RH ? wh : wo) ^ ((n ^ kq) << 5)) + r0 + rel);
        asm volatile("" : "+v"(wb[n]));
      }
    }
#pragma unroll
    for (int n = 0; n < 4; ++n) wf[t % (WL + 1)][n] = lds_tr2(wb[n] + (o0 - r0), wb[n] + (o0 - r0) + (t < RH ? 2048 : 1024));
  };
  if (w == 4) poll(flag_ptr(p, FL_H + ((l & 16) ? rhi : rlo) / 160 * 12 + min(l15, 11)), tag);
  BARRIER();
  STAMP(4);
  auto prio = [](int v) {
    if (v >= 3) __builtin_amdgcn_s_setprio(3); else if (v == 2) __builtin_amdgcn_s_setprio(2);
    else if (v == 1) __builtin_amdgcn_s_setprio(1); else __builtin_amdgcn_s_setprio(0);
  };
#pragma unroll
  for (int t = 0; t < AW; ++t) { prio(3 - t); lda(t); SCHED_FENCE(); }
  ldw(0);
  if (WL == 2) ldw(1);
  SCHED_FENCE();
#pragma unroll
  for (int t = 0; t < NR; ++t) {
    if (t == X1 || t == X2 || t == GATE || t == X3) {
      if (t == GATE && w == 7) poll(flag_ptr(p, (l & 8) ? FL_G + (((l & 1) ? rhi : rlo) >> 9) : FL_O + (((l & 4) ? rhi : rlo) >> 8) * 4 + (l & 3)), tag);
      WAIT_LGKM0();
      if (t == X1) FST(12); else if (t == X2) FST(14); else if (t == GATE) FST(16);
      BARRIER();
      if (t == X1) FST(13); else if (t == X2) FST(15); else if (t == GATE) FST(17);
      SCHED_FENCE();
      if (t == GATE) {
        const rsrc_t rg = mk_rsrc(p.ws + G_OFF, G_BYTES);
#pragma unroll
        for (int s = 0; s < 3; ++s) gi[s] = __builtin_bit_cast(float, __builtin_amdgcn_raw_buffer_load_b32(rg, (rt * 96 + sg * 48 + s * 16 + l15) * 4, 0, SC1));
        SCHED_FENCE();
#pragma unroll
        for (int u = RH; u < NR; ++u) { prio(3 - (u - RH)); lda(u); SCHED_FENCE(); }
      }
      SCHED_FENCE();
    }
    prio(t < 4 ? 3 - t : t < 8 ? 7 - t : t < GATE ? GATE - 1 - t : t < 12 ? 3 : (18 - t) / 2);
    if (t + AW < RH) lda(t + AW);
    auto hcut = [](int u) {
      if (WL == 2) return u <= 3 ? 0 : u == 4 ? 3 : u == 5 ? 6 : u == 6 ? 8 : u == 7 ? 10 : u == 8 ? 12 : u == 9 ? 14 : 15;
      if (X3 == 12) return u <= 4 ? 0 : u <= 11 ? 2 * (u - 4) : 15;
      return u <= 4 ? 0 : u <= 8 ? 2 * (u - 4) : u == 9 ? 12 : 15;
    };
    const int h0 = hcut(t), h1 = hcut(t + 1);
#pragma unroll
    for (int i = h0; i < h1; ++i) *(LAS u32x4*)(hdst + w2_off(39 + 2 * i)) = held[i];
    if (t + WL < NR) ldw(t + WL);
    if (t == RH) {
#pragma unroll
      for (int n = 0; n < 4; ++n)
#pragma unroll
        for (int s = 0; s < 3; ++s) acc[n][s] *= 1.f - gi[s];
    }
#pragma unroll
    for (int s = 0; s < 3; ++s)
#pragma unroll
      for (int n = 0; n < 4; ++n) {
        acc[n][s] = mfma16(wf[t % (WL + 1)][n], __builtin_bit_cast(bf16x8, a[t][s]), acc[n][s]);
      }
    SG_VM(3);
    SG_MFMA(1); SG_DS(2); SG_MFMA(1); SG_DS(2); SG_MFMA(1); SG_DS(2); SG_MFMA(1); SG_DS(2);
    SG_MFMA(2); SG_DW(1); SG_MFMA(2); SG_DW(1); SG_MFMA(2); SG_DW(1); SG_MFMA(1); SG_DW(1); SG_MFMA(1);
    asm volatile("" : "+v"(acc[0][0]), "+v"(acc[1][0]), "+v"(acc[2][0]), "+v"(acc[3][0]), "+v"(acc[0][1]), "+v"(acc[1][1]), "+v"(acc[2][1]), "+v"(acc[3][1]),
                 "+v"(acc[0][2]), "+v"(acc[1][2]), "+v"(acc[2][2]), "+v"(acc[3][2]));
    SCHED_FENCE();
  }
  STAMP(5);
  BARRIER();
  {
    ldsp wp = lds + (w * 9 * 1024 + l * 16);
#pragma unroll
    for (int d = 1; d < 4; ++d)
#pragma unroll
      for (int s = 0; s < 3; ++s) {
        *(LAS f32x4*)(wp + ((d - 1) * 3 + s) * 1024) = acc[d][s];
      }
  }
  WAIT_LGKM0();
  BARRIER();
  const rsrc_t rout = mk_rsrc(p.out, S * D * 2);
  const unsigned vo = (unsigned)((rt * 96 + sg * 48 + l15) * 1280 + (ct * 64 + kq * 16 + 4 * kg) * 2);
#pragma unroll
  for (int s = 0; s < 3; ++s) {
    f32x4 part[4];
#pragma unroll
    for (int d = 1; d < 4; ++d) part[d] = *(LAS f32x4*)(lds + ((w ^ (2 * d)) * 9 + (d - 1) * 3 + s) * 1024 + l * 16);
    const f32x4 v = (acc[0][s] + part[1]) + (part[2] + part[3]);
    __builtin_amdgcn_raw_buffer_store_b64(u32x2{pk2(v[0], v[1]), pk2(v[2], v[3])}, rout, vo + s * 16 * 1280, 0, OUT_AUX);
  }
}

__global__ __launch_bounds__(512, 2) void fused(Params p) {
  extern __shared__ __attribute__((aligned(16))) char smem[];
  ldsp lds = (ldsp)smem;
  asm volatile("" ::"s"(p.x), "s"(p.wqkv), "s"(p.wg), "s"(p.wu));
  u64 pc;
  asm volatile("s_getpc_b64 %0" : "=s"(pc));
  constexpr unsigned rest = CODE_BYTES;
  const unsigned b = blockIdx.x, xcd = b & 7, idx = b >> 3, cg = xcd & 3, rg = xcd >> 2;
  const int w = __builtin_amdgcn_readfirstlane(threadIdx.x >> 6), lw = w - 4;
  const int l = threadIdx.x & 63;
  FINIT();
  STAMP(0);
  const bool ta = idx < 12;
  const unsigned jj = ta ? idx : idx - 12 + 20 * rg, j3 = (jj * 43) >> 7, jr = jj - 3 * j3;
  const bool spare = jj == 39;
  const int ty = ta ? (jr == 0 ? T_Q : T_KV) : T_GLU;
  const int rt = ta ? 4 * rg + j3 : j3;
  const int ct = ta ? (jr == 0 ? cg : 2 * cg + jr - 1) : 3 * cg + jr;
  const unsigned t2 = (xcd < 4 ? 28 * xcd : 4 + 27 * xcd) + (ta ? 2 * j3 + jr - 1 : idx - 4);
  const int rt2 = (t2 * 205) >> 11, ct2 = t2 - 10 * rt2;
  auto get_tag = [&]() {
    u64 e;
    asm volatile("s_load_dwordx2 %0, %1, 0x0\n\ts_waitcnt lgkmcnt(0)" : "=s"(e) : "s"(p.ws + EPOCH_OFF) : "memory");
    return make_tag(e);
  };

  constexpr int PQX = 2 * SLOT;
  const bool loader = w >= 4 && !spare;
  Warm wm(lds);
  u64 tag;
  auto mfma_start = [&]() {
    if (idx < 2) {
      const unsigned o = (idx * 256 + w * 64 + l) * 128;
      wm.touch((const GAS char*)pc + (o < rest ? o : 0u));
    }
    if (threadIdx.x == 0) *(LAS u64*)(lds + M_CNT) = 0ull;
    tag = get_tag();
  };
  const W2Src src(p, w & 3, ct2);
  if (__builtin_expect(ty == T_Q, 0)) {
    bf16x8 pq[10];
    if (loader) {
      u32x4 none[30];
      p1_loader<true>(p, lds, lw, T_Q, rt, ct, ROT, src, none, [&]() {
        tag = get_tag();
        for (int i = lw * 64 + l; i < 2 * KVT_SZ / 16; i += 256)
          asm volatile("ds_write_b128 %0, %1" ::"v"((unsigned)(size_t)(lds + KVT) + i * 16), "v"(u32x4{0u, 0u, 0u, 0u}) : "memory");
        if (lw == 0) poll(flag_ptr(p, FL_KV + (l & 7) * 8 + 2 * ct + ((l >> 3) & 1)), tag);
        WAIT_LGKM0();
      });
      poll(flag_ptr(p, FL_G + (rt >> 1)), tag);
      const int tl = lw * 64 + l;
      *(LAS unsigned*)(lds + M_G + tl * 4) = __builtin_amdgcn_raw_buffer_load_b32(mk_rsrc(p.ws + G_OFF, G_BYTES), (rt * 256 + tl) * 4, 0, SC1);
    } else {
    mfma_start();
    f32x16 acc[5][2];
#pragma unroll
    for (int n = 0; n < 5; ++n) { acc[n][0] = f32x16{}; acc[n][1] = f32x16{}; }
    p1_main_q(lds, w, acc);
    STAMP(1);
    const Consts cn(1.f);
#pragma unroll
    for (int s = 1; s >= 0; --s)
#pragma unroll
      for (int n = 0; n < 5; ++n) {
        f32x16 t;
#pragma unroll
        for (int r = 0; r < 16; r += 4) {
          const f32x4 y = phi4(f32x4{acc[n][s][r], acc[n][s][r + 1], acc[n][s][r + 2], acc[n][s][r + 3]}, cn);
          t[r] = y[0]; t[r + 1] = y[1]; t[r + 2] = y[2]; t[r + 3] = y[3];
        }
        pq[2 * n] = pack8(t, 0); pq[2 * n + 1] = pack8(t, 8);
        if (s) {
          *(LAS bf16x8*)(lds + PQX + (w * 10 + 2 * n) * 1024 + l * 16) = pq[2 * n];
          *(LAS bf16x8*)(lds + PQX + (w * 10 + 2 * n + 1) * 1024 + l * 16) = pq[2 * n + 1];
        } else {
          asm volatile("" : "+v"(pq[2 * n]), "+v"(pq[2 * n + 1]));
        }
      }
    STAMP(2);
    }
    {
      const int ch = min((int)threadIdx.x, 419), hg = ch >= 210;
      ldsp dp[2];
      bool dup[2];
#pragma unroll
      for (int u = 0; u < 2; ++u) {
        const int un = 2 * (ch - 210 * hg) + u, row = un / 5, dq = un % 5, hd = row < 80 ? row / 20 : row - 80, run = 5 * hd + dq;
        dp[u] = lds + KVT + hg * KVT_SZ + row * KS + (run >> 2) * 32 + (run & 1) * 16 + (run & 2) * 4;
        dup[u] = row >= 80;
      }
      const rsrc_t rk = mk_rsrc(p.ws + KVP_OFF, KVP_BYTES);
      u32x4 pv[8];
#pragma unroll 1
      for (int spin = 0; spin < SPIN_MAX; ++spin) {
#pragma unroll
        for (int r = 0; r < 8; ++r) pv[r] = __builtin_amdgcn_raw_buffer_load_b128(rk, 2 * ct * 3360 + ch * 16, r * 8 * 3360, SC1);
        unsigned m = 0u;
#pragma unroll
        for (int r = 0; r < 8; ++r) m = max(max(m, max(pv[r][0], pv[r][1])), max(pv[r][2], pv[r][3]));
        if (__all(m != UNSET)) break;
        asm volatile("s_sleep 2" ::: "memory");
      }
      STAMP(3);
      f32x2 sum[4];
#pragma unroll
      for (int e = 0; e < 4; ++e) sum[e] = f32x2{0.f, 0.f};
#pragma unroll
      for (int r = 0; r < 8; ++r)
#pragma unroll
        for (int e = 0; e < 4; ++e) {
          const unsigned x = pv[r][e];
          sum[e] += f32x2{__uint_as_float(x << 16), __uint_as_float(x & 0xFFFF0000u)};
        }
#pragma unroll
      for (int u = 0; u < 2; ++u) {
        const u32x2 v = {pk2(sum[2 * u][0], sum[2 * u][1]), pk2(sum[2 * u + 1][0], sum[2 * u + 1][1])};
        *(LAS u32x2*)dp[u] = v;
        if (dup[u]) *(LAS u32x2*)(dp[u] + 4 * KS) = v;
      }
      WAIT_LGKM0();
      BARRIER();
      STAMP(4);
      if (loader) {
#pragma unroll
        for (int t = 0; t < 10; ++t) pq[t] = lds_b128(lds + PQX + (lw * 10 + t) * 1024 + l * 16);
      }
    }
  {
    const int l31 = l & 31, h = l >> 5, s32 = 2 * (w & 3) + (w >> 2);
    const rsrc_t ra = mk_rsrc(p.ws, A2_BYTES);
    const unsigned vo = (unsigned)(((rt * 16 + s32 * 2 + ((l >> 4) & 1)) * KB_ALL + 48 + 5 * ct) * 1024 + h * 256 + (l & 15) * 16);
    const float gv = *(LAS float*)(lds + M_G + (32 * s32 + l31) * 4);
    bf16x8 af[2][3][5];
#pragma unroll
    for (int hg = 0; hg < 2; ++hg)
#pragma unroll
      for (int et = 2; et >= 0; --et)
#pragma unroll
        for (int t = 0; t < 5; ++t)
          if (!((et == 0 && t > 2) || (et == 1 && t == 0))) af[hg][et][t] = lds_b128(lds + KVT + hg * KVT_SZ + (32 * et + l31) * KS + t * 32 + h * 16);
    FST(18);
    SCHED_FENCE();
#pragma unroll
    for (int hg = 0; hg < 2; ++hg) {
      f32x16 acc[3];
#pragma unroll
      for (int et = 0; et < 3; ++et) acc[et] = f32x16{};
#pragma unroll
      for (int et = 2; et >= 0; --et)
#pragma unroll
        for (int t = 0; t < 5; ++t)
          if (!((et == 0 && t > 2) || (et == 1 && t == 0))) acc[et] = mfma32(af[hg][et][t], pq[5 * hg + t], acc[et]);
      if (hg == 0) FST(19); else FST(20);
      f32x4 sc;
#pragma unroll
      for (int hd = 0; hd < 4; ++hd) sc[hd] = gv * __builtin_amdgcn_rcpf(acc[2][8 + hd] + 1e-6f);
#pragma unroll
      for (int pc_ = 4; pc_ >= 0; --pc_) {
        f32x16 t;
#pragma unroll
        for (int gq = 0; gq < 2; ++gq) {
          const int e0 = 16 * pc_ + 8 * gq;
          float fa = sc[e0 / 20], fb = sc[(e0 + 4) / 20];
          if (e0 / 20 != (e0 + 4) / 20) { asm volatile("" : "+v"(fa), "+v"(fb)); fa = h ? fb : fa; }
#pragma unroll
          for (int r = 0; r < 4; ++r) t[4 * gq + r] = acc[pc_ >> 1][8 * (pc_ & 1) + 4 * gq + r] * fa;
        }
        const int k16 = 5 * hg + pc_;
        st128_sc1(ra, vo + (k16 >> 1) * 1024 + (k16 & 1) * 512, __builtin_bit_cast(u32x4, pack8(t, 0)));
      }
    }
    STAMP(5);
    publish(p, lds, FL_O + rt * 4 + ct, tag, 8);
    STAMP(6);
    FDUMP();
    return;
  }
  }
  u32x4 held[15];
  if (__builtin_expect(loader, 1)) {
    u32x4 pre[30];
    p1_loader<false>(p, lds, lw, ty, rt, ct, ROT, src, pre, [&]() { tag = get_tag(); });
    src.fill<0, 24>(lds + lw * 1024);
    if (ty == T_KV) {
#pragma unroll
      for (int i = 0; i < 30; ++i) pre[i] = pre_load(src, i);
      SCHED_FENCE();
      BARRIER(); BARRIER();
    } else {
      lds_wait(lds + M_END, 4);
    }
    WAIT_VM(0);
#pragma unroll
    for (int i = 0; i < 15; ++i) *(LAS u32x4*)(lds + (lw * 1024 + l * 16) + w2_off(24 + i)) = pre[i];
#pragma unroll
    for (int i = 0; i < 15; ++i) held[i] = pre[15 + i];
    WAIT_LGKM0();
  } else {
    mfma_start();
    if (ty == T_KV && w == 0) {
      const rsrc_t rk = mk_rsrc(p.ws + KVP_OFF + (rt * 8 + ct) * 3360, 3360);
#pragma unroll
      for (int i = 0; i < 4; ++i) st128_sc1(rk, i * 1024 + l * 16, u32x4{UNSET, UNSET, UNSET, UNSET});
      WAIT_VM(0);
      if (l == 0) __hip_atomic_store(flag_ptr(p, FL_KV + rt * 8 + ct), tag, __ATOMIC_RELAXED, __HIP_MEMORY_SCOPE_AGENT);
    }
    if (spare) {
      BARRIER();
      const int l15 = l & 15, r0 = cg * 512 + w * 64 + (l >> 4);
      const rsrc_t rx = mk_rsrc(p.x, S * D * 2), rgw = mk_rsrc(p.gw, D * 2), rg_ = mk_rsrc(p.ws + G_OFF, G_BYTES);
      u32x4 gw[5], v[2][4][5];
#pragma unroll
      for (int c = 0; c < 5; ++c) gw[c] = __builtin_amdgcn_raw_buffer_load_b128(rgw, l15 * 16 + c * 256, 0, 0);
      auto ld = [&](int it) {
#pragma unroll
        for (int u = 0; u < 4; ++u)
#pragma unroll
          for (int c = 0; c < 5; ++c) v[it & 1][u][c] = __builtin_amdgcn_raw_buffer_load_b128(rx, (r0 + 4 * u) * 1280 + l15 * 16 + c * 256, it * 16 * 1280, 0);
      };
      ld(0);
#pragma unroll
      for (int it = 0; it < 4; ++it) {
        if (it < 3) ld(it + 1);
        SCHED_FENCE();
        float keep = 0.f;
#pragma unroll
        for (int u = 0; u < 4; ++u) {
          float sacc = 0.f;
#pragma unroll
          for (int c = 0; c < 5; ++c)
#pragma unroll
            for (int e = 0; e < 4; ++e) {
              const unsigned xw = v[it & 1][u][c][e], ge = gw[c][e];
              sacc = __builtin_amdgcn_fdot2_f32_bf16(__builtin_bit_cast(bf16x2, xw), __builtin_bit_cast(bf16x2, ge), sacc, false);
            }
          sacc += __shfl_xor(sacc, 1); sacc += __shfl_xor(sacc, 2); sacc += __shfl_xor(sacc, 4); sacc += __shfl_xor(sacc, 8);
          if (l15 == u) keep = sacc;
        }
        if (l15 < 4) __builtin_amdgcn_raw_buffer_store_b32(__builtin_bit_cast(unsigned, sigmoidf(keep)), rg_, (r0 + 16 * it + 4 * l15) * 4, 0, SC1);
        SCHED_FENCE();
      }
      publish(p, lds, FL_G + cg, tag, 8);
      STAMP(6);
      FDUMP();
      if (cg == 0 && w == 0) {
#pragma unroll 1
        for (int i = 0; i < 4; ++i) poll(flag_ptr(p, i * 64 + l), tag);
        if (l == 0) __hip_atomic_store((GAS u64*)(p.ws + EPOCH_OFF), tag, __ATOMIC_RELAXED, __HIP_MEMORY_SCOPE_AGENT);
      }
      return;
    }
      f32x4 acc[4][10];
#pragma unroll
      for (int i = 0; i < 4; ++i)
#pragma unroll
        for (int j = 0; j < 10; ++j) acc[i][j] = f32x4{};
      if (ty == T_GLU) {
        p1_main16<T_GLU>(lds, w, acc);
        STAMP(1);
        const Consts cn(-1.f);
        const rsrc_t ra = mk_rsrc(p.ws, A2_BYTES);
        const unsigned vo = (unsigned)((rt * 10 * KB_ALL + ct * 4 + w) * 1024 + l * 16);
#pragma unroll
        for (int j = 0; j < 10; ++j) {
          st128_sc1(ra, vo + j * KB_ALL * 1024, __builtin_bit_cast(u32x4, pack44(glu4(acc[0][j], acc[2][j], cn), glu4(acc[1][j], acc[3][j], cn))));
          SCHED_FENCE();
        }
        STAMP(2);
        publish(p, lds, FL_H + rt * 12 + ct, tag, 4);
      } else {
        int goff[2], gstr[2];
#pragma unroll
        for (int i = 0; i < 2; ++i) {
          const int en = min((int)threadIdx.x + 256 * i, 419), row = en / 5, dq = en % 5;
          const int hd = row < 80 ? row / 20 : row - 80, d0 = 20 * hd + 4 * dq, ti = d0 >> 4;
          goff[i] = row < 80 ? (2 * ti + (row >> 4)) * 1024 + (((d0 & 15) >> 2) * 16 + (row & 15)) * 16 : M_Z + d0 * 4;
          gstr[i] = row < 80 ? 13312 : 320;
          asm volatile("" : "+v"(goff[i]), "+v"(gstr[i]));
        }
        p1_main16<T_KV>(lds, w, acc);
        STAMP(1);
        const Consts cn(1.f);
        lds_wait(lds + M_END, 4);
        FST(18);
        f32x4 kv[13], z[5];
#pragma unroll
        for (int i = 0; i < 13; ++i) kv[i] = f32x4{};
#pragma unroll
        for (int i = 0; i < 5; ++i) z[i] = f32x4{};
        const bf16x8 ones = __builtin_bit_cast(bf16x8, u32x4{0x3F803F80u, 0x3F803F80u, 0x3F803F80u, 0x3F803F80u});
        bf16x8 vf[2][5];
#pragma unroll
        for (int u = 0; u < 2; ++u)
#pragma unroll
          for (int j = 0; j < 5; ++j) vf[u][j] = pack44(acc[2 * u][5 + j], acc[2 * u + 1][5 + j]);
#pragma unroll
        for (int i = 0; i < 5; ++i) {
#pragma unroll
          for (int u = 0; u < 2; ++u) {
            const bf16x8 kf = pack44(phi4(acc[2 * u][i], cn), phi4(acc[2 * u + 1][i], cn));
            if (i > 0) kv[3 * i - 1] = mfma16(kf, vf[u][i - 1], kv[3 * i - 1]);
            kv[3 * i] = mfma16(kf, vf[u][i], kv[3 * i]);
            if (i < 4) kv[3 * i + 1] = mfma16(kf, vf[u][i + 1], kv[3 * i + 1]);
            z[i] = mfma16(kf, ones, z[i]);
          }
          if (i > 0) {
#pragma unroll
            for (int t = max(3 * i - 4, 0); t < 3 * i - 1; ++t) *(LAS f32x4*)(lds + w * 13312 + t * 1024 + l * 16) = kv[t];
          }
        }
#pragma unroll
        for (int t = 11; t < 13; ++t) *(LAS f32x4*)(lds + w * 13312 + t * 1024 + l * 16) = kv[t];
        FST(19);
        if ((l & 15) == 0) {
#pragma unroll
          for (int i = 0; i < 5; ++i) *(LAS f32x4*)(lds + M_Z + w * 320 + i * 64 + (l >> 4) * 16) = z[i];
        }
        WAIT_LGKM0();
        FST(20);
        BARRIER();
        FST(21);
        const rsrc_t rk = mk_rsrc(p.ws + KVP_OFF + (rt * 8 + ct) * 3360, 3360);
#pragma unroll
        for (int i = 0; i < 2; ++i) {
          f32x4 sum = *(LAS f32x4*)(lds + goff[i]);
#pragma unroll
          for (int ww = 1; ww < 4; ++ww) sum += *(LAS f32x4*)(lds + goff[i] + ww * gstr[i]);
          __builtin_amdgcn_raw_buffer_store_b64(u32x2{min(pk2(sum[0], sum[1]), UNSET - 1), min(pk2(sum[2], sum[3]), UNSET - 1)}, rk, threadIdx.x * 8 + i * 2048, 0, SC1);
        }
        WAIT_LGKM0();
        FST(22);
        BARRIER();
        STAMP(2);
      }
      STAMP(3);
#pragma unroll
      for (int i = 0; i < 15; ++i) held[i] = src.load(39 + 2 * i, 0);
      SCHED_FENCE();
  }
  p2_main(p, lds, w, rt2, ct2, tag, held);
  STAMP(6);
  FDUMP();
}

#define STAMP_PTR nullptr
extern "C" void kernel_launch(void* const* d_in, const int* in_sizes, int n_in, void* d_out, int out_size, void* d_ws, size_t ws_size,
                              hipStream_t stream) {
  Params p;
  p.x = (const u16*)d_in[0]; p.wqkv = (const u16*)d_in[1]; p.wo = (const u16*)d_in[2];
  p.wg = (const u16*)d_in[4]; p.wu = (const u16*)d_in[5]; p.wd = (const u16*)d_in[6]; p.gw = (const u16*)d_in[7];
  p.out = (u16*)d_out; p.ws = (char*)d_ws; p.stamps = STAMP_PTR;
  (void)hipFuncSetAttribute((const void*)fused, hipFuncAttributeMaxDynamicSharedMemorySize, LDS_BYTES);
  hipLaunchKernelGGL(fused, dim3(256), dim3(512), LDS_BYTES, stream, p);
}
```

```cpp
#include <hip/hip_runtime.h>
#include <hip/hip_bf16.h>

typedef unsigned short u16;
typedef unsigned long long u64;
typedef __bf16 bf16x8 __attribute__((ext_vector_type(8)));
typedef __bf16 bf16x4 __attribute__((ext_vector_type(4)));
typedef __bf16 bf16x2 __attribute__((ext_vector_type(2)));
typedef float f32x16 __attribute__((ext_vector_type(16)));
typedef float f32x4 __attribute__((ext_vector_type(4)));
typedef unsigned u32x4 __attribute__((ext_vector_type(4)));
typedef unsigned u32x2 __attribute__((ext_vector_type(2)));
#define LAS __attribute__((address_space(3)))
#define GAS __attribute__((address_space(1)))
typedef LAS char* ldsp;
typedef __amdgpu_buffer_rsrc_t rsrc_t;

constexpr int S = 2048, D = 640, F = 1536;
constexpr float LOG2E = 1.4426950408889634f;
constexpr int KB_ALL = 68;
constexpr unsigned A2_BYTES = 128u * KB_ALL * 1024;
constexpr unsigned KVP_OFF = A2_BYTES, KVP_BYTES = 8 * 8 * 3360;
constexpr unsigned G_OFF = KVP_OFF + KVP_BYTES, G_BYTES = S * 4;
constexpr unsigned FLAG_OFF = G_OFF + G_BYTES;
constexpr unsigned EPOCH_OFF = FLAG_OFF + 256 * 128;
constexpr unsigned WS_USED = EPOCH_OFF + 128;
constexpr int FL_H = 0, FL_KV = 156, FL_O = 220, FL_G = 252;
constexpr int SC1 = 16;
constexpr unsigned UNSET = 0xFFFFFFFFu;
constexpr int SLOT = 53248, MISC = 3 * SLOT, LDS_BYTES = 163840;
constexpr int M_DUMMY = MISC, M_CNT = MISC + 256  , M_END = MISC + 260  , M_RND = MISC + 320  , M_Z = MISC + 512  , M_G = MISC + 2048  ;
constexpr int KS = 176, KVT = SLOT, KVT_SZ = 96 * KS;
constexpr int NST1 = 10;
constexpr int SPIN_MAX = 1 << 16;
constexpr unsigned CODE_BYTES = 44000;
#define A2AUX SC1
#define GLU_PACE 0
#define OUT_AUX 0
#define P2_AW 3
#define P2_X3 99
#define P2_PRIO 2
#define P2_A0 1
#define P2_RAMP 2
#define P2_G0 2
#define P2_G1 1
#define P2_WL 1
#define P2_POS 0
#define ROT (2 * (cg & 1) + 5 * (((cg >> 1) ^ rg) & 1))
#define W2_ST 4

struct Params {
  const u16 *x, *wqkv, *wo, *wg, *wu, *wd, *gw;
  u16* out;
  char* ws;
  u64* stamps;
};

#define STAMP(i) do {} while (0)
#define FST(i) do {} while (0)
#define FSTD(i) do {} while (0)
#define FINIT() do {} while (0)
#define FDUMP() do {} while (0)

#define WAIT_VM(n) asm volatile("s_waitcnt vmcnt(" #n ")" ::: "memory")
#define WAIT_LGKM0() asm volatile("s_waitcnt lgkmcnt(0)" ::: "memory")
#define BARRIER() do { asm volatile("" ::: "memory"); __builtin_amdgcn_s_barrier(); asm volatile("" ::: "memory"); } while (0)
#define SCHED_FENCE() __builtin_amdgcn_sched_barrier(0)
#define SG_MFMA(n) __builtin_amdgcn_sched_group_barrier(0x8, n, 0)
#define SG_DS(n) __builtin_amdgcn_sched_group_barrier(0x100, n, 0)
#define SG_DW(n) __builtin_amdgcn_sched_group_barrier(0x200, n, 0)
#define SG_VM(n) __builtin_amdgcn_sched_group_barrier(0x20, n, 0)

__device__ __forceinline__ rsrc_t mk_rsrc(const void* p, unsigned bytes) { return __builtin_amdgcn_make_buffer_rsrc((void*)p, 0, bytes, 0x00020000); }
__device__ __forceinline__ void dma16(rsrc_t r, ldsp dst, unsigned voff, unsigned soff) { __builtin_amdgcn_raw_ptr_buffer_load_lds(r, dst, 16, voff, soff, 0, 0); }
__device__ __forceinline__ bf16x8 lds_b128(ldsp a) { return *(LAS bf16x8*)a; }
__device__ __forceinline__ bf16x8 lds_tr2(ldsp a0, ldsp a1) {
  bf16x4 lo = __builtin_amdgcn_ds_read_tr16_b64_v4bf16((LAS bf16x4*)a0);
  bf16x4 hi = __builtin_amdgcn_ds_read_tr16_b64_v4bf16((LAS bf16x4*)a1);
  return __builtin_shufflevector(lo, hi, 0, 1, 2, 3, 4, 5, 6, 7);
}
__device__ __forceinline__ f32x16 mfma32(bf16x8 a, bf16x8 b, f32x16 c) { return __builtin_amdgcn_mfma_f32_32x32x16_bf16(a, b, c, 0, 0, 0); }
__device__ __forceinline__ f32x4 mfma16(bf16x8 a, bf16x8 b, f32x4 c) { return __builtin_amdgcn_mfma_f32_16x16x32_bf16(a, b, c, 0, 0, 0); }
__device__ __forceinline__ unsigned pk2(float a, float b) {
  bf16x2 v = {(__bf16)a, (__bf16)b};
  return __builtin_bit_cast(unsigned, v);
}
__device__ __forceinline__ bf16x8 pack8(const f32x16& c, int o) {
  u32x4 r = {pk2(c[o], c[o + 1]), pk2(c[o + 2], c[o + 3]), pk2(c[o + 4], c[o + 5]), pk2(c[o + 6], c[o + 7])};
  return __builtin_bit_cast(bf16x8, r);
}
__device__ __forceinline__ bf16x8 pack44(const f32x4& a, const f32x4& b) {
  u32x4 r = {pk2(a[0], a[1]), pk2(a[2], a[3]), pk2(b[0], b[1]), pk2(b[2], b[3])};
  return __builtin_bit_cast(bf16x8, r);
}
__device__ __forceinline__ float phi(float t) {
  const float e = __builtin_amdgcn_exp2f(t * LOG2E);
  return __builtin_amdgcn_fmed3f(t, 1e-6f, __builtin_inff()) + __builtin_amdgcn_fmed3f(e, 0.f, 1.f);
}
__device__ __forceinline__ float sigmoidf(float t) { return __builtin_amdgcn_rcpf(1.f + __builtin_amdgcn_exp2f(-LOG2E * t)); }
typedef float f32x2 __attribute__((ext_vector_type(2)));
struct Consts {
  f32x2 l2e;
  float eps, big;
  __device__ __forceinline__ Consts(float sign) : l2e{sign * LOG2E, sign * LOG2E}, eps(1e-6f), big(3.4028234664e38f) {
    asm volatile("" : "+s"(l2e), "+s"(eps), "+v"(big));
  }
};
__device__ __forceinline__ f32x4 phi4(f32x4 t, const Consts& c) {
  const f32x2 a = f32x2{t[0], t[1]} * c.l2e, b = f32x2{t[2], t[3]} * c.l2e;
  f32x2 m0, m1, e0, e1;
#pragma unroll
  for (int i = 0; i < 2; ++i) {
    m0[i] = __builtin_amdgcn_fmed3f(t[i], c.eps, c.big);
    m1[i] = __builtin_amdgcn_fmed3f(t[2 + i], c.eps, c.big);
    e0[i] = __builtin_amdgcn_fmed3f(__builtin_amdgcn_exp2f(a[i]), 0.f, 1.f);
    e1[i] = __builtin_amdgcn_fmed3f(__builtin_amdgcn_exp2f(b[i]), 0.f, 1.f);
  }
  const f32x2 r0 = m0 + e0, r1 = m1 + e1;
  return f32x4{r0[0], r0[1], r1[0], r1[1]};
}
__device__ __forceinline__ f32x4 glu4(f32x4 g, f32x4 u, const Consts& c) {
  f32x2 a = f32x2{g[0], g[1]} * c.l2e, b = f32x2{g[2], g[3]} * c.l2e;
#pragma unroll
  for (int i = 0; i < 2; ++i) { a[i] = __builtin_amdgcn_exp2f(a[i]); b[i] = __builtin_amdgcn_exp2f(b[i]); }
  a += 1.f; b += 1.f;
#pragma unroll
  for (int i = 0; i < 2; ++i) { a[i] = __builtin_amdgcn_rcpf(a[i]); b[i] = __builtin_amdgcn_rcpf(b[i]); }
  a *= f32x2{u[0], u[1]}; b *= f32x2{u[2], u[3]};
  return f32x4{a[0], a[1], b[0], b[1]};
}
__device__ __forceinline__ void st128_sc1(rsrc_t r, unsigned off, u32x4 v) { __builtin_amdgcn_raw_buffer_store_b128(v, r, off, 0, SC1); }

__device__ __forceinline__ u64 make_tag(u64 e) { return ((e ^ 0x9E3779B97F4A7C15ull) * 0xBF58476D1CE4E5B9ull) | 1ull; }
__device__ __forceinline__ GAS u64* flag_ptr(const Params& p, int f) { return (GAS u64*)(p.ws + FLAG_OFF + (size_t)f * 128); }
__device__ __forceinline__ u64 ld_flag(GAS u64* f) { return __hip_atomic_load(f, __ATOMIC_RELAXED, __HIP_MEMORY_SCOPE_AGENT); }
__device__ __forceinline__ void poll(GAS u64* f, u64 tag) {
#pragma unroll 1
  for (int spin = 0; spin < SPIN_MAX; ++spin) {
    if (__all(ld_flag(f) == tag)) break;
    __builtin_amdgcn_s_sleep(2);
  }
  __builtin_amdgcn_fence(__ATOMIC_ACQUIRE, "wavefront");
}
__device__ __forceinline__ void lds_arrive(ldsp c) {
  if ((threadIdx.x & 63) == 0) __hip_atomic_fetch_add((LAS unsigned*)c, 1u, __ATOMIC_RELAXED, __HIP_MEMORY_SCOPE_WORKGROUP);
}
__device__ __forceinline__ void lds_wait(ldsp c, unsigned n) {
#pragma unroll 1
  for (int spin = 0; spin < SPIN_MAX; ++spin) {
    unsigned v;
    asm volatile("ds_read_b32 %0, %1\n\ts_waitcnt lgkmcnt(0)" : "=v"(v) : "v"((unsigned)(size_t)c) : "memory");
    if (__builtin_amdgcn_readfirstlane(v) >= n) break;
    __builtin_amdgcn_s_sleep(1);
  }
}
__device__ __forceinline__ void publish(const Params& p, ldsp lds, int f, u64 tag, unsigned n) {
  WAIT_VM(0);
  if ((threadIdx.x & 63) == 0) {
    const unsigned old = __hip_atomic_fetch_add((LAS unsigned*)(lds + M_CNT), 1u, __ATOMIC_RELAXED, __HIP_MEMORY_SCOPE_WORKGROUP);
    if (old == n - 1) __hip_atomic_store(flag_ptr(p, f), tag, __ATOMIC_RELAXED, __HIP_MEMORY_SCOPE_AGENT);
  }
}

enum { T_Q = 0, T_KV = 1, T_GLU = 2 };

__device__ __forceinline__ constexpr int w2_off(int s) { return (SLOT + (s % 39) * 4096) % (3 * SLOT); }

struct W2Src {
  rsrc_t rd, ro;
  unsigned wv, ws0;
  __device__ __forceinline__ W2Src(const Params& p, int piece, int ct) {
    const int l = threadIdx.x & 63;
    rd = mk_rsrc(p.wd, F * D * 2);
    ro = mk_rsrc(p.wo, D * D * 2);
    wv = (l >> 3) * 1280 + (((l & 7) ^ ((l >> 4) << 1)) << 4);
    ws0 = (unsigned)(8 * piece) * 1280 + ct * 128;
  }
  __device__ __forceinline__ u32x4 load(int s, int add) const {
    if (s == 47) return __builtin_amdgcn_raw_buffer_load_b128(add ? ro : rd, wv, ws0 + (add ? 0u : 47u * 32 * 1280), 0);
    return __builtin_amdgcn_raw_buffer_load_b128(s < 48 ? rd : ro, wv, ws0 + (unsigned)((s < 48 ? s : s - 48) + add) * 32 * 1280, 0);
  }
  template <int S0, int S1> __device__ __forceinline__ void fill(ldsp dst) const {
#pragma unroll
    for (int s = S0; s < S1; ++s) dma16(rd, dst + w2_off(s), wv, ws0 + (unsigned)s * 32 * 1280);
  }
};

__device__ __forceinline__ u32x4 pre_load(const W2Src& src, int i) { return i < 15 ? src.load(24 + i, 0) : src.load(40 + 2 * (i - 15), 0); }

template <bool ISQ, class Gap>
__device__ __forceinline__ void p1_loader(const Params& p, ldsp lds, int lw, int tm, int rt, int ct, int k0, const W2Src& src, u32x4 (&pre)[30], Gap gap) {
  const bool tb = !ISQ && tm != 0;
  auto sel = [&](int a, int b) { return ISQ ? b : b ^ ((a ^ b) & tm); };
  auto selp = [&](const u16* a, const u16* b) { return ISQ ? b : (const u16*)((u64)b ^ (((u64)a ^ (u64)b) & (u64)(long long)tm)); };
  const int l = threadIdx.x & 63;
  const int i8 = sel(5 * lw, 8 * lw + 3);
  const unsigned xe = (l >> 3) * 1280 + (((l & 7) ^ (l >> 4)) << 4);
  const unsigned xa = (xe ^ ((i8 & 1) << 6)) + (unsigned)(sel(160, 256) * rt + 8 * i8) * 1280, xb = (xa ^ 64) + 8 * 1280;
  const int d8 = i8 * 1024;
  const rsrc_t rx = mk_rsrc(p.x, S * D * 2);
  FST(0);
#pragma unroll
  for (int j = 0; j < 5; ++j) dma16(rx, lds + d8 + j * 1024, (j & 1) ? xb : xa, k0 * 128 + (j >> 1) * 16 * 1280);
  SCHED_FENCE();
  const rsrc_t r0 = mk_rsrc(selp(p.wg, p.wqkv), sel(D * F * 2, D * 3 * D * 2));
  const rsrc_t r1 = mk_rsrc(selp(p.wu, p.wqkv), sel(D * F * 2, D * 3 * D * 2));
  const rsrc_t r2 = mk_rsrc(selp(p.wu, p.x), sel(D * F * 2, S * D * 2));
  unsigned v[8];
  {
    const int r = l >> 4;
    const unsigned g0 = (16 * lw + r) * 3072 + ct * 256 + (((l & 15) ^ (r << 1)) << 4);
    const int colA = ISQ ? 320 * ct : 1280 + 160 * ct, colB = ISQ ? 160 : 1280;
#pragma unroll
    for (int j = 0; j < 5; ++j) {
      const int c = 64 * j + l, rr = (c * 3277) >> 16;
      int cc = c - 20 * rr;
      if (!ISQ) cc ^= (rr >> 2) & 2;
      const unsigned a = (16 * lw + rr) * 3840 + colA + cc * 16 + (cc < 10 ? 0 : colB - 160);
      v[j] = sel((g0 ^ ((j & 2) << 6)) + (j & 3) * 4 * 3072, a);
    }
    v[5] = sel(v[1], xb - 4 * 8 * 1280);
    v[6] = sel(v[2], xa - 2 * 8 * 1280);
    v[7] = sel(v[3], xb - 2 * 8 * 1280);
  }
  const int d0 = sel(20480 + 4096 * lw, 32768 + 5120 * lw), d4 = d0 + sel(16384, 4096), d5 = sel(d4 + 1024, d8 - 3072);
  const unsigned wstep = sel(64 * 3072, 64 * 3840), mstep = sel(64 * 3072, 128);
  auto issue_a = [&](int st, int so) {
    ldsp sb = lds + so;
#pragma unroll
    for (int j = 0; j < 5; ++j) dma16(rx, sb + d8 + j * 1024, (j & 1) ? xb : xa, st * 128 + (j >> 1) * 16 * 1280);
#pragma unroll
    for (int j = 0; j < 2; ++j) dma16(r2, sb + d5 + j * 1024, v[5 + j], st * mstep);
  };
  auto issue_b = [&](int st, int so) {
    ldsp sb = lds + so;
    dma16(r2, sb + d5 + 2048, v[7], st * mstep);
#pragma unroll
    for (int j = 0; j < 4; ++j) dma16(r0, sb + d0 + j * 1024, v[j], st * wstep);
    dma16(r1, sb + d4, v[4], st * wstep);
  };
  auto rot = [&](int j) { const int t = k0 + j; return t >= NST1 ? t - NST1 : t; };
#pragma unroll
  for (int j = 0; j < 2; ++j) dma16(r2, lds + d5 + j * 1024, v[5 + j], k0 * mstep);
  issue_b(k0, 0);
  FST(1);
  issue_a(rot(1), SLOT);
  FST(2);
  WAIT_VM(7);
  FST(3);
  BARRIER();
  issue_b(rot(1), SLOT); issue_a(rot(2), 2 * SLOT);
  WAIT_VM(7);
  BARRIER();
  issue_b(rot(2), 2 * SLOT); issue_a(rot(3), 0); issue_b(rot(3), 0);
  WAIT_VM(13);
  if (tb) {
#pragma unroll
    for (int j = 1; j < NST1 - 3; ++j) {
      BARRIER();
      issue_a(rot(j + 3), (j % 3) * SLOT); issue_b(rot(j + 3), (j % 3) * SLOT);
#pragma unroll
      for (int i = 5 * (j - 1); i < 5 * j; ++i) pre[i] = pre_load(src, i);
      SCHED_FENCE();
      if (j == 1) WAIT_VM(18); else WAIT_VM(23);
    }
    BARRIER();
    gap();
    WAIT_VM(5);
  } else {
    int so = SLOT;
#pragma unroll 1
    for (int j = 1; j < NST1 - 3; ++j) {
      BARRIER();
      issue_a(rot(j + 3), so); issue_b(rot(j + 3), so);
      so = so == 2 * SLOT ? 0 : so + SLOT;
      WAIT_VM(13);
    }
    BARRIER();
    gap();
    WAIT_VM(0);
  }
  BARRIER();
}

struct Warm {
  unsigned sink;
  __device__ __forceinline__ Warm(ldsp lds) : sink((unsigned)(size_t)(lds + M_DUMMY)) {}
  __device__ __forceinline__ void touch(const GAS char* t) {
    unsigned keep;
    asm volatile("s_mov_b32 %0, m0\n\ts_mov_b32 m0, %2\n\ts_nop 0\n\tglobal_load_lds_dword %1, off\n\ts_mov_b32 m0, %0" : "=&s"(keep) : "v"(t), "s"(sink) : "memory");
  }
};

struct FragQ { bf16x8 w[5], x[2]; };
__device__ __forceinline__ void p1_main_q(ldsp lds, int w, f32x16 (&acc)[5][2]) {
  const int l = threadIdx.x & 63, l31 = l & 31, h = l >> 5;
  const int xa = (64 * w + l31) * 128 + ((h ^ ((l31 >> 1) & 7)) << 4);
  const int wa = 32768 + (8 * h + ((l & 15) >> 2)) * 320 + ((l >> 4) & 1) * 32 + (l & 3) * 8;
  auto load = [&](FragQ& f, ldsp sb, int ks) {
    ldsp xp = sb + (xa ^ (ks << 5));
    ldsp wp = sb + wa + ks * 16 * 320;
    f.x[0] = lds_b128(xp);
    f.w[0] = lds_tr2(wp, wp + 1280);
    f.x[1] = lds_b128(xp + 4096);
#pragma unroll
    for (int n = 1; n < 5; ++n) f.w[n] = lds_tr2(wp + n * 64, wp + n * 64 + 1280);
  };
  auto mma = [&](const FragQ& f) {
#pragma unroll
    for (int n = 0; n < 5; ++n)
#pragma unroll
      for (int s = 0; s < 2; ++s) acc[n][s] = mfma32(f.w[n], f.x[s], acc[n][s]);
#pragma unroll
    for (int i = 0; i < 6; ++i) { SG_MFMA(1); SG_DS(2); }
    SG_MFMA(4);
    SCHED_FENCE();
  };
  FST(0);
  BARRIER();
  FST(1);
  FragQ f0, f1;
  int so = 0;
  load(f0, lds, 0);
  SCHED_FENCE();
#pragma unroll 1
  for (int j = 0; j < NST1; ++j) {
    ldsp sb = lds + so;
    load(f1, sb, 1); mma(f0);
    load(f0, sb, 2); mma(f1);
    load(f1, sb, 3); mma(f0);
    so = so == 2 * SLOT ? 0 : so + SLOT;
    WAIT_LGKM0();
    if (j < NST1 - 1) BARRIER();
    FSTD(2 + j);
    SCHED_FENCE();
    load(f0, lds + so, 0); mma(f1);
  }
  WAIT_LGKM0();
}

template <int TY>
__device__ __forceinline__ void p1_main16(ldsp lds, int w, f32x4 (&acc)[4][10]) {
  constexpr bool TB = TY == T_GLU;
  const int l = threadIdx.x & 63, l15 = l & 15, g = l >> 4, q = l15 >> 2;
  const int xa = ((TB ? 0 : 64 * w) + l15) * 128 + ((g ^ ((l15 >> 1) & 7)) << 4);
  int wa0, wa1;
  if (TB) {
    wa0 = 20480 + (8 * g + q) * 256 + (((2 * w) ^ (q + 4 * (g & 1))) << 5) + (l & 3) * 8;
    wa1 = wa0 ^ 32;
  } else {
    wa0 = 32768 + (8 * g + q) * 320 + (l & 3) * 8 + 32 * (g & 1);
    wa1 = wa0 - 64 * (g & 1);
  }
  auto ldx = [&](ldsp sb, int u, int t) { return lds_b128(sb + (xa ^ (u << 6)) + t * 2048); };
  auto ldw = [&](ldsp sb, int u, int t) {
    ldsp a = sb + ((t & 1) ? wa1 : wa0) + (TB ? u * 8192 + (t >> 1) * 16384 : u * 10240 + t * 32);
    return lds_tr2(a, a + (TB ? 1024 : 1280));
  };
  auto lda = [&](ldsp sb, int u, int i) { return TB ? ldw(sb, u, i) : ldx(sb, u, i); };
  auto ldb = [&](ldsp sb, int u, int j) { return TB ? ldx(sb, u, j) : ldw(sb, u, j); };
  auto mma = [&](const bf16x8 (&a)[4], const bf16x8& b, int j) {
#pragma unroll
    for (int i = 0; i < 4; ++i) acc[i][j] = mfma16(a[i], b, acc[i][j]);
    SG_MFMA(1); SG_DS(2); SG_MFMA(1); SG_DS(2); SG_MFMA(1); SG_DS(2); SG_MFMA(1);
    SCHED_FENCE();
  };
  FST(0);
  BARRIER();
  FST(1);
  bf16x8 a0[4], a1[4], b0[10], b1[10];
  int so = 0;
#pragma unroll
  for (int i = 0; i < 4; ++i) a0[i] = lda(lds, 0, i);
#pragma unroll
  for (int j = 0; j < 5; ++j) b0[j] = ldb(lds, 0, j);
  SCHED_FENCE();
#pragma unroll 1
  for (int st = 0; st < NST1; ++st) {
    ldsp sb = lds + so;
#pragma unroll
    for (int j = 0; j < 10; ++j) {
      if (j < 5) b0[j + 5] = ldb(sb, 0, j + 5); else b1[j - 5] = ldb(sb, 1, j - 5);
      if (j >= 2 && j < 6) a1[j - 2] = lda(sb, 1, j - 2);
      mma(a0, b0[j], j);
    }
#pragma unroll
    for (int j = 0; j < 6; ++j) {
      if (j < 5) b1[j + 5] = ldb(sb, 1, j + 5);
      mma(a1, b1[j], j);
    }
    so = so == 2 * SLOT ? 0 : so + SLOT;
    sb = lds + so;
    WAIT_LGKM0();
    if (st < NST1 - 1) BARRIER(); else lds_arrive(lds + M_END);
    FSTD(2 + st);
    SCHED_FENCE();
    a0[0] = lda(sb, 0, 0); b0[0] = ldb(sb, 0, 0); a0[1] = lda(sb, 0, 1); mma(a1, b1[6], 6);
    a0[2] = lda(sb, 0, 2); a0[3] = lda(sb, 0, 3); b0[1] = ldb(sb, 0, 1); mma(a1, b1[7], 7);
    b0[2] = ldb(sb, 0, 2); b0[3] = ldb(sb, 0, 3); mma(a1, b1[8], 8);
    b0[4] = ldb(sb, 0, 4); mma(a1, b1[9], 9);
  }
  WAIT_LGKM0();
}

__device__ __forceinline__ void p2_main(const Params& p, ldsp lds, int w, int rt, int ct, u64 tag, const u32x4 (&held)[15]) {
  constexpr int WL = 1, LAG = 3, AW = P2_AW, NR = 17, RH = 12, GATE = 10;
  const int l = threadIdx.x & 63, l15 = l & 15, kg = l >> 4, q = l15 >> 2;
  const int sg = w & 1, kq = w >> 1, hf = w >> 2;
  const int rlo = rt * 96, rhi = min(rlo + 95, S - 1);
  const rsrc_t ra = mk_rsrc(p.ws, A2_BYTES);
  const unsigned av = (unsigned)(((rt * 6 + sg * 3) * KB_ALL + kq) * 1024 + l * 16);
  const int rh = 4 * kg + q, ro = 16 * (kg >> 1) + 4 * (kg & 1) + q;
  const int wh = rh * 128 + (((rh >> 1) & 3) << 5) + (l & 3) * 8;
  const int wo = ro * 128 + (((ro >> 1) & 3) << 5) + (l & 3) * 8;
  ldsp hdst = lds + ((w & 3) * 1024 + hf * 4096 + l * 16);
  f32x4 acc[4][3];
#pragma unroll
  for (int n = 0; n < 4; ++n)
#pragma unroll
    for (int s = 0; s < 3; ++s) acc[n][s] = f32x4{};
  u32x4 a[NR][3];
  bf16x8 wf[WL + 1][4];
  float gi[3];
  auto lda = [&](int t) {
#pragma unroll
    for (int s = 0; s < 3; ++s) {
      a[t][s] = __builtin_amdgcn_raw_buffer_load_b128(ra, av + s * KB_ALL * 1024, t * 4096, A2AUX);
    }
  };
  ldsp wb[4];
  auto ldw = [&](int t) {
    const int o0 = w2_off(4 * t), r0 = o0 & ~0x7FFF;
    auto straight = [](int u) { return w2_off(4 * u + 3) == w2_off(4 * u) + 3 * 4096; };
    if (t == 0 || t == RH || r0 != (w2_off(4 * t - 4) & ~0x7FFF) || !straight(t) || !straight(t - 1)) {
      int rel = kq * 4096;
      if (!straight(t)) rel = kq == 0 ? 0 : kq == 1 ? w2_off(4 * t + 1) - o0 : kq == 2 ? w2_off(4 * t + 2) - o0 : w2_off(4 * t + 3) - o0;
#pragma unroll
      for (int n = 0; n < 4; ++n) {
        wb[n] = lds + (((t < RH ? wh : wo) ^ ((n ^ kq) << 5)) + r0 + rel);
        asm volatile("" : "+v"(wb[n]));
      }
    }
#pragma unroll
    for (int n = 0; n < 4; ++n) wf[t % (WL + 1)][n] = lds_tr2(wb[n] + (o0 - r0), wb[n] + (o0 - r0) + (t < RH ? 2048 : 1024));
  };
  if (w == 4) poll(flag_ptr(p, (l & 32) ? FL_G + (((l & 1) ? rhi : rlo) >> 9) : FL_H + ((l & 16) ? rhi : rlo) / 160 * 12 + min(l15, 11)), tag);
  const unsigned cnt = (unsigned)(size_t)(lds + M_RND);
  auto arrive = [&](int r) {
    asm volatile("s_mov_b64 exec, 1\n\tds_add_u32 %0, %1 offset:%2\n\ts_mov_b64 exec, -1" ::"v"(cnt), "v"(1u), "i"(4 * r) : "memory");
  };
  auto counted = [](int r) { return (r >= 0 && r < GATE - LAG) || r == GATE || r == RH - 1; };
  unsigned peek = 0;
  auto check = [&](int r) {
    unsigned sv, n;
    asm volatile(
        "s_nop 0\n\tv_readfirstlane_b32 %0, %2\n\ts_cmp_ge_u32 %0, 8\n\ts_cbranch_scc1 .Lok%=\n\ts_mov_b32 %1, 0x100000\n"
        ".Lagain%=:\n\tds_read_b32 %2, %3 offset:%4\n\ts_waitcnt lgkmcnt(0)\n\tv_readfirstlane_b32 %0, %2\n\ts_sub_u32 %1, %1, 1\n\t"
        "s_cbranch_scc1 .Lok%=\n\ts_cmp_ge_u32 %0, 8\n\ts_cbranch_scc0 .Lagain%=\n.Lok%=:"
        : "=&s"(sv), "=&s"(n), "+v"(peek) : "v"(cnt), "i"(4 * r) : "memory", "scc");
  };
  auto prio = [](int v) {
    if (v >= 3) __builtin_amdgcn_s_setprio(3); else if (v == 2) __builtin_amdgcn_s_setprio(2);
    else if (v == 1) __builtin_amdgcn_s_setprio(1); else __builtin_amdgcn_s_setprio(0);
  };
  auto upto = [](int t) {
    if (t < GATE) return min(min(t + 1 + AW, P2_A0 + P2_RAMP * (t + 1)), RH);
    return min(RH + P2_G0 + P2_G1 * (t - GATE), NR);
  };
  BARRIER();
  STAMP(4);
  {
    const rsrc_t rg = mk_rsrc(p.ws + G_OFF, G_BYTES);
#pragma unroll
    for (int t = 0; t < upto(-1); ++t) {
      prio(3 - t); lda(t);
      if (t == 0) {
        ldw(0);
#pragma unroll
        for (int s = 0; s < 3; ++s) gi[s] = __builtin_bit_cast(float, __builtin_amdgcn_raw_buffer_load_b32(rg, (rt * 96 + sg * 48 + s * 16 + l15) * 4, 0, SC1));
      }
      SCHED_FENCE();
    }
  }
#pragma unroll
  for (int t = 0; t < NR; ++t) {
    if (t == GATE) {
      if (w == 7) poll(flag_ptr(p, FL_O + (((l & 4) ? rhi : rlo) >> 8) * 4 + (l & 3)), tag);
      WAIT_LGKM0();
      FST(16);
      BARRIER();
      FST(17);
      SCHED_FENCE();
    }
    if (counted(t - LAG)) check(t - LAG);
    if (counted(t + 1 - LAG)) peek = __hip_atomic_load((LAS unsigned*)(lds + M_RND + 4 * (t + 1 - LAG)), __ATOMIC_RELAXED, __HIP_MEMORY_SCOPE_WORKGROUP);
    SCHED_FENCE();
    prio(3 - (t & 3));
#pragma unroll
    for (int u = upto(t - 1); u < upto(t); ++u) lda(u);
    if (t + 1 < NR) ldw(t + 1);
    if (t >= LAG) {
#pragma unroll
      for (int i = 2 * (t - LAG); i < min(2 * (t - LAG) + 2, 15); ++i) {
        *(LAS u32x4*)(hdst + w2_off(39 + 2 * i)) = held[i];
      }
    }
    if (t == RH) {
#pragma unroll
      for (int n = 0; n < 4; ++n)
#pragma unroll
        for (int s = 0; s < 3; ++s) acc[n][s] *= 1.f - gi[s];
    }
#pragma unroll
    for (int s = 0; s < 3; ++s)
#pragma unroll
      for (int n = 0; n < 4; ++n) {
        acc[n][s] = mfma16(wf[t % (WL + 1)][n], __builtin_bit_cast(bf16x8, a[t][s]), acc[n][s]);
      }
    SG_VM(9);
    SG_MFMA(1); SG_DS(2); SG_MFMA(1); SG_DS(2); SG_MFMA(1); SG_DS(2); SG_MFMA(1); SG_DS(2);
    SG_MFMA(2); SG_DW(1); SG_MFMA(2); SG_DW(1); SG_MFMA(2); SG_DW(1); SG_MFMA(1); SG_DW(1); SG_MFMA(1);
    asm volatile("" : "+v"(acc[0][0]), "+v"(acc[1][0]), "+v"(acc[2][0]), "+v"(acc[3][0]), "+v"(acc[0][1]), "+v"(acc[1][1]), "+v"(acc[2][1]), "+v"(acc[3][1]),
                 "+v"(acc[0][2]), "+v"(acc[1][2]), "+v"(acc[2][2]), "+v"(acc[3][2]));
    if (counted(t)) arrive(t);
    SCHED_FENCE();
  }
  STAMP(5);
  constexpr int XB = 16384;
  static_assert(w2_off(30) == XB && w2_off(39) == XB + 9 * 4096, "ring layout");
  {
    ldsp wp = lds + (XB + w * 9 * 1024 + l * 16);
#pragma unroll
    for (int d = 1; d < 4; ++d)
#pragma unroll
      for (int s = 0; s < 3; ++s) {
        *(LAS f32x4*)(wp + ((d - 1) * 3 + s) * 1024) = acc[d][s];
      }
  }
  const rsrc_t rout = mk_rsrc(p.out, S * D * 2);
  unsigned vo = (unsigned)((rt * 96 + sg * 48 + l15) * 1280 + (ct * 64 + kq * 16 + 4 * kg) * 2);
  asm volatile("" : "+v"(vo));
  WAIT_LGKM0();
  BARRIER();
  f32x4 part[3][4];
#pragma unroll
  for (int s = 0; s < 3; ++s)
#pragma unroll
    for (int d = 1; d < 4; ++d) part[s][d] = *(LAS f32x4*)(lds + XB + ((w ^ (2 * d)) * 9 + (d - 1) * 3 + s) * 1024 + l * 16);
  SCHED_FENCE();
#pragma unroll
  for (int s = 0; s < 3; ++s) {
    const f32x4 v = (acc[0][s] + part[s][1]) + (part[s][2] + part[s][3]);
    __builtin_amdgcn_raw_buffer_store_b64(u32x2{pk2(v[0], v[1]), pk2(v[2], v[3])}, rout, vo + s * 16 * 1280, 0, OUT_AUX);
  }
}

__global__ __launch_bounds__(512, 2) void fused(Params p) {
  extern __shared__ __attribute__((aligned(16))) char smem[];
  ldsp lds = (ldsp)smem;
  u64 pc;
  asm volatile("s_getpc_b64 %0" : "=s"(pc));
  constexpr unsigned rest = CODE_BYTES;
  const int b = blockIdx.x, xcd = b & 7, idx = b >> 3, cg = xcd & 3, rg = xcd >> 2;
  const int w = __builtin_amdgcn_readfirstlane(threadIdx.x >> 6), lw = w - 4;
  const int l = threadIdx.x & 63;
  FINIT();
  STAMP(0);
  const int tm = (11 - idx) >> 31;
  const int jj = idx + ((20 * rg - 12) & tm), j3 = (jj * 43) >> 7, jr = jj - 3 * j3;
  const bool spare = jj == 39;
  const int ty = tm ? T_GLU : (jr == 0 ? T_Q : T_KV);
  const int rt = j3 + (4 * rg & ~tm);
  const int ctn = 2 * cg + jr - 1 + ((cg + 1) & tm);
  const int t2 = 28 * xcd - ((xcd - 4) & -rg) + ((idx - 4) & tm) + ((2 * j3 + jr - 1) & ~tm);
  const int rt2 = (t2 * 205) >> 11, ct2 = t2 - 10 * rt2;
  const int rot0 = ROT;
  auto get_tag = [&]() {
    u64 e;
    asm volatile("s_load_dwordx2 %0, %1, 0x0\n\ts_waitcnt lgkmcnt(0)" : "=s"(e) : "s"(p.ws + EPOCH_OFF) : "memory");
    return make_tag(e);
  };

  constexpr int PQX = 2 * SLOT;
  const bool loader = w >= 4 && !spare;
  Warm wm(lds);
  u64 tag;
  auto mfma_start = [&]() {
    if (idx < 2) {
      const unsigned o = (idx * 256 + w * 64 + l) * 128;
      wm.touch((const GAS char*)pc + (o < rest ? o : 0u));
    }
    if (threadIdx.x < 32) *(LAS unsigned*)(lds + M_CNT + threadIdx.x * 4) = 0u;
    tag = get_tag();
  };
  const W2Src src(p, w & 3, ct2);
  if (__builtin_expect(ty == T_Q, 0)) {
    const int ct = cg;
    bf16x8 pq[10];
    if (loader) {
      u32x4 none[30];
      p1_loader<true>(p, lds, lw, 0, rt, ct, rot0, src, none, [&]() {
        {
          const unsigned o = ((j3 * 4 + lw) * 24 + min(l, 23)) * 128;
          wm.touch((const GAS char*)pc + (o < rest ? o : 0u));
        }
        tag = get_tag();
        for (int i = lw * 64 + l; i < 2 * KVT_SZ / 16; i += 256)
          asm volatile("ds_write_b128 %0, %1" ::"v"((unsigned)(size_t)(lds + KVT) + i * 16), "v"(u32x4{0u, 0u, 0u, 0u}) : "memory");
        if (lw == 0) poll(flag_ptr(p, FL_KV + (l & 7) * 8 + 2 * ct + ((l >> 3) & 1)), tag);
        WAIT_LGKM0();
      });
      poll(flag_ptr(p, FL_G + (rt >> 1)), tag);
      const int tl = lw * 64 + l;
      *(LAS unsigned*)(lds + M_G + tl * 4) = __builtin_amdgcn_raw_buffer_load_b32(mk_rsrc(p.ws + G_OFF, G_BYTES), (rt * 256 + tl) * 4, 0, SC1);
      lds_wait(lds + M_END, 4);
#pragma unroll
      for (int t = 0; t < 10; ++t) { pq[t] = lds_b128(lds + PQX + (lw * 10 + t) * 1024 + l * 16); asm volatile("" : "+v"(pq[t])); }
    } else {
    mfma_start();
    f32x16 acc[5][2];
#pragma unroll
    for (int n = 0; n < 5; ++n) { acc[n][0] = f32x16{}; acc[n][1] = f32x16{}; }
    p1_main_q(lds, w, acc);
    STAMP(1);
    const Consts cn(1.f);
#pragma unroll
    for (int k = 9; k >= 0; --k)
#pragma unroll
      for (int s = 0; s < 2; ++s) {
        const f32x16& c = acc[k >> 1][s];
        const int o = 8 * (k & 1);
        const bf16x8 f = pack44(phi4(f32x4{c[o], c[o + 1], c[o + 2], c[o + 3]}, cn), phi4(f32x4{c[o + 4], c[o + 5], c[o + 6], c[o + 7]}, cn));
        if (k >= 5) *(LAS bf16x8*)(lds + PQX + (w * 10 + 5 * s + k - 5) * 1024 + l * 16) = f;
        else { pq[5 * s + k] = f; asm volatile("" : "+v"(pq[5 * s + k])); }
        if (k == 5 && s == 1) { WAIT_LGKM0(); lds_arrive(lds + M_END); }
      }
    STAMP(2);
    }
    {
      const int ch = min((int)threadIdx.x, 419), hg = ch >= 210;
      ldsp dp[2];
      bool dup[2];
#pragma unroll
      for (int u = 0; u < 2; ++u) {
        const int un = 2 * (ch - 210 * hg) + u, row = un / 5, dq = un % 5, hd = row < 80 ? row / 20 : row - 80, run = 5 * hd + dq;
        dp[u] = lds + KVT + hg * KVT_SZ + row * KS + (run >> 2) * 32 + (run & 1) * 16 + (run & 2) * 4;
        dup[u] = row >= 80;
      }
      const rsrc_t rk = mk_rsrc(p.ws + KVP_OFF, KVP_BYTES);
      u32x4 pv[8];
      unsigned todo = 0xFFu;
#pragma unroll 1
      for (int spin = 0; spin < SPIN_MAX && todo; ++spin) {
#pragma unroll
        for (int r = 0; r < 8; ++r)
          if (todo >> r & 1) pv[r] = __builtin_amdgcn_raw_buffer_load_b128(rk, 2 * ct * 3360 + ch * 16, r * 8 * 3360, SC1);
#pragma unroll
        for (int r = 0; r < 8; ++r)
          if (__all(max(max(pv[r][0], pv[r][1]), max(pv[r][2], pv[r][3])) != UNSET)) todo &= ~(1u << r);
        asm volatile("s_sleep 1" ::: "memory");
      }
      STAMP(3);
      f32x2 sum[4];
#pragma unroll
      for (int e = 0; e < 4; ++e) sum[e] = f32x2{0.f, 0.f};
#pragma unroll
      for (int r = 0; r < 8; ++r)
#pragma unroll
        for (int e = 0; e < 4; ++e) {
          const unsigned x = pv[r][e];
          sum[e] += f32x2{__uint_as_float(x << 16), __uint_as_float(x & 0xFFFF0000u)};
        }
#pragma unroll
      for (int u = 0; u < 2; ++u) {
        const u32x2 v = {pk2(sum[2 * u][0], sum[2 * u][1]), pk2(sum[2 * u + 1][0], sum[2 * u + 1][1])};
        *(LAS u32x2*)dp[u] = v;
        if (dup[u]) *(LAS u32x2*)(dp[u] + 4 * KS) = v;
      }
      WAIT_LGKM0();
      BARRIER();
      STAMP(4);
    }
  {
    const int l31 = l & 31, h = l >> 5, hg = w >> 2, wr = w & 3;
    const rsrc_t ra = mk_rsrc(p.ws, A2_BYTES);
    const unsigned vo = (unsigned)(((rt * 16 + wr * 4 + ((l >> 4) & 1)) * KB_ALL + 48 + 5 * ct) * 1024 + hg * 2560 + h * 256 + (l & 15) * 16);
    bf16x8 af[3][5];
#pragma unroll
    for (int et = 2; et >= 0; --et)
#pragma unroll
      for (int t = 0; t < 5; ++t)
        if (!((et == 0 && t > 2) || (et == 1 && t == 0))) af[et][t] = lds_b128(lds + (KVT + hg * KVT_SZ + l31 * KS + h * 16) + 32 * et * KS + t * 32);
    float gv[2];
#pragma unroll
    for (int s = 0; s < 2; ++s) gv[s] = *(LAS float*)(lds + M_G + (64 * wr + 32 * s + l31) * 4);
    FST(18);
    SCHED_FENCE();
    f32x16 acc[2][3];
#pragma unroll
    for (int s = 0; s < 2; ++s)
#pragma unroll
      for (int et = 2; et >= 0; --et) {
        acc[s][et] = f32x16{};
#pragma unroll
        for (int t = 0; t < 5; ++t)
          if (!((et == 0 && t > 2) || (et == 1 && t == 0))) acc[s][et] = mfma32(af[et][t], pq[5 * s + t], acc[s][et]);
      }
#pragma unroll
    for (int s = 0; s < 2; ++s) {
      f32x4 sc;
#pragma unroll
      for (int hd = 0; hd < 4; ++hd) sc[hd] = gv[s] * __builtin_amdgcn_rcpf(acc[s][2][8 + hd] + 1e-6f);
#pragma unroll
      for (int pc_ = 4; pc_ >= 0; --pc_) {
        f32x4 t[2];
#pragma unroll
        for (int gq = 0; gq < 2; ++gq) {
          const int e0 = 16 * pc_ + 8 * gq, o = 8 * (pc_ & 1) + 4 * gq;
          float fa = sc[e0 / 20], fb = sc[(e0 + 4) / 20];
          if (e0 / 20 != (e0 + 4) / 20) { asm volatile("" : "+v"(fa), "+v"(fb)); fa = h ? fb : fa; }
          const f32x16& c = acc[s][pc_ >> 1];
          const f32x2 lo = f32x2{c[o], c[o + 1]} * f32x2{fa, fa}, hi = f32x2{c[o + 2], c[o + 3]} * f32x2{fa, fa};
          t[gq] = f32x4{lo[0], lo[1], hi[0], hi[1]};
        }
        st128_sc1(ra, vo + s * 2 * KB_ALL * 1024 + pc_ * 512, __builtin_bit_cast(u32x4, pack44(t[0], t[1])));
      }
    }
    STAMP(5);
    publish(p, lds, FL_O + rt * 4 + ct, tag, 8);
    STAMP(6);
    FDUMP();
    return;
  }
  }
  const int ct = ctn;
  u32x4 held[15];
  if (__builtin_expect(loader, 1)) {
    u32x4 pre[30];
    p1_loader<false>(p, lds, lw, tm, rt, ct, rot0, src, pre, [&]() { tag = get_tag(); });
    src.fill<0, 24>(lds + lw * 1024);
    if (ty == T_KV) {
#pragma unroll
      for (int i = 0; i < 30; ++i) pre[i] = pre_load(src, i);
      SCHED_FENCE();
      BARRIER(); BARRIER();
    } else {
      lds_wait(lds + M_END, 4);
    }
    WAIT_VM(0);
#pragma unroll
    for (int i = 0; i < 15; ++i) *(LAS u32x4*)(lds + (lw * 1024 + l * 16) + w2_off(24 + i)) = pre[i];
#pragma unroll
    for (int i = 0; i < 15; ++i) held[i] = pre[15 + i];
    WAIT_LGKM0();
  } else {
    mfma_start();
    if (ty == T_KV && w == 0) {
      const rsrc_t rk = mk_rsrc(p.ws + KVP_OFF + (rt * 8 + ct) * 3360, 3360);
#pragma unroll
      for (int i = 0; i < 4; ++i) st128_sc1(rk, i * 1024 + l * 16, u32x4{UNSET, UNSET, UNSET, UNSET});
      WAIT_VM(0);
      if (l == 0) __hip_atomic_store(flag_ptr(p, FL_KV + rt * 8 + ct), tag, __ATOMIC_RELAXED, __HIP_MEMORY_SCOPE_AGENT);
    }
    if (spare) {
      BARRIER();
      const int l15 = l & 15, r0 = cg * 512 + w * 64 + (l >> 4);
      const rsrc_t rx = mk_rsrc(p.x, S * D * 2), rgw = mk_rsrc(p.gw, D * 2), rg_ = mk_rsrc(p.ws + G_OFF, G_BYTES);
      u32x4 gw[5], v[2][4][5];
#pragma unroll
      for (int c = 0; c < 5; ++c) gw[c] = __builtin_amdgcn_raw_buffer_load_b128(rgw, l15 * 16 + c * 256, 0, 0);
      auto ld = [&](int it) {
#pragma unroll
        for (int u = 0; u < 4; ++u)
#pragma unroll
          for (int c = 0; c < 5; ++c) v[it & 1][u][c] = __builtin_amdgcn_raw_buffer_load_b128(rx, (r0 + 4 * u) * 1280 + l15 * 16 + c * 256, it * 16 * 1280, 0);
      };
      ld(0);
#pragma unroll
      for (int it = 0; it < 4; ++it) {
        if (it < 3) ld(it + 1);
        SCHED_FENCE();
        float keep = 0.f;
#pragma unroll
        for (int u = 0; u < 4; ++u) {
          float sacc = 0.f;
#pragma unroll
          for (int c = 0; c < 5; ++c)
#pragma unroll
            for (int e = 0; e < 4; ++e) {
              const unsigned xw = v[it & 1][u][c][e], ge = gw[c][e];
              sacc = __builtin_amdgcn_fdot2_f32_bf16(__builtin_bit_cast(bf16x2, xw), __builtin_bit_cast(bf16x2, ge), sacc, false);
            }
          sacc += __shfl_xor(sacc, 1); sacc += __shfl_xor(sacc, 2); sacc += __shfl_xor(sacc, 4); sacc += __shfl_xor(sacc, 8);
          if (l15 == u) keep = sacc;
        }
        if (l15 < 4) __builtin_amdgcn_raw_buffer_store_b32(__builtin_bit_cast(unsigned, sigmoidf(keep)), rg_, (r0 + 16 * it + 4 * l15) * 4, 0, SC1);
        SCHED_FENCE();
      }
      publish(p, lds, FL_G + cg, tag, 8);
      STAMP(6);
      FDUMP();
      if (cg == 0 && w == 0) {
#pragma unroll 1
        for (int i = 0; i < 4; ++i) poll(flag_ptr(p, i * 64 + l), tag);
        if (l == 0) __hip_atomic_store((GAS u64*)(p.ws + EPOCH_OFF), tag, __ATOMIC_RELAXED, __HIP_MEMORY_SCOPE_AGENT);
      }
      return;
    }
      f32x4 acc[4][10];
#pragma unroll
      for (int i = 0; i < 4; ++i)
#pragma unroll
        for (int j = 0; j < 10; ++j) acc[i][j] = f32x4{};
      if (ty == T_GLU) {
        p1_main16<T_GLU>(lds, w, acc);
        STAMP(1);
        const Consts cn(-1.f);
        const rsrc_t ra = mk_rsrc(p.ws, A2_BYTES);
        const unsigned vo = (unsigned)((rt * 10 * KB_ALL + ct * 4 + w) * 1024 + l * 16);
#pragma unroll
        for (int j = 0; j < 10; ++j) {
          st128_sc1(ra, vo + j * KB_ALL * 1024, __builtin_bit_cast(u32x4, pack44(glu4(acc[0][j], acc[2][j], cn), glu4(acc[1][j], acc[3][j], cn))));
          SCHED_FENCE();
        }
        STAMP(2);
        publish(p, lds, FL_H + rt * 12 + ct, tag, 4);
      } else {
        int goff[2];
#pragma unroll
        for (int i = 0; i < 2; ++i) {
          const int en = min((int)threadIdx.x + 256 * i, 419), row = en / 5, dq = en % 5;
          const int hd = row < 80 ? row / 20 : row - 80, d0 = 20 * hd + 4 * dq, ti = d0 >> 4, tj = row >> 4;
          const int tile = row >= 80 ? 9 : ti == tj ? ti : (ti == 0 || ti == 4) ? 8 : 4 + ti;
          goff[i] = tile * 1024 + (((d0 & 15) >> 2) * 16 + (row < 80 ? row & 15 : ti)) * 16;
          asm volatile("" : "+v"(goff[i]));
        }
        p1_main16<T_KV>(lds, w, acc);
        STAMP(1);
        const Consts cn(1.f);
        const int l15 = l & 15, g = l >> 4;
        ldsp tp = lds + (w * 10240 + l * 16);
        bf16x8 vf[2][5];
#pragma unroll
        for (int u = 0; u < 2; ++u)
#pragma unroll
          for (int j = 0; j < 5; ++j) vf[u][j] = pack44(acc[2 * u][5 + j], acc[2 * u + 1][5 + j]);
        f32x4 z = {}, dg[5], lo[5], hi[5];
        SCHED_FENCE();
#pragma unroll
        for (int i = 0; i < 5; ++i) {
          const unsigned o1 = l15 == i ? 0x3F803F80u : 0u;
          const bf16x8 one = __builtin_bit_cast(bf16x8, u32x4{o1, o1, o1, o1});
          dg[i] = f32x4{}; lo[i] = f32x4{}; hi[i] = f32x4{};
#pragma unroll
          for (int u = 0; u < 2; ++u) {
            const bf16x8 kf = pack44(phi4(acc[2 * u][i], cn), phi4(acc[2 * u + 1][i], cn));
            if (i > 0) lo[i] = mfma16(kf, vf[u][i - 1], lo[i]);
            if (i < 4) hi[i] = mfma16(kf, vf[u][i + 1], hi[i]);
            dg[i] = mfma16(kf, vf[u][i], dg[i]);
            z = mfma16(kf, one, z);
            if (i > 0) {
              if (u == 0) *(LAS f32x4*)(tp + (i - 1) * 1024) = dg[i - 1];
              else if (i > 1) *(LAS f32x4*)(tp + (3 + i) * 1024) = g < i - 1 ? lo[i - 1] : hi[i - 1];
            }
            SCHED_FENCE();
          }
          if (i == 0) { FST(18); lds_wait(lds + M_END, 4); }
        }
        FST(19);
        *(LAS f32x4*)(tp + 8 * 1024) = l15 < 8 ? hi[0] : lo[4];
        *(LAS f32x4*)(tp + 4 * 1024) = dg[4];
        *(LAS f32x4*)(tp + 9 * 1024) = z;
        WAIT_LGKM0();
        FST(20);
        BARRIER();
        FST(21);
        const rsrc_t rk = mk_rsrc(p.ws + KVP_OFF + (rt * 8 + ct) * 3360, 3360);
#pragma unroll
        for (int i = 0; i < 2; ++i) {
          f32x4 sum = *(LAS f32x4*)(lds + goff[i]);
#pragma unroll
          for (int ww = 1; ww < 4; ++ww) sum += *(LAS f32x4*)(lds + goff[i] + ww * 10240);
          __builtin_amdgcn_raw_buffer_store_b64(u32x2{min(pk2(sum[0], sum[1]), UNSET - 1), min(pk2(sum[2], sum[3]), UNSET - 1)}, rk, threadIdx.x * 8 + i * 2048, 0, SC1);
        }
        WAIT_LGKM0();
        FST(22);
        BARRIER();
        STAMP(2);
      }
      STAMP(3);
#pragma unroll
      for (int i = 0; i < 15; ++i) held[i] = src.load(39 + 2 * i, 0);
      SCHED_FENCE();
  }
  p2_main(p, lds, w, rt2, ct2, tag, held);
  STAMP(6);
  FDUMP();
}

#define STAMP_PTR nullptr
extern "C" void kernel_launch(void* const* d_in, const int* in_sizes, int n_in, void* d_out, int out_size, void* d_ws, size_t ws_size,
                              hipStream_t stream) {
  Params p;
  p.x = (const u16*)d_in[0]; p.wqkv = (const u16*)d_in[1]; p.wo = (const u16*)d_in[2];
  p.wg = (const u16*)d_in[4]; p.wu = (const u16*)d_in[5]; p.wd = (const u16*)d_in[6]; p.gw = (const u16*)d_in[7];
  p.out = (u16*)d_out; p.ws = (char*)d_ws; p.stamps = STAMP_PTR;
  (void)hipFuncSetAttribute((const void*)fused, hipFuncAttributeMaxDynamicSharedMemorySize, LDS_BYTES);
  hipLaunchKernelGGL(fused, dim3(256), dim3(512), LDS_BYTES, stream, p);
}
```

```cpp
#define DIAG 0
#define DIAG_THR 2800
#include <hip/hip_runtime.h>
#include <hip/hip_bf16.h>

typedef unsigned short u16;
typedef unsigned long long u64;
typedef __bf16 bf16x8 __attribute__((ext_vector_type(8)));
typedef __bf16 bf16x4 __attribute__((ext_vector_type(4)));
typedef __bf16 bf16x2 __attribute__((ext_vector_type(2)));
typedef float f32x16 __attribute__((ext_vector_type(16)));
typedef float f32x4 __attribute__((ext_vector_type(4)));
typedef unsigned u32x4 __attribute__((ext_vector_type(4)));
typedef unsigned u32x2 __attribute__((ext_vector_type(2)));
#define LAS __attribute__((address_space(3)))
#define GAS __attribute__((address_space(1)))
typedef LAS char* ldsp;
typedef __amdgpu_buffer_rsrc_t rsrc_t;

constexpr int S = 2048, D = 640, F = 1536;
constexpr float LOG2E = 1.4426950408889634f;
constexpr int KB_ALL = 68;
constexpr unsigned A2_BYTES = 128u * KB_ALL * 1024;
constexpr unsigned KVP_OFF = A2_BYTES, KVP_BYTES = 8 * 8 * 3360;
constexpr unsigned G_OFF = KVP_OFF + KVP_BYTES, G_BYTES = S * 4;
constexpr unsigned FLAG_OFF = G_OFF + G_BYTES;
constexpr unsigned EPOCH_OFF = FLAG_OFF + 256 * 128;
constexpr unsigned WS_USED = EPOCH_OFF + 128;
constexpr int FL_H = 0, FL_KV = 156, FL_O = 220, FL_G = 252;
constexpr int SC1 = 16;
constexpr unsigned UNSET = 0xFFFFFFFFu;
constexpr int SLOT = 53248, MISC = 3 * SLOT, LDS_BYTES = 163840;
constexpr int M_DUMMY = MISC, M_CNT = MISC + 256  , M_END = MISC + 260  , M_RND = MISC + 320  , M_Z = MISC + 512  , M_G = MISC + 2048  ;
constexpr int KS = 176, KVT = SLOT, KVT_SZ = 96 * KS;
constexpr int NST1 = 10;
constexpr int SPIN_MAX = 1 << 16;
constexpr unsigned CODE_BYTES = 44000;
#define A2AUX SC1
#define GLU_PACE 0
#define OUT_AUX 0
#define P2_AW 3
#define P2_X3 99
#define P2_PRIO 2
#define P2_A0 1
#define P2_RAMP 2
#define P2_G0 2
#define P2_G1 1
#define P2_WL 1
#define P2_POS 0
#define ROT (2 * (cg & 1) + 5 * (((cg >> 1) ^ rg) & 1))
#define W2_ST 4

struct Params {
  const u16 *x, *wqkv, *wo, *wg, *wu, *wd, *gw;
  u16* out;
  char* ws;
  u64* stamps;
};

#define STAMP(i) do {} while (0)
#define FST(i) do {} while (0)
#define FSTD(i) do {} while (0)
#define FINIT() do {} while (0)
#define FDUMP() do {} while (0)

#define DT(v) asm volatile("s_memrealtime %0\n\ts_waitcnt lgkmcnt(0)" : "=s"(v)::"memory")
struct Diag { u64 t0, t1, t2, t3, t4; };
#define WAIT_VM(n) asm volatile("s_waitcnt vmcnt(" #n ")" ::: "memory")
#define WAIT_LGKM0() asm volatile("s_waitcnt lgkmcnt(0)" ::: "memory")
#define BARRIER() do { asm volatile("" ::: "memory"); __builtin_amdgcn_s_barrier(); asm volatile("" ::: "memory"); } while (0)
#define SCHED_FENCE() __builtin_amdgcn_sched_barrier(0)
#define SG_MFMA(n) __builtin_amdgcn_sched_group_barrier(0x8, n, 0)
#define SG_DS(n) __builtin_amdgcn_sched_group_barrier(0x100, n, 0)
#define SG_DW(n) __builtin_amdgcn_sched_group_barrier(0x200, n, 0)
#define SG_VM(n) __builtin_amdgcn_sched_group_barrier(0x20, n, 0)

__device__ __forceinline__ rsrc_t mk_rsrc(const void* p, unsigned bytes) { return __builtin_amdgcn_make_buffer_rsrc((void*)p, 0, bytes, 0x00020000); }
__device__ __forceinline__ void dma16(rsrc_t r, ldsp dst, unsigned voff, unsigned soff) { __builtin_amdgcn_raw_ptr_buffer_load_lds(r, dst, 16, voff, soff, 0, 0); }
__device__ __forceinline__ bf16x8 lds_b128(ldsp a) { return *(LAS bf16x8*)a; }
__device__ __forceinline__ bf16x8 lds_tr2(ldsp a0, ldsp a1) {
  bf16x4 lo = __builtin_amdgcn_ds_read_tr16_b64_v4bf16((LAS bf16x4*)a0);
  bf16x4 hi = __builtin_amdgcn_ds_read_tr16_b64_v4bf16((LAS bf16x4*)a1);
  return __builtin_shufflevector(lo, hi, 0, 1, 2, 3, 4, 5, 6, 7);
}
__device__ __forceinline__ f32x16 mfma32(bf16x8 a, bf16x8 b, f32x16 c) { return __builtin_amdgcn_mfma_f32_32x32x16_bf16(a, b, c, 0, 0, 0); }
__device__ __forceinline__ f32x4 mfma16(bf16x8 a, bf16x8 b, f32x4 c) { return __builtin_amdgcn_mfma_f32_16x16x32_bf16(a, b, c, 0, 0, 0); }
__device__ __forceinline__ unsigned pk2(float a, float b) {
  bf16x2 v = {(__bf16)a, (__bf16)b};
  return __builtin_bit_cast(unsigned, v);
}
__device__ __forceinline__ bf16x8 pack8(const f32x16& c, int o) {
  u32x4 r = {pk2(c[o], c[o + 1]), pk2(c[o + 2], c[o + 3]), pk2(c[o + 4], c[o + 5]), pk2(c[o + 6], c[o + 7])};
  return __builtin_bit_cast(bf16x8, r);
}
__device__ __forceinline__ bf16x8 pack44(const f32x4& a, const f32x4& b) {
  u32x4 r = {pk2(a[0], a[1]), pk2(a[2], a[3]), pk2(b[0], b[1]), pk2(b[2], b[3])};
  return __builtin_bit_cast(bf16x8, r);
}
__device__ __forceinline__ float phi(float t) {
  const float e = __builtin_amdgcn_exp2f(t * LOG2E);
  return __builtin_amdgcn_fmed3f(t, 1e-6f, __builtin_inff()) + __builtin_amdgcn_fmed3f(e, 0.f, 1.f);
}
__device__ __forceinline__ float sigmoidf(float t) { return __builtin_amdgcn_rcpf(1.f + __builtin_amdgcn_exp2f(-LOG2E * t)); }
typedef float f32x2 __attribute__((ext_vector_type(2)));
struct Consts {
  f32x2 l2e;
  float eps, big;
  __device__ __forceinline__ Consts(float sign) : l2e{sign * LOG2E, sign * LOG2E}, eps(1e-6f), big(3.4028234664e38f) {
    asm volatile("" : "+s"(l2e), "+s"(eps), "+v"(big));
  }
};
__device__ __forceinline__ f32x4 phi4(f32x4 t, const Consts& c) {
  const f32x2 a = f32x2{t[0], t[1]} * c.l2e, b = f32x2{t[2], t[3]} * c.l2e;
  f32x2 m0, m1, e0, e1;
#pragma unroll
  for (int i = 0; i < 2; ++i) {
    m0[i] = __builtin_amdgcn_fmed3f(t[i], c.eps, c.big);
    m1[i] = __builtin_amdgcn_fmed3f(t[2 + i], c.eps, c.big);
    e0[i] = __builtin_amdgcn_fmed3f(__builtin_amdgcn_exp2f(a[i]), 0.f, 1.f);
    e1[i] = __builtin_amdgcn_fmed3f(__builtin_amdgcn_exp2f(b[i]), 0.f, 1.f);
  }
  const f32x2 r0 = m0 + e0, r1 = m1 + e1;
  return f32x4{r0[0], r0[1], r1[0], r1[1]};
}
__device__ __forceinline__ f32x4 glu4(f32x4 g, f32x4 u, const Consts& c) {
  f32x2 a = f32x2{g[0], g[1]} * c.l2e, b = f32x2{g[2], g[3]} * c.l2e;
#pragma unroll
  for (int i = 0; i < 2; ++i) { a[i] = __builtin_amdgcn_exp2f(a[i]); b[i] = __builtin_amdgcn_exp2f(b[i]); }
  a += 1.f; b += 1.f;
#pragma unroll
  for (int i = 0; i < 2; ++i) { a[i] = __builtin_amdgcn_rcpf(a[i]); b[i] = __builtin_amdgcn_rcpf(b[i]); }
  a *= f32x2{u[0], u[1]}; b *= f32x2{u[2], u[3]};
  return f32x4{a[0], a[1], b[0], b[1]};
}
__device__ __forceinline__ void st128_sc1(rsrc_t r, unsigned off, u32x4 v) { __builtin_amdgcn_raw_buffer_store_b128(v, r, off, 0, SC1); }

__device__ __forceinline__ u64 make_tag(u64 e) { return ((e ^ 0x9E3779B97F4A7C15ull) * 0xBF58476D1CE4E5B9ull) | 1ull; }
__device__ __forceinline__ GAS u64* flag_ptr(const Params& p, int f) { return (GAS u64*)(p.ws + FLAG_OFF + (size_t)f * 128); }
__device__ __forceinline__ u64 ld_flag(GAS u64* f) { return __hip_atomic_load(f, __ATOMIC_RELAXED, __HIP_MEMORY_SCOPE_AGENT); }
__device__ __forceinline__ void poll(GAS u64* f, u64 tag) {
#pragma unroll 1
  for (int spin = 0; spin < SPIN_MAX; ++spin) {
    if (__all(ld_flag(f) == tag)) break;
    __builtin_amdgcn_s_sleep(2);
  }
  __builtin_amdgcn_fence(__ATOMIC_ACQUIRE, "wavefront");
}
__device__ __forceinline__ void lds_arrive(ldsp c) {
  if ((threadIdx.x & 63) == 0) __hip_atomic_fetch_add((LAS unsigned*)c, 1u, __ATOMIC_RELAXED, __HIP_MEMORY_SCOPE_WORKGROUP);
}
__device__ __forceinline__ void lds_wait(ldsp c, unsigned n) {
#pragma unroll 1
  for (int spin = 0; spin < SPIN_MAX; ++spin) {
    unsigned v;
    asm volatile("ds_read_b32 %0, %1\n\ts_waitcnt lgkmcnt(0)" : "=v"(v) : "v"((unsigned)(size_t)c) : "memory");
    if (__builtin_amdgcn_readfirstlane(v) >= n) break;
    __builtin_amdgcn_s_sleep(1);
  }
}
__device__ __forceinline__ void publish(const Params& p, ldsp lds, int f, u64 tag, unsigned n) {
  WAIT_VM(0);
  if ((threadIdx.x & 63) == 0) {
    const unsigned old = __hip_atomic_fetch_add((LAS unsigned*)(lds + M_CNT), 1u, __ATOMIC_RELAXED, __HIP_MEMORY_SCOPE_WORKGROUP);
    if (old == n - 1) __hip_atomic_store(flag_ptr(p, f), tag, __ATOMIC_RELAXED, __HIP_MEMORY_SCOPE_AGENT);
  }
}

enum { T_Q = 0, T_KV = 1, T_GLU = 2 };

__device__ __forceinline__ constexpr int w2_off(int s) { return (SLOT + (s % 39) * 4096) % (3 * SLOT); }

struct W2Src {
  rsrc_t rd, ro;
  unsigned wv, ws0;
  __device__ __forceinline__ W2Src(const Params& p, int piece, int ct) {
    const int l = threadIdx.x & 63;
    rd = mk_rsrc(p.wd, F * D * 2);
    ro = mk_rsrc(p.wo, D * D * 2);
    wv = (l >> 3) * 1280 + (((l & 7) ^ ((l >> 4) << 1)) << 4);
    ws0 = (unsigned)(8 * piece) * 1280 + ct * 128;
  }
  __device__ __forceinline__ u32x4 load(int s, int add) const {
    if (s == 47) return __builtin_amdgcn_raw_buffer_load_b128(add ? ro : rd, wv, ws0 + (add ? 0u : 47u * 32 * 1280), 0);
    return __builtin_amdgcn_raw_buffer_load_b128(s < 48 ? rd : ro, wv, ws0 + (unsigned)((s < 48 ? s : s - 48) + add) * 32 * 1280, 0);
  }
  template <int S0, int S1> __device__ __forceinline__ void fill(ldsp dst) const {
#pragma unroll
    for (int s = S0; s < S1; ++s) dma16(rd, dst + w2_off(s), wv, ws0 + (unsigned)s * 32 * 1280);
  }
};

__device__ __forceinline__ u32x4 pre_load(const W2Src& src, int i) { return i < 15 ? src.load(24 + i, 0) : src.load(40 + 2 * (i - 15), 0); }

template <bool ISQ, class Gap>
__device__ __forceinline__ void p1_loader(const Params& p, ldsp lds, int lw, int tm, int rt, int ct, int k0, const W2Src& src, u32x4 (&pre)[30], Gap gap) {
  const bool tb = !ISQ && tm != 0;
  auto sel = [&](int a, int b) { return ISQ ? b : b ^ ((a ^ b) & tm); };
  auto selp = [&](const u16* a, const u16* b) { return ISQ ? b : (const u16*)((u64)b ^ (((u64)a ^ (u64)b) & (u64)(long long)tm)); };
  const int l = threadIdx.x & 63;
  const int i8 = sel(5 * lw, 8 * lw + 3);
  const unsigned xe = (l >> 3) * 1280 + (((l & 7) ^ (l >> 4)) << 4);
  const unsigned xa = (xe ^ ((i8 & 1) << 6)) + (unsigned)(sel(160, 256) * rt + 8 * i8) * 1280, xb = (xa ^ 64) + 8 * 1280;
  const int d8 = i8 * 1024;
  const rsrc_t rx = mk_rsrc(p.x, S * D * 2);
  FST(0);
#pragma unroll
  for (int j = 0; j < 5; ++j) dma16(rx, lds + d8 + j * 1024, (j & 1) ? xb : xa, k0 * 128 + (j >> 1) * 16 * 1280);
  SCHED_FENCE();
  const rsrc_t r0 = mk_rsrc(selp(p.wg, p.wqkv), sel(D * F * 2, D * 3 * D * 2));
  const rsrc_t r1 = mk_rsrc(selp(p.wu, p.wqkv), sel(D * F * 2, D * 3 * D * 2));
  const rsrc_t r2 = mk_rsrc(selp(p.wu, p.x), sel(D * F * 2, S * D * 2));
  unsigned v[8];
  {
    const int r = l >> 4;
    const unsigned g0 = (16 * lw + r) * 3072 + ct * 256 + (((l & 15) ^ (r << 1)) << 4);
    const int colA = ISQ ? 320 * ct : 1280 + 160 * ct, colB = ISQ ? 160 : 1280;
#pragma unroll
    for (int j = 0; j < 5; ++j) {
      const int c = 64 * j + l, rr = (c * 3277) >> 16;
      int cc = c - 20 * rr;
      if (!ISQ) cc ^= (rr >> 2) & 2;
      const unsigned a = (16 * lw + rr) * 3840 + colA + cc * 16 + (cc < 10 ? 0 : colB - 160);
      v[j] = sel((g0 ^ ((j & 2) << 6)) + (j & 3) * 4 * 3072, a);
    }
    v[5] = sel(v[1], xb - 4 * 8 * 1280);
    v[6] = sel(v[2], xa - 2 * 8 * 1280);
    v[7] = sel(v[3], xb - 2 * 8 * 1280);
  }
  const int d0 = sel(20480 + 4096 * lw, 32768 + 5120 * lw), d4 = d0 + sel(16384, 4096), d5 = sel(d4 + 1024, d8 - 3072);
  const unsigned wstep = sel(64 * 3072, 64 * 3840), mstep = sel(64 * 3072, 128);
  auto issue_a = [&](int st, int so) {
    ldsp sb = lds + so;
#pragma unroll
    for (int j = 0; j < 5; ++j) dma16(rx, sb + d8 + j * 1024, (j & 1) ? xb : xa, st * 128 + (j >> 1) * 16 * 1280);
#pragma unroll
    for (int j = 0; j < 2; ++j) dma16(r2, sb + d5 + j * 1024, v[5 + j], st * mstep);
  };
  auto issue_b = [&](int st, int so) {
    ldsp sb = lds + so;
    dma16(r2, sb + d5 + 2048, v[7], st * mstep);
#pragma unroll
    for (int j = 0; j < 4; ++j) dma16(r0, sb + d0 + j * 1024, v[j], st * wstep);
    dma16(r1, sb + d4, v[4], st * wstep);
  };
  auto rot = [&](int j) { const int t = k0 + j; return t >= NST1 ? t - NST1 : t; };
#pragma unroll
  for (int j = 0; j < 2; ++j) dma16(r2, lds + d5 + j * 1024, v[5 + j], k0 * mstep);
  issue_b(k0, 0);
  FST(1);
  issue_a(rot(1), SLOT);
  FST(2);
  WAIT_VM(7);
  FST(3);
  BARRIER();
  issue_b(rot(1), SLOT); issue_a(rot(2), 2 * SLOT);
  WAIT_VM(7);
  BARRIER();
  issue_b(rot(2), 2 * SLOT); issue_a(rot(3), 0); issue_b(rot(3), 0);
  WAIT_VM(13);
  if (tb) {
#pragma unroll
    for (int j = 1; j < NST1 - 3; ++j) {
      BARRIER();
      issue_a(rot(j + 3), (j % 3) * SLOT); issue_b(rot(j + 3), (j % 3) * SLOT);
#pragma unroll
      for (int i = 5 * (j - 1); i < 5 * j; ++i) pre[i] = pre_load(src, i);
      SCHED_FENCE();
      if (j == 1) WAIT_VM(18); else WAIT_VM(23);
    }
    BARRIER();
    gap();
    WAIT_VM(5);
  } else {
    int so = SLOT;
#pragma unroll 1
    for (int j = 1; j < NST1 - 3; ++j) {
      BARRIER();
      issue_a(rot(j + 3), so); issue_b(rot(j + 3), so);
      so = so == 2 * SLOT ? 0 : so + SLOT;
      WAIT_VM(13);
    }
    BARRIER();
    gap();
    WAIT_VM(0);
  }
  BARRIER();
}

struct Warm {
  unsigned sink;
  __device__ __forceinline__ Warm(ldsp lds) : sink((unsigned)(size_t)(lds + M_DUMMY)) {}
  __device__ __forceinline__ void touch(const GAS char* t) {
    unsigned keep;
    asm volatile("s_mov_b32 %0, m0\n\ts_mov_b32 m0, %2\n\ts_nop 0\n\tglobal_load_lds_dword %1, off\n\ts_mov_b32 m0, %0" : "=&s"(keep) : "v"(t), "s"(sink) : "memory");
  }
};

struct FragQ { bf16x8 w[5], x[2]; };
__device__ __forceinline__ void p1_main_q(ldsp lds, int w, f32x16 (&acc)[5][2]) {
  const int l = threadIdx.x & 63, l31 = l & 31, h = l >> 5;
  const int xa = (64 * w + l31) * 128 + ((h ^ ((l31 >> 1) & 7)) << 4);
  const int wa = 32768 + (8 * h + ((l & 15) >> 2)) * 320 + ((l >> 4) & 1) * 32 + (l & 3) * 8;
  auto load = [&](FragQ& f, ldsp sb, int ks) {
    ldsp xp = sb + (xa ^ (ks << 5));
    ldsp wp = sb + wa + ks * 16 * 320;
    f.x[0] = lds_b128(xp);
    f.w[0] = lds_tr2(wp, wp + 1280);
    f.x[1] = lds_b128(xp + 4096);
#pragma unroll
    for (int n = 1; n < 5; ++n) f.w[n] = lds_tr2(wp + n * 64, wp + n * 64 + 1280);
  };
  auto mma = [&](const FragQ& f) {
#pragma unroll
    for (int n = 0; n < 5; ++n)
#pragma unroll
      for (int s = 0; s < 2; ++s) acc[n][s] = mfma32(f.w[n], f.x[s], acc[n][s]);
#pragma unroll
    for (int i = 0; i < 6; ++i) { SG_MFMA(1); SG_DS(2); }
    SG_MFMA(4);
    SCHED_FENCE();
  };
  FST(0);
  BARRIER();
  FST(1);
  FragQ f0, f1;
  int so = 0;
  load(f0, lds, 0);
  SCHED_FENCE();
#pragma unroll 1
  for (int j = 0; j < NST1; ++j) {
    ldsp sb = lds + so;
    load(f1, sb, 1); mma(f0);
    load(f0, sb, 2); mma(f1);
    load(f1, sb, 3); mma(f0);
    so = so == 2 * SLOT ? 0 : so + SLOT;
    WAIT_LGKM0();
    if (j < NST1 - 1) BARRIER();
    FSTD(2 + j);
    SCHED_FENCE();
    load(f0, lds + so, 0); mma(f1);
  }
  WAIT_LGKM0();
}

template <int TY>
__device__ __forceinline__ void p1_main16(ldsp lds, int w, f32x4 (&acc)[4][10], Diag& dg) {
  constexpr bool TB = TY == T_GLU;
  const int l = threadIdx.x & 63, l15 = l & 15, g = l >> 4, q = l15 >> 2;
  const int xa = ((TB ? 0 : 64 * w) + l15) * 128 + ((g ^ ((l15 >> 1) & 7)) << 4);
  int wa0, wa1;
  if (TB) {
    wa0 = 20480 + (8 * g + q) * 256 + (((2 * w) ^ (q + 4 * (g & 1))) << 5) + (l & 3) * 8;
    wa1 = wa0 ^ 32;
  } else {
    wa0 = 32768 + (8 * g + q) * 320 + (l & 3) * 8 + 32 * (g & 1);
    wa1 = wa0 - 64 * (g & 1);
  }
  auto ldx = [&](ldsp sb, int u, int t) { return lds_b128(sb + (xa ^ (u << 6)) + t * 2048); };
  auto ldw = [&](ldsp sb, int u, int t) {
    ldsp a = sb + ((t & 1) ? wa1 : wa0) + (TB ? u * 8192 + (t >> 1) * 16384 : u * 10240 + t * 32);
    return lds_tr2(a, a + (TB ? 1024 : 1280));
  };
  auto lda = [&](ldsp sb, int u, int i) { return TB ? ldw(sb, u, i) : ldx(sb, u, i); };
  auto ldb = [&](ldsp sb, int u, int j) { return TB ? ldx(sb, u, j) : ldw(sb, u, j); };
  auto mma = [&](const bf16x8 (&a)[4], const bf16x8& b, int j) {
#pragma unroll
    for (int i = 0; i < 4; ++i) acc[i][j] = mfma16(a[i], b, acc[i][j]);
    SG_MFMA(1); SG_DS(2); SG_MFMA(1); SG_DS(2); SG_MFMA(1); SG_DS(2); SG_MFMA(1);
    SCHED_FENCE();
  };
  FST(0);
  BARRIER();
  FST(1);
  DT(dg.t1);
  bf16x8 a0[4], a1[4], b0[10], b1[10];
  int so = 0;
#pragma unroll
  for (int i = 0; i < 4; ++i) a0[i] = lda(lds, 0, i);
#pragma unroll
  for (int j = 0; j < 5; ++j) b0[j] = ldb(lds, 0, j);
  SCHED_FENCE();
#pragma unroll 1
  for (int st = 0; st < NST1; ++st) {
    ldsp sb = lds + so;
#pragma unroll
    for (int j = 0; j < 10; ++j) {
      if (j < 5) b0[j + 5] = ldb(sb, 0, j + 5); else b1[j - 5] = ldb(sb, 1, j - 5);
      if (j >= 2 && j < 6) a1[j - 2] = lda(sb, 1, j - 2);
      mma(a0, b0[j], j);
    }
#pragma unroll
    for (int j = 0; j < 6; ++j) {
      if (j < 5) b1[j + 5] = ldb(sb, 1, j + 5);
      mma(a1, b1[j], j);
    }
    so = so == 2 * SLOT ? 0 : so + SLOT;
    sb = lds + so;
    WAIT_LGKM0();
    if (st < NST1 - 1) BARRIER(); else lds_arrive(lds + M_END);
    FSTD(2 + st);
    SCHED_FENCE();
    a0[0] = lda(sb, 0, 0); b0[0] = ldb(sb, 0, 0); a0[1] = lda(sb, 0, 1); mma(a1, b1[6], 6);
    a0[2] = lda(sb, 0, 2); a0[3] = lda(sb, 0, 3); b0[1] = ldb(sb, 0, 1); mma(a1, b1[7], 7);
    b0[2] = ldb(sb, 0, 2); b0[3] = ldb(sb, 0, 3); mma(a1, b1[8], 8);
    b0[4] = ldb(sb, 0, 4); mma(a1, b1[9], 9);
  }
  WAIT_LGKM0();
}

__device__ __forceinline__ void p2_main(const Params& p, ldsp lds, int w, int rt, int ct, u64 tag, const u32x4 (&held)[15], Diag& dg) {
  constexpr int WL = 1, LAG = 3, AW = P2_AW, NR = 17, RH = 12, GATE = 10;
  const int l = threadIdx.x & 63, l15 = l & 15, kg = l >> 4, q = l15 >> 2;
  const int sg = w & 1, kq = w >> 1, hf = w >> 2;
  const int rlo = rt * 96, rhi = min(rlo + 95, S - 1);
  const rsrc_t ra = mk_rsrc(p.ws, A2_BYTES);
  const unsigned av = (unsigned)(((rt * 6 + sg * 3) * KB_ALL + kq) * 1024 + l * 16);
  const int rh = 4 * kg + q, ro = 16 * (kg >> 1) + 4 * (kg & 1) + q;
  const int wh = rh * 128 + (((rh >> 1) & 3) << 5) + (l & 3) * 8;
  const int wo = ro * 128 + (((ro >> 1) & 3) << 5) + (l & 3) * 8;
  ldsp hdst = lds + ((w & 3) * 1024 + hf * 4096 + l * 16);
  f32x4 acc[4][3];
#pragma unroll
  for (int n = 0; n < 4; ++n)
#pragma unroll
    for (int s = 0; s < 3; ++s) acc[n][s] = f32x4{};
  u32x4 a[NR][3];
  bf16x8 wf[WL + 1][4];
  float gi[3];
  auto lda = [&](int t) {
#pragma unroll
    for (int s = 0; s < 3; ++s) {
      a[t][s] = __builtin_amdgcn_raw_buffer_load_b128(ra, av + s * KB_ALL * 1024, t * 4096, A2AUX);
    }
  };
  ldsp wb[4];
  auto ldw = [&](int t) {
    const int o0 = w2_off(4 * t), r0 = o0 & ~0x7FFF;
    auto straight = [](int u) { return w2_off(4 * u + 3) == w2_off(4 * u) + 3 * 4096; };
    if (t == 0 || t == RH || r0 != (w2_off(4 * t - 4) & ~0x7FFF) || !straight(t) || !straight(t - 1)) {
      int rel = kq * 4096;
      if (!straight(t)) rel = kq == 0 ? 0 : kq == 1 ? w2_off(4 * t + 1) - o0 : kq == 2 ? w2_off(4 * t + 2) - o0 : w2_off(4 * t + 3) - o0;
#pragma unroll
      for (int n = 0; n < 4; ++n) {
        wb[n] = lds + (((t < RH ? wh : wo) ^ ((n ^ kq) << 5)) + r0 + rel);
        asm volatile("" : "+v"(wb[n]));
      }
    }
#pragma unroll
    for (int n = 0; n < 4; ++n) wf[t % (WL + 1)][n] = lds_tr2(wb[n] + (o0 - r0), wb[n] + (o0 - r0) + (t < RH ? 2048 : 1024));
  };
  if (w == 4) poll(flag_ptr(p, (l & 32) ? FL_G + (((l & 1) ? rhi : rlo) >> 9) : FL_H + ((l & 16) ? rhi : rlo) / 160 * 12 + min(l15, 11)), tag);
  const unsigned cnt = (unsigned)(size_t)(lds + M_RND);
  auto arrive = [&](int r) {
    asm volatile("s_mov_b64 exec, 1\n\tds_add_u32 %0, %1 offset:%2\n\ts_mov_b64 exec, -1" ::"v"(cnt), "v"(1u), "i"(4 * r) : "memory");
  };
  auto counted = [](int r) { return (r >= 0 && r < GATE - LAG) || r == GATE || r == RH - 1; };
  unsigned peek = 0;
  auto check = [&](int r) {
    unsigned sv, n;
    asm volatile(
        "s_nop 0\n\tv_readfirstlane_b32 %0, %2\n\ts_cmp_ge_u32 %0, 8\n\ts_cbranch_scc1 .Lok%=\n\ts_mov_b32 %1, 0x100000\n"
        ".Lagain%=:\n\tds_read_b32 %2, %3 offset:%4\n\ts_waitcnt lgkmcnt(0)\n\tv_readfirstlane_b32 %0, %2\n\ts_sub_u32 %1, %1, 1\n\t"
        "s_cbranch_scc1 .Lok%=\n\ts_cmp_ge_u32 %0, 8\n\ts_cbranch_scc0 .Lagain%=\n.Lok%=:"
        : "=&s"(sv), "=&s"(n), "+v"(peek) : "v"(cnt), "i"(4 * r) : "memory", "scc");
  };
  auto prio = [](int v) {
    if (v >= 3) __builtin_amdgcn_s_setprio(3); else if (v == 2) __builtin_amdgcn_s_setprio(2);
    else if (v == 1) __builtin_amdgcn_s_setprio(1); else __builtin_amdgcn_s_setprio(0);
  };
  auto upto = [](int t) {
    if (t < GATE) return min(min(t + 1 + AW, P2_A0 + P2_RAMP * (t + 1)), RH);
    return min(RH + P2_G0 + P2_G1 * (t - GATE), NR);
  };
  BARRIER();
  DT(dg.t3);
  STAMP(4);
  {
    const rsrc_t rg = mk_rsrc(p.ws + G_OFF, G_BYTES);
#pragma unroll
    for (int t = 0; t < upto(-1); ++t) {
      prio(3 - t); lda(t);
      if (t == 0) {
        ldw(0);
#pragma unroll
        for (int s = 0; s < 3; ++s) gi[s] = __builtin_bit_cast(float, __builtin_amdgcn_raw_buffer_load_b32(rg, (rt * 96 + sg * 48 + s * 16 + l15) * 4, 0, SC1));
      }
      SCHED_FENCE();
    }
  }
#pragma unroll
  for (int t = 0; t < NR; ++t) {
    if (t == GATE) {
      if (w == 7) poll(flag_ptr(p, FL_O + (((l & 4) ? rhi : rlo) >> 8) * 4 + (l & 3)), tag);
      WAIT_LGKM0();
      FST(16);
      BARRIER();
      DT(dg.t4);
      SCHED_FENCE();
    }
    if (counted(t - LAG)) check(t - LAG);
    if (counted(t + 1 - LAG)) peek = __hip_atomic_load((LAS unsigned*)(lds + M_RND + 4 * (t + 1 - LAG)), __ATOMIC_RELAXED, __HIP_MEMORY_SCOPE_WORKGROUP);
    SCHED_FENCE();
    prio(3 - (t & 3));
#pragma unroll
    for (int u = upto(t - 1); u < upto(t); ++u) lda(u);
    if (t + 1 < NR) ldw(t + 1);
    if (t >= LAG) {
#pragma unroll
      for (int i = 2 * (t - LAG); i < min(2 * (t - LAG) + 2, 15); ++i) {
        *(LAS u32x4*)(hdst + w2_off(39 + 2 * i)) = held[i];
      }
    }
    if (t == RH) {
#pragma unroll
      for (int n = 0; n < 4; ++n)
#pragma unroll
        for (int s = 0; s < 3; ++s) acc[n][s] *= 1.f - gi[s];
    }
#pragma unroll
    for (int s = 0; s < 3; ++s)
#pragma unroll
      for (int n = 0; n < 4; ++n) {
        acc[n][s] = mfma16(wf[t % (WL + 1)][n], __builtin_bit_cast(bf16x8, a[t][s]), acc[n][s]);
      }
    SG_VM(9);
    SG_MFMA(1); SG_DS(2); SG_MFMA(1); SG_DS(2); SG_MFMA(1); SG_DS(2); SG_MFMA(1); SG_DS(2);
    SG_MFMA(2); SG_DW(1); SG_MFMA(2); SG_DW(1); SG_MFMA(2); SG_DW(1); SG_MFMA(1); SG_DW(1); SG_MFMA(1);
    asm volatile("" : "+v"(acc[0][0]), "+v"(acc[1][0]), "+v"(acc[2][0]), "+v"(acc[3][0]), "+v"(acc[0][1]), "+v"(acc[1][1]), "+v"(acc[2][1]), "+v"(acc[3][1]),
                 "+v"(acc[0][2]), "+v"(acc[1][2]), "+v"(acc[2][2]), "+v"(acc[3][2]));
    if (counted(t)) arrive(t);
    SCHED_FENCE();
  }
  STAMP(5);
  constexpr int XB = 16384;
  static_assert(w2_off(30) == XB && w2_off(39) == XB + 9 * 4096, "ring layout");
  {
    ldsp wp = lds + (XB + w * 9 * 1024 + l * 16);
#pragma unroll
    for (int d = 1; d < 4; ++d)
#pragma unroll
      for (int s = 0; s < 3; ++s) {
        *(LAS f32x4*)(wp + ((d - 1) * 3 + s) * 1024) = acc[d][s];
      }
  }
  const rsrc_t rout = mk_rsrc(p.out, S * D * 2);
  unsigned vo = (unsigned)((rt * 96 + sg * 48 + l15) * 1280 + (ct * 64 + kq * 16 + 4 * kg) * 2);
  asm volatile("" : "+v"(vo));
  WAIT_LGKM0();
  BARRIER();
  f32x4 part[3][4];
#pragma unroll
  for (int s = 0; s < 3; ++s)
#pragma unroll
    for (int d = 1; d < 4; ++d) part[s][d] = *(LAS f32x4*)(lds + XB + ((w ^ (2 * d)) * 9 + (d - 1) * 3 + s) * 1024 + l * 16);
  SCHED_FENCE();
#pragma unroll
  for (int s = 0; s < 3; ++s) {
    const f32x4 v = (acc[0][s] + part[s][1]) + (part[s][2] + part[s][3]);
    __builtin_amdgcn_raw_buffer_store_b64(u32x2{pk2(v[0], v[1]), pk2(v[2], v[3])}, rout, vo + s * 16 * 1280, 0, OUT_AUX);
  }
  {
    u64 te; DT(te);
    if (te - dg.t0 > DIAG_THR && w == 0 && l == 0) {
      const u64 d = DIAG == 0 ? te - dg.t0 : DIAG == 1 ? dg.t1 - dg.t0 : DIAG == 2 ? dg.t2 - dg.t0 : DIAG == 3 ? dg.t3 - dg.t0 : dg.t4 - dg.t0;
      WAIT_VM(0);
      __builtin_amdgcn_raw_buffer_store_b64(u32x2{pk2((float)(d / 25), 0.f), 0u}, rout, vo, 0, 0);
    }
  }
}

__global__ __launch_bounds__(512, 2) void fused(Params p) {
  extern __shared__ __attribute__((aligned(16))) char smem[];
  ldsp lds = (ldsp)smem;
  Diag dg;
  DT(dg.t0); dg.t1 = dg.t2 = dg.t3 = dg.t4 = dg.t0;
  u64 pc;
  asm volatile("s_getpc_b64 %0" : "=s"(pc));
  constexpr unsigned rest = CODE_BYTES;
  const int b = blockIdx.x, xcd = b & 7, idx = b >> 3, cg = xcd & 3, rg = xcd >> 2;
  const int w = __builtin_amdgcn_readfirstlane(threadIdx.x >> 6), lw = w - 4;
  const int l = threadIdx.x & 63;
  FINIT();
  STAMP(0);
  const int tm = (11 - idx) >> 31;
  const int jj = idx + ((20 * rg - 12) & tm), j3 = (jj * 43) >> 7, jr = jj - 3 * j3;
  const bool spare = jj == 39;
  const int ty = tm ? T_GLU : (jr == 0 ? T_Q : T_KV);
  const int rt = j3 + (4 * rg & ~tm);
  const int ctn = 2 * cg + jr - 1 + ((cg + 1) & tm);
  const int t2 = 28 * xcd - ((xcd - 4) & -rg) + ((idx - 4) & tm) + ((2 * j3 + jr - 1) & ~tm);
  const int rt2 = (t2 * 205) >> 11, ct2 = t2 - 10 * rt2;
  const int rot0 = ROT;
  auto get_tag = [&]() {
    u64 e;
    asm volatile("s_load_dwordx2 %0, %1, 0x0\n\ts_waitcnt lgkmcnt(0)" : "=s"(e) : "s"(p.ws + EPOCH_OFF) : "memory");
    return make_tag(e);
  };

  constexpr int PQX = 2 * SLOT;
  const bool loader = w >= 4 && !spare;
  Warm wm(lds);
  u64 tag;
  auto mfma_start = [&]() {
    if (idx < 2) {
      const unsigned o = (idx * 256 + w * 64 + l) * 128;
      wm.touch((const GAS char*)pc + (o < rest ? o : 0u));
    }
    if (threadIdx.x < 32) *(LAS unsigned*)(lds + M_CNT + threadIdx.x * 4) = 0u;
    tag = get_tag();
  };
  const W2Src src(p, w & 3, ct2);
  if (__builtin_expect(ty == T_Q, 0)) {
    const int ct = cg;
    bf16x8 pq[10];
    if (loader) {
      u32x4 none[30];
      p1_loader<true>(p, lds, lw, 0, rt, ct, rot0, src, none, [&]() {
        {
          const unsigned o = ((j3 * 4 + lw) * 24 + min(l, 23)) * 128;
          wm.touch((const GAS char*)pc + (o < rest ? o : 0u));
        }
        tag = get_tag();
        for (int i = lw * 64 + l; i < 2 * KVT_SZ / 16; i += 256)
          asm volatile("ds_write_b128 %0, %1" ::"v"((unsigned)(size_t)(lds + KVT) + i * 16), "v"(u32x4{0u, 0u, 0u, 0u}) : "memory");
        if (lw == 0) poll(flag_ptr(p, FL_KV + (l & 7) * 8 + 2 * ct + ((l >> 3) & 1)), tag);
        WAIT_LGKM0();
      });
      poll(flag_ptr(p, FL_G + (rt >> 1)), tag);
      const int tl = lw * 64 + l;
      *(LAS unsigned*)(lds + M_G + tl * 4) = __builtin_amdgcn_raw_buffer_load_b32(mk_rsrc(p.ws + G_OFF, G_BYTES), (rt * 256 + tl) * 4, 0, SC1);
      lds_wait(lds + M_END, 4);
#pragma unroll
      for (int t = 0; t < 10; ++t) { pq[t] = lds_b128(lds + PQX + (lw * 10 + t) * 1024 + l * 16); asm volatile("" : "+v"(pq[t])); }
    } else {
    mfma_start();
    f32x16 acc[5][2];
#pragma unroll
    for (int n = 0; n < 5; ++n) { acc[n][0] = f32x16{}; acc[n][1] = f32x16{}; }
    p1_main_q(lds, w, acc);
    STAMP(1);
    const Consts cn(1.f);
#pragma unroll
    for (int k = 9; k >= 0; --k)
#pragma unroll
      for (int s = 0; s < 2; ++s) {
        const f32x16& c = acc[k >> 1][s];
        const int o = 8 * (k & 1);
        const bf16x8 f = pack44(phi4(f32x4{c[o], c[o + 1], c[o + 2], c[o + 3]}, cn), phi4(f32x4{c[o + 4], c[o + 5], c[o + 6], c[o + 7]}, cn));
        if (k >= 5) *(LAS bf16x8*)(lds + PQX + (w * 10 + 5 * s + k - 5) * 1024 + l * 16) = f;
        else { pq[5 * s + k] = f; asm volatile("" : "+v"(pq[5 * s + k])); }
        if (k == 5 && s == 1) { WAIT_LGKM0(); lds_arrive(lds + M_END); }
      }
    STAMP(2);
    }
    {
      const int ch = min((int)threadIdx.x, 419), hg = ch >= 210;
      ldsp dp[2];
      bool dup[2];
#pragma unroll
      for (int u = 0; u < 2; ++u) {
        const int un = 2 * (ch - 210 * hg) + u, row = un / 5, dq = un % 5, hd = row < 80 ? row / 20 : row - 80, run = 5 * hd + dq;
        dp[u] = lds + KVT + hg * KVT_SZ + row * KS + (run >> 2) * 32 + (run & 1) * 16 + (run & 2) * 4;
        dup[u] = row >= 80;
      }
      const rsrc_t rk = mk_rsrc(p.ws + KVP_OFF, KVP_BYTES);
      u32x4 pv[8];
      unsigned todo = 0xFFu;
#pragma unroll 1
      for (int spin = 0; spin < SPIN_MAX && todo; ++spin) {
#pragma unroll
        for (int r = 0; r < 8; ++r)
          if (todo >> r & 1) pv[r] = __builtin_amdgcn_raw_buffer_load_b128(rk, 2 * ct * 3360 + ch * 16, r * 8 * 3360, SC1);
#pragma unroll
        for (int r = 0; r < 8; ++r)
          if (__all(max(max(pv[r][0], pv[r][1]), max(pv[r][2], pv[r][3])) != UNSET)) todo &= ~(1u << r);
        asm volatile("s_sleep 1" ::: "memory");
      }
      STAMP(3);
      f32x2 sum[4];
#pragma unroll
      for (int e = 0; e < 4; ++e) sum[e] = f32x2{0.f, 0.f};
#pragma unroll
      for (int r = 0; r < 8; ++r)
#pragma unroll
        for (int e = 0; e < 4; ++e) {
          const unsigned x = pv[r][e];
          sum[e] += f32x2{__uint_as_float(x << 16), __uint_as_float(x & 0xFFFF0000u)};
        }
#pragma unroll
      for (int u = 0; u < 2; ++u) {
        const u32x2 v = {pk2(sum[2 * u][0], sum[2 * u][1]), pk2(sum[2 * u + 1][0], sum[2 * u + 1][1])};
        *(LAS u32x2*)dp[u] = v;
        if (dup[u]) *(LAS u32x2*)(dp[u] + 4 * KS) = v;
      }
      WAIT_LGKM0();
      BARRIER();
      STAMP(4);
    }
  {
    const int l31 = l & 31, h = l >> 5, hg = w >> 2, wr = w & 3;
    const rsrc_t ra = mk_rsrc(p.ws, A2_BYTES);
    const unsigned vo = (unsigned)(((rt * 16 + wr * 4 + ((l >> 4) & 1)) * KB_ALL + 48 + 5 * ct) * 1024 + hg * 2560 + h * 256 + (l & 15) * 16);
    bf16x8 af[3][5];
#pragma unroll
    for (int et = 2; et >= 0; --et)
#pragma unroll
      for (int t = 0; t < 5; ++t)
        if (!((et == 0 && t > 2) || (et == 1 && t == 0))) af[et][t] = lds_b128(lds + (KVT + hg * KVT_SZ + l31 * KS + h * 16) + 32 * et * KS + t * 32);
    float gv[2];
#pragma unroll
    for (int s = 0; s < 2; ++s) gv[s] = *(LAS float*)(lds + M_G + (64 * wr + 32 * s + l31) * 4);
    FST(18);
    SCHED_FENCE();
    f32x16 acc[2][3];
#pragma unroll
    for (int s = 0; s < 2; ++s)
#pragma unroll
      for (int et = 2; et >= 0; --et) {
        acc[s][et] = f32x16{};
#pragma unroll
        for (int t = 0; t < 5; ++t)
          if (!((et == 0 && t > 2) || (et == 1 && t == 0))) acc[s][et] = mfma32(af[et][t], pq[5 * s + t], acc[s][et]);
      }
#pragma unroll
    for (int s = 0; s < 2; ++s) {
      f32x4 sc;
#pragma unroll
      for (int hd = 0; hd < 4; ++hd) sc[hd] = gv[s] * __builtin_amdgcn_rcpf(acc[s][2][8 + hd] + 1e-6f);
#pragma unroll
      for (int pc_ = 4; pc_ >= 0; --pc_) {
        f32x4 t[2];
#pragma unroll
        for (int gq = 0; gq < 2; ++gq) {
          const int e0 = 16 * pc_ + 8 * gq, o = 8 * (pc_ & 1) + 4 * gq;
          float fa = sc[e0 / 20], fb = sc[(e0 + 4) / 20];
          if (e0 / 20 != (e0 + 4) / 20) { asm volatile("" : "+v"(fa), "+v"(fb)); fa = h ? fb : fa; }
          const f32x16& c = acc[s][pc_ >> 1];
          const f32x2 lo = f32x2{c[o], c[o + 1]} * f32x2{fa, fa}, hi = f32x2{c[o + 2], c[o + 3]} * f32x2{fa, fa};
          t[gq] = f32x4{lo[0], lo[1], hi[0], hi[1]};
        }
        st128_sc1(ra, vo + s * 2 * KB_ALL * 1024 + pc_ * 512, __builtin_bit_cast(u32x4, pack44(t[0], t[1])));
      }
    }
    STAMP(5);
    publish(p, lds, FL_O + rt * 4 + ct, tag, 8);
    STAMP(6);
    FDUMP();
    return;
  }
  }
  const int ct = ctn;
  u32x4 held[15];
  if (__builtin_expect(loader, 1)) {
    u32x4 pre[30];
    p1_loader<false>(p, lds, lw, tm, rt, ct, rot0, src, pre, [&]() { tag = get_tag(); });
    src.fill<0, 24>(lds + lw * 1024);
    if (ty == T_KV) {
#pragma unroll
      for (int i = 0; i < 30; ++i) pre[i] = pre_load(src, i);
      SCHED_FENCE();
      BARRIER(); BARRIER();
    } else {
      lds_wait(lds + M_END, 4);
    }
    WAIT_VM(0);
#pragma unroll
    for (int i = 0; i < 15; ++i) *(LAS u32x4*)(lds + (lw * 1024 + l * 16) + w2_off(24 + i)) = pre[i];
#pragma unroll
    for (int i = 0; i < 15; ++i) held[i] = pre[15 + i];
    WAIT_LGKM0();
  } else {
    mfma_start();
    if (ty == T_KV && w == 0) {
      const rsrc_t rk = mk_rsrc(p.ws + KVP_OFF + (rt * 8 + ct) * 3360, 3360);
#pragma unroll
      for (int i = 0; i < 4; ++i) st128_sc1(rk, i * 1024 + l * 16, u32x4{UNSET, UNSET, UNSET, UNSET});
      WAIT_VM(0);
      if (l == 0) __hip_atomic_store(flag_ptr(p, FL_KV + rt * 8 + ct), tag, __ATOMIC_RELAXED, __HIP_MEMORY_SCOPE_AGENT);
    }
    if (spare) {
      BARRIER();
      const int l15 = l & 15, r0 = cg * 512 + w * 64 + (l >> 4);
      const rsrc_t rx = mk_rsrc(p.x, S * D * 2), rgw = mk_rsrc(p.gw, D * 2), rg_ = mk_rsrc(p.ws + G_OFF, G_BYTES);
      u32x4 gw[5], v[2][4][5];
#pragma unroll
      for (int c = 0; c < 5; ++c) gw[c] = __builtin_amdgcn_raw_buffer_load_b128(rgw, l15 * 16 + c * 256, 0, 0);
      auto ld = [&](int it) {
#pragma unroll
        for (int u = 0; u < 4; ++u)
#pragma unroll
          for (int c = 0; c < 5; ++c) v[it & 1][u][c] = __builtin_amdgcn_raw_buffer_load_b128(rx, (r0 + 4 * u) * 1280 + l15 * 16 + c * 256, it * 16 * 1280, 0);
      };
      ld(0);
#pragma unroll
      for (int it = 0; it < 4; ++it) {
        if (it < 3) ld(it + 1);
        SCHED_FENCE();
        float keep = 0.f;
#pragma unroll
        for (int u = 0; u < 4; ++u) {
          float sacc = 0.f;
#pragma unroll
          for (int c = 0; c < 5; ++c)
#pragma unroll
            for (int e = 0; e < 4; ++e) {
              const unsigned xw = v[it & 1][u][c][e], ge = gw[c][e];
              sacc = __builtin_amdgcn_fdot2_f32_bf16(__builtin_bit_cast(bf16x2, xw), __builtin_bit_cast(bf16x2, ge), sacc, false);
            }
          sacc += __shfl_xor(sacc, 1); sacc += __shfl_xor(sacc, 2); sacc += __shfl_xor(sacc, 4); sacc += __shfl_xor(sacc, 8);
          if (l15 == u) keep = sacc;
        }
        if (l15 < 4) __builtin_amdgcn_raw_buffer_store_b32(__builtin_bit_cast(unsigned, sigmoidf(keep)), rg_, (r0 + 16 * it + 4 * l15) * 4, 0, SC1);
        SCHED_FENCE();
      }
      publish(p, lds, FL_G + cg, tag, 8);
      STAMP(6);
      FDUMP();
      if (cg == 0 && w == 0) {
#pragma unroll 1
        for (int i = 0; i < 4; ++i) poll(flag_ptr(p, i * 64 + l), tag);
        if (l == 0) __hip_atomic_store((GAS u64*)(p.ws + EPOCH_OFF), tag, __ATOMIC_RELAXED, __HIP_MEMORY_SCOPE_AGENT);
      }
      return;
    }
      f32x4 acc[4][10];
#pragma unroll
      for (int i = 0; i < 4; ++i)
#pragma unroll
        for (int j = 0; j < 10; ++j) acc[i][j] = f32x4{};
      if (ty == T_GLU) {
        p1_main16<T_GLU>(lds, w, acc, dg);
        DT(dg.t2);
        STAMP(1);
        const Consts cn(-1.f);
        const rsrc_t ra = mk_rsrc(p.ws, A2_BYTES);
        const unsigned vo = (unsigned)((rt * 10 * KB_ALL + ct * 4 + w) * 1024 + l * 16);
#pragma unroll
        for (int j = 0; j < 10; ++j) {
          st128_sc1(ra, vo + j * KB_ALL * 1024, __builtin_bit_cast(u32x4, pack44(glu4(acc[0][j], acc[2][j], cn), glu4(acc[1][j], acc[3][j], cn))));
          SCHED_FENCE();
        }
        STAMP(2);
        publish(p, lds, FL_H + rt * 12 + ct, tag, 4);
      } else {
        int goff[2];
#pragma unroll
        for (int i = 0; i < 2; ++i) {
          const int en = min((int)threadIdx.x + 256 * i, 419), row = en / 5, dq = en % 5;
          const int hd = row < 80 ? row / 20 : row - 80, d0 = 20 * hd + 4 * dq, ti = d0 >> 4, tj = row >> 4;
          const int tile = row >= 80 ? 9 : ti == tj ? ti : (ti == 0 || ti == 4) ? 8 : 4 + ti;
          goff[i] = tile * 1024 + (((d0 & 15) >> 2) * 16 + (row < 80 ? row & 15 : ti)) * 16;
          asm volatile("" : "+v"(goff[i]));
        }
        p1_main16<T_KV>(lds, w, acc, dg);
        DT(dg.t2);
        STAMP(1);
        const Consts cn(1.f);
        const int l15 = l & 15, g = l >> 4;
        ldsp tp = lds + (w * 10240 + l * 16);
        bf16x8 vf[2][5];
#pragma unroll
        for (int u = 0; u < 2; ++u)
#pragma unroll
          for (int j = 0; j < 5; ++j) vf[u][j] = pack44(acc[2 * u][5 + j], acc[2 * u + 1][5 + j]);
        f32x4 z = {}, dg[5], lo[5], hi[5];
        SCHED_FENCE();
#pragma unroll
        for (int i = 0; i < 5; ++i) {
          const unsigned o1 = l15 == i ? 0x3F803F80u : 0u;
          const bf16x8 one = __builtin_bit_cast(bf16x8, u32x4{o1, o1, o1, o1});
          dg[i] = f32x4{}; lo[i] = f32x4{}; hi[i] = f32x4{};
#pragma unroll
          for (int u = 0; u < 2; ++u) {
            const bf16x8 kf = pack44(phi4(acc[2 * u][i], cn), phi4(acc[2 * u + 1][i], cn));
            if (i > 0) lo[i] = mfma16(kf, vf[u][i - 1], lo[i]);
            if (i < 4) hi[i] = mfma16(kf, vf[u][i + 1], hi[i]);
            dg[i] = mfma16(kf, vf[u][i], dg[i]);
            z = mfma16(kf, one, z);
            if (i > 0) {
              if (u == 0) *(LAS f32x4*)(tp + (i - 1) * 1024) = dg[i - 1];
              else if (i > 1) *(LAS f32x4*)(tp + (3 + i) * 1024) = g < i - 1 ? lo[i - 1] : hi[i - 1];
            }
            SCHED_FENCE();
          }
          if (i == 0) { FST(18); lds_wait(lds + M_END, 4); }
        }
        FST(19);
        *(LAS f32x4*)(tp + 8 * 1024) = l15 < 8 ? hi[0] : lo[4];
        *(LAS f32x4*)(tp + 4 * 1024) = dg[4];
        *(LAS f32x4*)(tp + 9 * 1024) = z;
        WAIT_LGKM0();
        FST(20);
        BARRIER();
        FST(21);
        const rsrc_t rk = mk_rsrc(p.ws + KVP_OFF + (rt * 8 + ct) * 3360, 3360);
#pragma unroll
        for (int i = 0; i < 2; ++i) {
          f32x4 sum = *(LAS f32x4*)(lds + goff[i]);
#pragma unroll
          for (int ww = 1; ww < 4; ++ww) sum += *(LAS f32x4*)(lds + goff[i] + ww * 10240);
          __builtin_amdgcn_raw_buffer_store_b64(u32x2{min(pk2(sum[0], sum[1]), UNSET - 1), min(pk2(sum[2], sum[3]), UNSET - 1)}, rk, threadIdx.x * 8 + i * 2048, 0, SC1);
        }
        WAIT_LGKM0();
        FST(22);
        BARRIER();
        STAMP(2);
      }
      STAMP(3);
#pragma unroll
      for (int i = 0; i < 15; ++i) held[i] = src.load(39 + 2 * i, 0);
      SCHED_FENCE();
  }
  p2_main(p, lds, w, rt2, ct2, tag, held, dg);
  STAMP(6);
  FDUMP();
}

#define STAMP_PTR nullptr
extern "C" void kernel_launch(void* const* d_in, const int* in_sizes, int n_in, void* d_out, int out_size, void* d_ws, size_t ws_size,
                              hipStream_t stream) {
  Params p;
  p.x = (const u16*)d_in[0]; p.wqkv = (const u16*)d_in[1]; p.wo = (const u16*)d_in[2];
  p.wg = (const u16*)d_in[4]; p.wu = (const u16*)d_in[5]; p.wd = (const u16*)d_in[6]; p.gw = (const u16*)d_in[7];
  p.out = (u16*)d_out; p.ws = (char*)d_ws; p.stamps = STAMP_PTR;
  (void)hipFuncSetAttribute((const void*)fused, hipFuncAttributeMaxDynamicSharedMemorySize, LDS_BYTES);
  hipLaunchKernelGGL(fused, dim3(256), dim3(512), LDS_BYTES, stream, p);
}
```

```cpp
#include <hip/hip_runtime.h>
#include <hip/hip_bf16.h>

typedef unsigned short u16;
typedef unsigned long long u64;
typedef __bf16 bf16x8 __attribute__((ext_vector_type(8)));
typedef __bf16 bf16x4 __attribute__((ext_vector_type(4)));
typedef __bf16 bf16x2 __attribute__((ext_vector_type(2)));
typedef float f32x16 __attribute__((ext_vector_type(16)));
typedef float f32x4 __attribute__((ext_vector_type(4)));
typedef unsigned u32x4 __attribute__((ext_vector_type(4)));
typedef unsigned u32x2 __attribute__((ext_vector_type(2)));
#define LAS __attribute__((address_space(3)))
#define GAS __attribute__((address_space(1)))
typedef LAS char* ldsp;
typedef __amdgpu_buffer_rsrc_t rsrc_t;

constexpr int S = 2048, D = 640, F = 1536;
constexpr float LOG2E = 1.4426950408889634f;
constexpr int KB_ALL = 68;
constexpr unsigned A2_BYTES = 128u * KB_ALL * 1024;
constexpr unsigned KVP_OFF = A2_BYTES, KVP_BYTES = 8 * 8 * 3360;
constexpr unsigned G_OFF = KVP_OFF + KVP_BYTES, G_BYTES = S * 4;
constexpr unsigned FLAG_OFF = G_OFF + G_BYTES;
constexpr unsigned EPOCH_OFF = FLAG_OFF + 256 * 128;
constexpr unsigned WS_USED = EPOCH_OFF + 128;
constexpr int FL_H = 0, FL_KV = 156, FL_O = 220, FL_G = 252;
constexpr int SC1 = 16;
constexpr unsigned UNSET = 0xFFFFFFFFu;
constexpr int SLOT = 53248, MISC = 3 * SLOT, LDS_BYTES = 163840;
constexpr int M_DUMMY = MISC, M_CNT = MISC + 256  , M_END = MISC + 260  , M_RND = MISC + 320  , M_TAG = MISC + 384, M_Z = MISC + 512  , M_G = MISC + 2048  ;
constexpr int KS = 80, KVT = SLOT, KVT_SZ = 4 * 32 * KS;
constexpr int NST1 = 10;
constexpr int SPIN_MAX = 1 << 16;
constexpr unsigned CODE_BYTES = 49000;
#define A2AUX SC1
#define GLU_PACE 0
#define OUT_AUX 0
#define P2_AW 3
#define P2_X3 99
#define P2_PRIO 2
#define P2_A0 1
#define P2_RAMP 2
#define P2_G0 2
#define P2_G1 1
#define GATE_HG 3
#define P2_GATE 10
#define P2_POS 0
#define ROT (2 * (cg & 1) + 5 * (((cg >> 1) ^ rg) & 1))
#define W2_ST 4

struct Params {
  const u16 *x, *wqkv, *wo, *wg, *wu, *wd, *gw;
  u16* out;
  char* ws;
  u64* stamps;
};

#define STAMP(i) do {} while (0)
#define FST(i) do {} while (0)
#define FSTD(i) do {} while (0)
#define FINIT() do {} while (0)
#define FDUMP() do {} while (0)

#define WAIT_VM(n) asm volatile("s_waitcnt vmcnt(" #n ")" ::: "memory")
#define WAIT_LGKM0() asm volatile("s_waitcnt lgkmcnt(0)" ::: "memory")
#define BARRIER() do { asm volatile("" ::: "memory"); __builtin_amdgcn_s_barrier(); asm volatile("" ::: "memory"); } while (0)
#define SCHED_FENCE() __builtin_amdgcn_sched_barrier(0)
#define ORDER_FENCE() do { asm volatile("" ::: "memory"); __builtin_amdgcn_sched_barrier(0); } while (0)
#define SG_MFMA(n) __builtin_amdgcn_sched_group_barrier(0x8, n, 0)
#define SG_DS(n) __builtin_amdgcn_sched_group_barrier(0x100, n, 0)
#define SG_VALU(n) __builtin_amdgcn_sched_group_barrier(0x2, n, 0)
#define SG_DW(n) __builtin_amdgcn_sched_group_barrier(0x200, n, 0)
#define SG_VM(n) __builtin_amdgcn_sched_group_barrier(0x20, n, 0)

__device__ __forceinline__ rsrc_t mk_rsrc(const void* p, unsigned bytes) { return __builtin_amdgcn_make_buffer_rsrc((void*)p, 0, bytes, 0x00020000); }
__device__ __forceinline__ void dma16(rsrc_t r, ldsp dst, unsigned voff, unsigned soff) { __builtin_amdgcn_raw_ptr_buffer_load_lds(r, dst, 16, voff, soff, 0, 0); }
__device__ __forceinline__ bf16x8 lds_b128(ldsp a) { return *(LAS bf16x8*)a; }
__device__ __forceinline__ bf16x8 lds_tr2(ldsp a0, ldsp a1) {
  bf16x4 lo = __builtin_amdgcn_ds_read_tr16_b64_v4bf16((LAS bf16x4*)a0);
  bf16x4 hi = __builtin_amdgcn_ds_read_tr16_b64_v4bf16((LAS bf16x4*)a1);
  return __builtin_shufflevector(lo, hi, 0, 1, 2, 3, 4, 5, 6, 7);
}
__device__ __forceinline__ f32x16 mfma32(bf16x8 a, bf16x8 b, f32x16 c) { return __builtin_amdgcn_mfma_f32_32x32x16_bf16(a, b, c, 0, 0, 0); }
__device__ __forceinline__ f32x4 mfma16(bf16x8 a, bf16x8 b, f32x4 c) { return __builtin_amdgcn_mfma_f32_16x16x32_bf16(a, b, c, 0, 0, 0); }
__device__ __forceinline__ unsigned pk2(float a, float b) {
  bf16x2 v = {(__bf16)a, (__bf16)b};
  return __builtin_bit_cast(unsigned, v);
}
__device__ __forceinline__ bf16x8 pack8(const f32x16& c, int o) {
  u32x4 r = {pk2(c[o], c[o + 1]), pk2(c[o + 2], c[o + 3]), pk2(c[o + 4], c[o + 5]), pk2(c[o + 6], c[o + 7])};
  return __builtin_bit_cast(bf16x8, r);
}
__device__ __forceinline__ bf16x8 pack44(const f32x4& a, const f32x4& b) {
  u32x4 r = {pk2(a[0], a[1]), pk2(a[2], a[3]), pk2(b[0], b[1]), pk2(b[2], b[3])};
  return __builtin_bit_cast(bf16x8, r);
}
__device__ __forceinline__ float phi(float t) {
  const float e = __builtin_amdgcn_exp2f(t * LOG2E);
  return __builtin_amdgcn_fmed3f(t, 1e-6f, __builtin_inff()) + __builtin_amdgcn_fmed3f(e, 0.f, 1.f);
}
__device__ __forceinline__ float sigmoidf(float t) { return __builtin_amdgcn_rcpf(1.f + __builtin_amdgcn_exp2f(-LOG2E * t)); }
typedef float f32x2 __attribute__((ext_vector_type(2)));
struct Consts {
  f32x2 l2e;
  float eps, big;
  __device__ __forceinline__ Consts(float sign) : l2e{sign * LOG2E, sign * LOG2E}, eps(1e-6f), big(3.4028234664e38f) {
    asm volatile("" : "+s"(l2e), "+s"(eps), "+v"(big));
  }
};
__device__ __forceinline__ f32x4 phi4(f32x4 t, const Consts& c) {
  const f32x2 a = f32x2{t[0], t[1]} * c.l2e, b = f32x2{t[2], t[3]} * c.l2e;
  f32x2 m0, m1, e0, e1;
#pragma unroll
  for (int i = 0; i < 2; ++i) {
    m0[i] = __builtin_amdgcn_fmed3f(t[i], c.eps, c.big);
    m1[i] = __builtin_amdgcn_fmed3f(t[2 + i], c.eps, c.big);
    e0[i] = __builtin_amdgcn_fmed3f(__builtin_amdgcn_exp2f(a[i]), 0.f, 1.f);
    e1[i] = __builtin_amdgcn_fmed3f(__builtin_amdgcn_exp2f(b[i]), 0.f, 1.f);
  }
  const f32x2 r0 = m0 + e0, r1 = m1 + e1;
  return f32x4{r0[0], r0[1], r1[0], r1[1]};
}
__device__ __forceinline__ f32x4 glu4(f32x4 g, f32x4 u, const Consts& c) {
  f32x2 a = f32x2{g[0], g[1]} * c.l2e, b = f32x2{g[2], g[3]} * c.l2e;
#pragma unroll
  for (int i = 0; i < 2; ++i) { a[i] = __builtin_amdgcn_exp2f(a[i]); b[i] = __builtin_amdgcn_exp2f(b[i]); }
  a += 1.f; b += 1.f;
#pragma unroll
  for (int i = 0; i < 2; ++i) { a[i] = __builtin_amdgcn_rcpf(a[i]); b[i] = __builtin_amdgcn_rcpf(b[i]); }
  a *= f32x2{u[0], u[1]}; b *= f32x2{u[2], u[3]};
  return f32x4{a[0], a[1], b[0], b[1]};
}
__device__ __forceinline__ void st128_sc1(rsrc_t r, unsigned off, u32x4 v) { __builtin_amdgcn_raw_buffer_store_b128(v, r, off, 0, SC1); }

__device__ __forceinline__ u64 make_tag(u64 e) { return ((e ^ 0x9E3779B97F4A7C15ull) * 0xBF58476D1CE4E5B9ull) | 1ull; }
__device__ __forceinline__ GAS u64* flag_ptr(const Params& p, int f) { return (GAS u64*)(p.ws + FLAG_OFF + (size_t)f * 128); }
__device__ __forceinline__ u64 ld_flag(GAS u64* f) { return __hip_atomic_load(f, __ATOMIC_RELAXED, __HIP_MEMORY_SCOPE_AGENT); }
__device__ __forceinline__ void poll(GAS u64* f, u64 tag) {
#pragma unroll 1
  for (int spin = 0; spin < SPIN_MAX; ++spin) {
    if (__all(ld_flag(f) == tag)) break;
    __builtin_amdgcn_s_sleep(2);
  }
  __builtin_amdgcn_fence(__ATOMIC_ACQUIRE, "wavefront");
}
__device__ __forceinline__ void lds_arrive(ldsp c) {
  asm volatile("s_mov_b64 exec, 1\n\tds_add_u32 %0, %1\n\ts_mov_b64 exec, -1" ::"v"((unsigned)(size_t)c), "v"(1u) : "memory");
}
__device__ __forceinline__ void lds_wait(ldsp c, unsigned n) {
#pragma unroll 1
  for (int spin = 0; spin < SPIN_MAX; ++spin) {
    unsigned v;
    asm volatile("ds_read_b32 %0, %1\n\ts_waitcnt lgkmcnt(0)" : "=v"(v) : "v"((unsigned)(size_t)c) : "memory");
    if (__builtin_amdgcn_readfirstlane(v) >= n) break;
    __builtin_amdgcn_s_sleep(1);
  }
}
__device__ __forceinline__ void publish(const Params& p, ldsp lds, int f, u64 tag, unsigned n) {
  WAIT_VM(0);
  if ((threadIdx.x & 63) == 0) {
    const unsigned old = __hip_atomic_fetch_add((LAS unsigned*)(lds + M_CNT), 1u, __ATOMIC_RELAXED, __HIP_MEMORY_SCOPE_WORKGROUP);
    if (old == n - 1) __hip_atomic_store(flag_ptr(p, f), tag, __ATOMIC_RELAXED, __HIP_MEMORY_SCOPE_AGENT);
  }
}

enum { T_Q = 0, T_KV = 1, T_GLU = 2 };

__device__ __forceinline__ constexpr int w2_off(int s) { return (SLOT + (s % 39) * 4096) % (3 * SLOT); }

struct W2Src {
  rsrc_t rd, ro;
  unsigned wv, wv4, ws0, wo0, wo1;
  __device__ __forceinline__ W2Src(const Params& p, int piece, int t2) {
    int l = threadIdx.x & 63;
    t2 = __builtin_amdgcn_readfirstlane(t2);
    asm volatile("" : "+s"(piece), "+s"(t2), "+v"(l));
    const int ct = t2 - 10 * ((t2 * 205) >> 11);
    rd = mk_rsrc(p.wd, F * D * 2);
    ro = mk_rsrc(p.wo, D * D * 2);
    wv = (l >> 3) * 1280 + (((l & 7) ^ ((l >> 4) << 1)) << 4);
    wv4 = wv + (l >> 5) * 36 * 1280;
    ws0 = (unsigned)(8 * piece) * 1280 + ct * 128;
    wo0 = (unsigned)(20 * (piece >> 1) + 8 * (piece & 1)) * 1280 + ct * 128;
    wo1 = (unsigned)(80 * (piece >> 1) + 16 + 20 * (piece & 1)) * 1280 + ct * 128;
  }
  __device__ __forceinline__ u32x4 load(int s) const {
    if (s < 48) return __builtin_amdgcn_raw_buffer_load_b128(rd, wv, ws0 + (unsigned)s * 32 * 1280, 0);
    if (s < 64) return __builtin_amdgcn_raw_buffer_load_b128(ro, wv, wo0 + (unsigned)(s - 48) * 40 * 1280, 0);
    return __builtin_amdgcn_raw_buffer_load_b128(ro, wv4, wo1 + (unsigned)(s - 64) * 160 * 1280, 0);
  }
  template <int S0, int S1> __device__ __forceinline__ void fill(ldsp dst) const {
#pragma unroll
    for (int s = S0; s < S1; ++s) dma16(rd, dst + w2_off(s), wv, ws0 + (unsigned)s * 32 * 1280);
  }
};

__device__ __forceinline__ u32x4 pre_load(const W2Src& src, int i) { return i < 15 ? src.load(24 + i) : src.load(40 + 2 * (i - 15)); }

template <bool ISQ, class Reset, class Raise, class Gap>
__device__ __forceinline__ void p1_loader(const Params& p, ldsp lds, int lw, int tm, int rt, int ct, int k0, int t2, u32x4 (&pre)[30],
                                          Reset reset, Raise raise, Gap gap) {
  const bool tb = !ISQ && tm != 0;
  auto sel = [&](int a, int b) { return ISQ ? b : b ^ ((a ^ b) & tm); };
  auto selp = [&](const u16* a, const u16* b) { return ISQ ? b : (const u16*)((u64)b ^ (((u64)a ^ (u64)b) & (u64)(long long)tm)); };
  const int l = threadIdx.x & 63;
  const int i8 = sel(5 * lw, 8 * lw + 3);
  const unsigned xe = (l >> 3) * 1280 + (((l & 7) ^ (l >> 4)) << 4);
  const unsigned xa = (xe ^ ((i8 & 1) << 6)) + (unsigned)(sel(160, 256) * rt + 8 * i8) * 1280, xb = (xa ^ 64) + 8 * 1280;
  const int d8 = i8 * 1024;
  const rsrc_t rx = mk_rsrc(p.x, S * D * 2);
  FST(0);
#pragma unroll
  for (int j = 0; j < 5; ++j) dma16(rx, lds + d8 + j * 1024, (j & 1) ? xb : xa, k0 * 128 + (j >> 1) * 16 * 1280);
  SCHED_FENCE();
  const rsrc_t r0 = mk_rsrc(selp(p.wg, p.wqkv), sel(D * F * 2, D * 3 * D * 2));
  const rsrc_t r1 = mk_rsrc(selp(p.wu, p.wqkv), sel(D * F * 2, D * 3 * D * 2));
  const rsrc_t r2 = mk_rsrc(selp(p.wu, p.x), sel(D * F * 2, S * D * 2));
  unsigned v[8];
  {
    const int r = l >> 4;
    const unsigned g0 = (16 * lw + r) * 3072 + ct * 256 + (((l & 15) ^ (r << 1)) << 4);
    const int colA = ISQ ? 320 * ct : 1280 + 160 * ct, colB = ISQ ? 160 : 1280;
#pragma unroll
    for (int j = 0; j < 5; ++j) {
      const int c = 64 * j + l, rr = (c * 3277) >> 16;
      int cc = c - 20 * rr;
      if (!ISQ) cc ^= (rr >> 2) & 2;
      const unsigned a = (16 * lw + rr) * 3840 + colA + cc * 16 + (cc < 10 ? 0 : colB - 160);
      v[j] = sel((g0 ^ ((j & 2) << 6)) + (j & 3) * 4 * 3072, a);
    }
    v[5] = sel(v[1], xb - 4 * 8 * 1280);
    v[6] = sel(v[2], xa - 2 * 8 * 1280);
    v[7] = sel(v[3], xb - 2 * 8 * 1280);
  }
  const int d0 = sel(20480 + 4096 * lw, 32768 + 5120 * lw), d4 = d0 + sel(16384, 4096), d5 = sel(d4 + 1024, d8 - 3072);
  const unsigned wstep = sel(64 * 3072, 64 * 3840), mstep = sel(64 * 3072, 128);
  auto issue_a = [&](int st, int so) {
    ldsp sb = lds + so;
#pragma unroll
    for (int j = 0; j < 5; ++j) dma16(rx, sb + d8 + j * 1024, (j & 1) ? xb : xa, st * 128 + (j >> 1) * 16 * 1280);
#pragma unroll
    for (int j = 0; j < 2; ++j) dma16(r2, sb + d5 + j * 1024, v[5 + j], st * mstep);
    ORDER_FENCE();
  };
  auto issue_b = [&](int st, int so) {
    ldsp sb = lds + so;
    dma16(r2, sb + d5 + 2048, v[7], st * mstep);
#pragma unroll
    for (int j = 0; j < 4; ++j) dma16(r0, sb + d0 + j * 1024, v[j], st * wstep);
    dma16(r1, sb + d4, v[4], st * wstep);
    ORDER_FENCE();
  };
  auto rot = [&](int j) { const int t = k0 + j; return t >= NST1 ? t - NST1 : t; };
#pragma unroll
  for (int j = 0; j < 2; ++j) dma16(r2, lds + d5 + j * 1024, v[5 + j], k0 * mstep);
  issue_b(k0, 0);
  FST(1);
  issue_a(rot(1), SLOT);
  FST(2);
  WAIT_VM(7);
  FST(3);
  BARRIER();
  issue_b(rot(1), SLOT); issue_a(rot(2), 2 * SLOT);
  WAIT_VM(7);
  BARRIER();
  issue_b(rot(2), 2 * SLOT); issue_a(rot(3), 0); issue_b(rot(3), 0);
  WAIT_VM(13);
  if (tb) {
    const W2Src src(p, lw, t2);
#pragma unroll
    for (int j = 1; j < NST1 - 3; ++j) {
      BARRIER();
      issue_a(rot(j + 3), (j % 3) * SLOT); issue_b(rot(j + 3), (j % 3) * SLOT);
#pragma unroll
      for (int i = 5 * (j - 1); i < 5 * j; ++i) pre[i] = pre_load(src, i);
      SCHED_FENCE();
      if (j == 1) WAIT_VM(18); else WAIT_VM(23);
    }
    BARRIER();
    gap();
    WAIT_VM(5);
  } else {
    int so = SLOT;
#pragma unroll 1
    for (int j = 1; j < NST1 - 3; ++j) {
      BARRIER();
      if (!ISQ && j == 3) raise();
      issue_a(rot(j + 3), so); issue_b(rot(j + 3), so);
      so = so == 2 * SLOT ? 0 : so + SLOT;
      if (ISQ) {
        if (j < 6) { reset(j - 1); if (j == 1) WAIT_VM(17); else WAIT_VM(21); } else WAIT_VM(13);
      } else {
        if (j == 1) { reset(0); WAIT_VM(14); } else WAIT_VM(13);
      }
    }
    BARRIER();
    if (ISQ) raise();
    gap();
    WAIT_VM(0);
  }
  BARRIER();
}

struct Warm {
  unsigned sink;
  __device__ __forceinline__ Warm(ldsp lds) : sink((unsigned)(size_t)(lds + M_DUMMY)) {}
  __device__ __forceinline__ void touch(const GAS char* t) {
    unsigned keep;
    asm volatile("s_mov_b32 %0, m0\n\ts_mov_b32 m0, %2\n\ts_nop 0\n\tglobal_load_lds_dword %1, off\n\ts_mov_b32 m0, %0" : "=&s"(keep) : "v"(t), "s"(sink) : "memory");
  }
};

__device__ __forceinline__ void stage_barrier(int st) {
  asm volatile("s_waitcnt lgkmcnt(0)\n\ts_cmp_eq_u32 %0, %1\n\ts_cbranch_scc1 1\n\ts_barrier" ::"s"(st), "i"(NST1 - 1) : "memory", "scc");
}
struct FragQ { bf16x8 w[5], x[2]; };
__device__ __forceinline__ void p1_main_q(ldsp lds, int w, f32x16 (&acc)[5][2]) {
  const int l = threadIdx.x & 63, l31 = l & 31, h = l >> 5;
  const int xa = (64 * w + l31) * 128 + ((h ^ ((l31 >> 1) & 7)) << 4);
  const int wa = 32768 + (8 * h + ((l & 15) >> 2)) * 320 + ((l >> 4) & 1) * 32 + (l & 3) * 8;
  auto load = [&](FragQ& f, ldsp sb, int ks) {
    ldsp xp = sb + (xa ^ (ks << 5));
    ldsp wp = sb + wa + ks * 16 * 320;
    f.x[0] = lds_b128(xp);
    f.w[0] = lds_tr2(wp, wp + 1280);
    f.x[1] = lds_b128(xp + 4096);
#pragma unroll
    for (int n = 1; n < 5; ++n) f.w[n] = lds_tr2(wp + n * 64, wp + n * 64 + 1280);
  };
  auto mma = [&](const FragQ& f) {
#pragma unroll
    for (int n = 0; n < 5; ++n)
#pragma unroll
      for (int s = 0; s < 2; ++s) acc[n][s] = mfma32(f.w[n], f.x[s], acc[n][s]);
#pragma unroll
    for (int i = 0; i < 6; ++i) { SG_MFMA(1); SG_DS(2); }
    SG_MFMA(4);
    SCHED_FENCE();
  };
  FST(0);
  BARRIER();
  FST(1);
  FragQ f0, f1;
  int so = 0;
  load(f0, lds, 0);
  SCHED_FENCE();
#pragma unroll 1
  for (int j = 0; j < NST1; ++j) {
    ldsp sb = lds + so;
    load(f1, sb, 1); mma(f0);
    load(f0, sb, 2); mma(f1);
    load(f1, sb, 3); mma(f0);
    so = so == 2 * SLOT ? 0 : so + SLOT;
    stage_barrier(j);
    FSTD(2 + j);
    SCHED_FENCE();
    load(f0, lds + so, 0); mma(f1);
  }
  WAIT_LGKM0();
}

template <int TY>
__device__ __forceinline__ void p1_main16(ldsp lds, int w, f32x4 (&acc)[4][10]) {
  constexpr bool TB = TY == T_GLU;
  const int l = threadIdx.x & 63, l15 = l & 15, g = l >> 4, q = l15 >> 2;
  const int xa = ((TB ? 0 : 64 * w) + l15) * 128 + ((g ^ ((l15 >> 1) & 7)) << 4);
  int wa0, wa1;
  if (TB) {
    wa0 = 20480 + (8 * g + q) * 256 + (((2 * w) ^ (q + 4 * (g & 1))) << 5) + (l & 3) * 8;
    wa1 = wa0 ^ 32;
  } else {
    wa0 = 32768 + (8 * g + q) * 320 + (l & 3) * 8 + 32 * (g & 1);
    wa1 = wa0 - 64 * (g & 1);
  }
  ldsp px0 = lds + xa, px1 = lds + (xa ^ 64), pw0 = lds + wa0, pw1 = lds + wa1;
  auto ldx = [&](int u, int t) { return lds_b128((u ? px1 : px0) + t * 2048); };
  auto ldw = [&](int u, int t) {
    ldsp a = ((t & 1) ? pw1 : pw0) + (TB ? u * 8192 + (t >> 1) * 16384 : u * 10240 + t * 32);
    return lds_tr2(a, a + (TB ? 1024 : 1280));
  };
  auto lda = [&](int u, int i) { return TB ? ldw(u, i) : ldx(u, i); };
  auto ldb = [&](int u, int j) { return TB ? ldx(u, j) : ldw(u, j); };
  auto mma = [&](const bf16x8 (&a)[4], const bf16x8& b, int j) {
#pragma unroll
    for (int i = 0; i < 4; ++i) acc[i][j] = mfma16(a[i], b, acc[i][j]);
    SG_MFMA(1); SG_DS(2); SG_MFMA(1); SG_DS(2); SG_MFMA(1); SG_DS(2); SG_MFMA(1);
    SCHED_FENCE();
  };
  FST(0);
  BARRIER();
  FST(1);
  bf16x8 a0[4], a1[4], b0[10], b1[10];
#pragma unroll
  for (int i = 0; i < 4; ++i) a0[i] = lda(0, i);
#pragma unroll
  for (int j = 0; j < 5; ++j) b0[j] = ldb(0, j);
  SCHED_FENCE();
  int ph = 0;
#pragma unroll 1
  for (int st = 0; st < NST1; ++st) {
#pragma unroll
    for (int j = 0; j < 10; ++j) {
      if (j < 5) b0[j + 5] = ldb(0, j + 5); else b1[j - 5] = ldb(1, j - 5);
      if (j >= 2 && j < 6) a1[j - 2] = lda(1, j - 2);
      mma(a0, b0[j], j);
    }
#pragma unroll
    for (int j = 0; j < 5; ++j) {
      b1[j + 5] = ldb(1, j + 5);
      mma(a1, b1[j], j);
    }
    {
      const int dlt = ph == 2 ? -2 * SLOT : SLOT;
      ph = ph == 2 ? 0 : ph + 1;
      px0 += dlt; px1 += dlt; pw0 += dlt; pw1 += dlt;
      asm volatile("" : "+v"(px0), "+v"(px1), "+v"(pw0), "+v"(pw1));
#pragma unroll
      for (int i = 0; i < 4; ++i) acc[i][5] = mfma16(a1[i], b1[5], acc[i][5]);
      SG_MFMA(1); SG_VALU(2); SG_MFMA(1); SG_VALU(2); SG_MFMA(2);
      SCHED_FENCE();
    }
    stage_barrier(st);
    FSTD(2 + st);
    SCHED_FENCE();
    a0[0] = lda(0, 0); b0[0] = ldb(0, 0); a0[1] = lda(0, 1); mma(a1, b1[6], 6);
    a0[2] = lda(0, 2); a0[3] = lda(0, 3); b0[1] = ldb(0, 1); mma(a1, b1[7], 7);
    b0[2] = ldb(0, 2); b0[3] = ldb(0, 3); mma(a1, b1[8], 8);
    b0[4] = ldb(0, 4); mma(a1, b1[9], 9);
  }
  lds_arrive(lds + M_END);
  WAIT_LGKM0();
}

__device__ __forceinline__ void p2_main(const Params& p, ldsp lds, int w, int rt, int ct, u64 tag, const u32x4 (&held)[15]) {
  constexpr int LAG = 3, AW = P2_AW, NR = 17, RH = 12, GATE = P2_GATE;
  const int l = threadIdx.x & 63, l15 = l & 15, kg = l >> 4, q = l15 >> 2;
  const int sg = w & 1, kq = w >> 1, hf = w >> 2;
  const int rlo = rt * 96, rhi = min(rlo + 95, S - 1);
  const rsrc_t ra = mk_rsrc(p.ws, A2_BYTES);
  const unsigned av = (unsigned)(((rt * 6 + sg * 3) * KB_ALL + kq) * 1024 + l * 16);
  const int rh = 4 * kg + q, ro = 16 * (kg >> 1) + 4 * (kg & 1) + q;
  const int wh = rh * 128 + (((rh >> 1) & 3) << 5) + (l & 3) * 8;
  const int wo = ro * 128 + (((ro >> 1) & 3) << 5) + (l & 3) * 8;
  ldsp hdst = lds + ((w & 3) * 1024 + hf * 4096 + l * 16);
  f32x4 acc[4][3];
#pragma unroll
  for (int n = 0; n < 4; ++n)
#pragma unroll
    for (int s = 0; s < 3; ++s) acc[n][s] = f32x4{};
#define PIN_ACC(text) \
  asm volatile(text : "+v"(acc[0][0]), "+v"(acc[1][0]), "+v"(acc[2][0]), "+v"(acc[3][0]), "+v"(acc[0][1]), "+v"(acc[1][1]), "+v"(acc[2][1]), \
               "+v"(acc[3][1]), "+v"(acc[0][2]), "+v"(acc[1][2]), "+v"(acc[2][2]), "+v"(acc[3][2]))
  PIN_ACC("");
  u32x4 a[NR][3];
  bf16x8 wf[2][4];
  float gi[3];
  auto lda = [&](int t) {
#pragma unroll
    for (int s = 0; s < 3; ++s) {
      a[t][s] = __builtin_amdgcn_raw_buffer_load_b128(ra, av + s * KB_ALL * 1024, t * 4096, A2AUX);
    }
  };
  ldsp wb[4];
  auto ldw = [&](int t) {
    const int o0 = w2_off(4 * t), r0 = o0 & ~0x7FFF;
    auto straight = [](int u) { return w2_off(4 * u + 3) == w2_off(4 * u) + 3 * 4096; };
    if (t == 0 || t == RH || r0 != (w2_off(4 * t - 4) & ~0x7FFF) || !straight(t) || !straight(t - 1)) {
      int rel = kq * 4096;
      if (!straight(t)) rel = kq == 0 ? 0 : kq == 1 ? w2_off(4 * t + 1) - o0 : kq == 2 ? w2_off(4 * t + 2) - o0 : w2_off(4 * t + 3) - o0;
#pragma unroll
      for (int n = 0; n < 4; ++n) {
        wb[n] = lds + (((t < RH ? wh : wo) ^ ((n ^ kq) << 5)) + r0 + rel);
        asm volatile("" : "+v"(wb[n]));
      }
    }
#pragma unroll
    for (int n = 0; n < 4; ++n) wf[t & 1][n] = lds_tr2(wb[n] + (o0 - r0), wb[n] + (o0 - r0) + (t < RH ? 2048 : 1024));
  };
  if (w == 4)
    poll(flag_ptr(p, l < 32 ? FL_H + ((l & 16) ? rhi : rlo) / 160 * 12 + min(l15, 11)
                     : l < 48 ? FL_G + (((l & 1) ? rhi : rlo) >> 9) : FL_O + (((l & 4) ? rhi : rlo) >> 8) * 4 + (l & 3)), tag);
  const unsigned cnt = (unsigned)(size_t)(lds + M_RND);
  auto arrive = [&](int r) {
    asm volatile("s_mov_b64 exec, 1\n\tds_add_u32 %0, %1 offset:%2\n\ts_mov_b64 exec, -1" ::"v"(cnt), "v"(1u), "i"(4 * r) : "memory");
  };
  auto counted = [](int r) { return (r >= 0 && r < GATE - LAG) || (r >= GATE && r < RH); };
  unsigned peek = 0;
  auto check = [&](int r) {
    unsigned sv, n;
    asm volatile(
        "s_nop 0\n\tv_readfirstlane_b32 %0, %2\n\ts_cmp_ge_u32 %0, 8\n\ts_cbranch_scc1 .Lok%=\n\ts_mov_b32 %1, 0x100000\n"
        ".Lagain%=:\n\tds_read_b32 %2, %3 offset:%4\n\ts_waitcnt lgkmcnt(0)\n\tv_readfirstlane_b32 %0, %2\n\ts_sub_u32 %1, %1, 1\n\t"
        "s_cbranch_scc1 .Lok%=\n\ts_cmp_ge_u32 %0, 8\n\ts_cbranch_scc0 .Lagain%=\n.Lok%=:"
        : "=&s"(sv), "=&s"(n), "+v"(peek) : "v"(cnt), "i"(4 * r) : "memory", "scc");
  };
  auto prio = [](int v) {
    if (v >= 3) __builtin_amdgcn_s_setprio(3); else if (v == 2) __builtin_amdgcn_s_setprio(2);
    else if (v == 1) __builtin_amdgcn_s_setprio(1); else __builtin_amdgcn_s_setprio(0);
  };
  auto upto = [](int t) {
    if (t < GATE) return min(min(t + 1 + AW, P2_A0 + P2_RAMP * (t + 1)), RH);
    return min(RH + P2_G0 + P2_G1 * (t - GATE), NR);
  };
  BARRIER();
  STAMP(4);
  {
    const rsrc_t rg = mk_rsrc(p.ws + G_OFF, G_BYTES);
#pragma unroll
    for (int t = 0; t < upto(-1); ++t) {
      prio(3 - t); lda(t);
      if (t == 0) {
        ldw(0);
#pragma unroll
        for (int s = 0; s < 3; ++s) gi[s] = __builtin_bit_cast(float, __builtin_amdgcn_raw_buffer_load_b32(rg, (rt * 96 + sg * 48 + s * 16 + l15) * 4, 0, SC1));
      }
      SCHED_FENCE();
    }
  }
#pragma unroll
  for (int t = 0; t < NR; ++t) {
    if (t == GATE) {
      if (w == 7) {
        const unsigned co = (unsigned)(((rt * 6 + min(l >> 3, 5)) * KB_ALL + 48) * 1024 + (l & GATE_HG) * 2048);
#pragma unroll 1
        for (int spin = 0; spin < SPIN_MAX; ++spin) {
          if (__all(__builtin_amdgcn_raw_buffer_load_b32(ra, co, 0, SC1) != UNSET)) break;
          asm volatile("s_sleep 1" ::: "memory");
        }
      }
      WAIT_LGKM0();
      FST(16);
      BARRIER();
      FST(17);
      SCHED_FENCE();
    }
    if (counted(t - LAG)) check(t - LAG);
    if (counted(t + 1 - LAG)) peek = __hip_atomic_load((LAS unsigned*)(lds + M_RND + 4 * (t + 1 - LAG)), __ATOMIC_RELAXED, __HIP_MEMORY_SCOPE_WORKGROUP);
    SCHED_FENCE();
    prio(3 - (t & 3));
#pragma unroll
    for (int u = upto(t - 1); u < upto(t); ++u) lda(u);
    if (t + 1 < NR) ldw(t + 1);
    if (t >= LAG) {
#pragma unroll
      for (int i = 2 * (t - LAG); i < min(2 * (t - LAG) + 2, 15); ++i) {
        *(LAS u32x4*)(hdst + w2_off(39 + 2 * i)) = held[i];
      }
    }
    if (t >= RH) {
      auto unset = [&]() {
        unsigned m = 0u;
#pragma unroll
        for (int s = 0; s < 3; ++s) m = max(max(m, max(a[t][s][0], a[t][s][1])), max(a[t][s][2], a[t][s][3]));
        return __any(m == UNSET);
      };
      if (__builtin_expect(unset(), 0)) {
        int spin = 0;
#pragma unroll 1
        do { lda(t); __builtin_amdgcn_s_waitcnt(0x0F70); } while (unset() && ++spin < SPIN_MAX);
      }
      SCHED_FENCE();
    }
    if (t == RH) {
      PIN_ACC("s_nop 7");
#pragma unroll
      for (int n = 0; n < 4; ++n)
#pragma unroll
        for (int s = 0; s < 3; ++s) acc[n][s] *= 1.f - gi[s];
    }
    if (t >= RH) PIN_ACC("s_nop 1");
#pragma unroll
    for (int s = 0; s < 3; ++s)
#pragma unroll
      for (int n = 0; n < 4; ++n) {
        asm volatile("v_mfma_f32_16x16x32_bf16 %0, %1, %2, %0" : "+v"(acc[n][s]) : "v"(wf[t & 1][n]), "v"(a[t][s]));
      }
    if (counted(t)) arrive(t);
    SCHED_FENCE();
  }
  PIN_ACC("s_nop 7");
  STAMP(5);
  constexpr int XB = 16384;
  static_assert(w2_off(30) == XB && w2_off(39) == XB + 9 * 4096, "ring layout");
  {
    ldsp wp = lds + (XB + w * 9 * 1024 + l * 16);
#pragma unroll
    for (int d = 1; d < 4; ++d)
#pragma unroll
      for (int s = 0; s < 3; ++s) {
        *(LAS f32x4*)(wp + ((d - 1) * 3 + s) * 1024) = acc[d][s];
      }
  }
  const rsrc_t rout = mk_rsrc(p.out, S * D * 2);
  unsigned vo = (unsigned)((rt * 96 + sg * 48 + l15) * 1280 + (ct * 64 + kq * 16 + 4 * kg) * 2);
  asm volatile("" : "+v"(vo));
  WAIT_LGKM0();
  BARRIER();
  f32x4 part[3][4];
#pragma unroll
  for (int s = 0; s < 3; ++s)
#pragma unroll
    for (int d = 1; d < 4; ++d) part[s][d] = *(LAS f32x4*)(lds + XB + ((w ^ (2 * d)) * 9 + (d - 1) * 3 + s) * 1024 + l * 16);
  SCHED_FENCE();
#pragma unroll
  for (int s = 0; s < 3; ++s) {
    const f32x4 v = (acc[0][s] + part[s][1]) + (part[s][2] + part[s][3]);
    __builtin_amdgcn_raw_buffer_store_b64(u32x2{pk2(v[0], v[1]), pk2(v[2], v[3])}, rout, vo + s * 16 * 1280, 0, OUT_AUX);
  }
}

__global__ __launch_bounds__(512, 2) void fused(Params p) {
  extern __shared__ __attribute__((aligned(16))) char smem[];
  ldsp lds = (ldsp)smem;
  u64 pc;
  asm volatile("s_getpc_b64 %0" : "=s"(pc));
  constexpr unsigned rest = CODE_BYTES;
  const int b = blockIdx.x, xcd = b & 7, idx = b >> 3, cg = xcd & 3, rg = xcd >> 2;
  const int w = __builtin_amdgcn_readfirstlane(threadIdx.x >> 6), lw = w - 4;
  const int l = threadIdx.x & 63;
  FINIT();
  STAMP(0);
  const int tm = (11 - idx) >> 31;
  const int jj = idx + ((20 * rg - 12) & tm), j3 = (jj * 43) >> 7, jr = jj - 3 * j3;
  const bool spare = jj == 39;
  const int ty = tm ? T_GLU : (jr == 0 ? T_Q : T_KV);
  const int rt = j3 + (4 * rg & ~tm);
  const int ctn = 2 * cg + jr - 1 + ((cg + 1) & tm);
  const int t2 = 28 * xcd - ((xcd - 4) & -rg) + ((idx - 4) & tm) + ((2 * j3 + jr - 1) & ~tm);
  const int rot0 = ROT;
  auto get_tag = [&]() {
    u64 e;
    asm volatile("s_load_dwordx2 %0, %1, 0x0\n\ts_waitcnt lgkmcnt(0)" : "=s"(e) : "s"(p.ws + EPOCH_OFF) : "memory");
    return make_tag(e);
  };

  constexpr int PQX = 2 * SLOT;
  const bool loader = w >= 4 && !spare;
  Warm wm(lds);
  u64 tag;
  auto mfma_start = [&]() {
    if (idx < 2) {
      const unsigned o = (idx * 256 + w * 64 + l) * 128;
      wm.touch((const GAS char*)pc + (o < rest ? o : 0u));
    }
    if (w == 0) {
      tag = get_tag();
      if (l < 32) *(LAS unsigned*)(lds + M_CNT + l * 4) = 0u;
      if (l == 0) *(LAS u64*)(lds + M_TAG) = tag;
      WAIT_LGKM0();
    }
  };
  auto lds_tag = [&]() {
    u32x2 v;
    asm volatile("ds_read_b64 %0, %1\n\ts_waitcnt lgkmcnt(0)" : "=v"(v) : "v"((unsigned)(size_t)(lds + M_TAG)) : "memory");
    const unsigned lo = v[0], hi = v[1];
    tag = (u64)(unsigned)__builtin_amdgcn_readfirstlane(hi) << 32 | (unsigned)__builtin_amdgcn_readfirstlane(lo);
  };
  if (__builtin_expect(ty == T_Q, 0)) {
    const int ct = cg;
    bf16x8 pq[10];
    if (loader) {
      u32x4 none[30];
      p1_loader<true>(p, lds, lw, 0, rt, ct, rot0, 0, none, [&](int kb) {
        const rsrc_t ra = mk_rsrc(p.ws, A2_BYTES);
        const unsigned vo = (unsigned)(((rt * 16 + lw * 4) * KB_ALL + 48 + (kb < 4 ? 4 * ct + kb : 16 + ct)) * 1024 + l * 16);
#pragma unroll
        for (int i = 0; i < 4; ++i) st128_sc1(ra, vo + i * KB_ALL * 1024, u32x4{UNSET, UNSET, UNSET, UNSET});
      }, [&]() {
        lds_tag();
        if (lw == 0 && l == 0) __hip_atomic_store(flag_ptr(p, FL_O + rt * 4 + ct), tag, __ATOMIC_RELAXED, __HIP_MEMORY_SCOPE_AGENT);
      }, [&]() {
        {
          const unsigned o = ((j3 * 4 + lw) * 26 + min(l, 25)) * 128;
          wm.touch((const GAS char*)pc + (o < rest ? o : 0u));
        }
        for (int i = lw * 64 + l; i < 2 * KVT_SZ / 16; i += 256)
          asm volatile("ds_write_b128 %0, %1" ::"v"((unsigned)(size_t)(lds + KVT) + i * 16), "v"(u32x4{0u, 0u, 0u, 0u}) : "memory");
        if (lw == 0) poll(flag_ptr(p, FL_KV + (l & 7) * 8 + 2 * ct + ((l >> 3) & 1)), tag);
        WAIT_LGKM0();
      });
      poll(flag_ptr(p, FL_G + (rt >> 1)), tag);
      const int tl = lw * 64 + l;
      *(LAS unsigned*)(lds + M_G + tl * 4) = __builtin_amdgcn_raw_buffer_load_b32(mk_rsrc(p.ws + G_OFF, G_BYTES), (rt * 256 + tl) * 4, 0, SC1);
      lds_wait(lds + M_END, 4);
#pragma unroll
      for (int t = 0; t < 10; ++t) { pq[t] = lds_b128(lds + PQX + (lw * 10 + t) * 1024 + l * 16); asm volatile("" : "+v"(pq[t])); }
    } else {
    mfma_start();
    f32x16 acc[5][2];
#pragma unroll
    for (int n = 0; n < 5; ++n) { acc[n][0] = f32x16{}; acc[n][1] = f32x16{}; }
    p1_main_q(lds, w, acc);
    STAMP(1);
    const Consts cn(1.f);
#pragma unroll
    for (int k = 9; k >= 0; --k)
#pragma unroll
      for (int s = 0; s < 2; ++s) {
        const f32x16& c = acc[k >> 1][s];
        const int o = 8 * (k & 1);
        const bf16x8 f = pack44(phi4(f32x4{c[o], c[o + 1], c[o + 2], c[o + 3]}, cn), phi4(f32x4{c[o + 4], c[o + 5], c[o + 6], c[o + 7]}, cn));
        if (k >= 5) *(LAS bf16x8*)(lds + PQX + (w * 10 + 5 * s + k - 5) * 1024 + l * 16) = f;
        else { pq[5 * s + k] = f; asm volatile("" : "+v"(pq[5 * s + k])); }
        if (k == 5 && s == 1) { WAIT_LGKM0(); lds_arrive(lds + M_END); }
      }
    STAMP(2);
    }
    {
      const int ch = min((int)threadIdx.x, 419), hg = ch >= 210;
      ldsp dp[2];
      bool dup[2];
#pragma unroll
      for (int u = 0; u < 2; ++u) {
        const int un = 2 * (ch - 210 * hg) + u, row = un / 5, dq = un % 5, hd = row < 80 ? row / 20 : row - 80, run = 5 * hd + dq;
        dp[u] = lds + KVT + hg * KVT_SZ + (32 * hd + (row < 80 ? row - 20 * hd : 20)) * KS + ((run >> 2) - hd) * 32 + (run & 1) * 16 + (run & 2) * 4;
        dup[u] = row >= 80;
      }
      const rsrc_t rk = mk_rsrc(p.ws + KVP_OFF, KVP_BYTES);
      u32x4 pv[8];
      unsigned todo = 0xFFu;
#pragma unroll 1
      for (int spin = 0; spin < SPIN_MAX && todo; ++spin) {
#pragma unroll
        for (int r = 0; r < 8; ++r)
          if (todo >> r & 1) pv[r] = __builtin_amdgcn_raw_buffer_load_b128(rk, 2 * ct * 3360 + ch * 16, r * 8 * 3360, SC1);
#pragma unroll
        for (int r = 0; r < 8; ++r)
          if (__all(max(max(pv[r][0], pv[r][1]), max(pv[r][2], pv[r][3])) != UNSET)) todo &= ~(1u << r);
        asm volatile("s_sleep 1" ::: "memory");
      }
      STAMP(3);
      f32x2 sum[4];
#pragma unroll
      for (int e = 0; e < 4; ++e) sum[e] = f32x2{0.f, 0.f};
#pragma unroll
      for (int r = 0; r < 8; ++r)
#pragma unroll
        for (int e = 0; e < 4; ++e) {
          const unsigned x = pv[r][e];
          sum[e] += f32x2{__uint_as_float(x << 16), __uint_as_float(x & 0xFFFF0000u)};
        }
#pragma unroll
      for (int u = 0; u < 2; ++u) {
        const u32x2 v = {pk2(sum[2 * u][0], sum[2 * u][1]), pk2(sum[2 * u + 1][0], sum[2 * u + 1][1])};
        *(LAS u32x2*)dp[u] = v;
        if (dup[u]) *(LAS u32x2*)(dp[u] + 4 * KS) = v;
      }
      WAIT_LGKM0();
      BARRIER();
      STAMP(4);
    }
  {
    const int l31 = l & 31, h = l >> 5, hg = w >> 2, wr = w & 3;
    const rsrc_t ra = mk_rsrc(p.ws, A2_BYTES);
    const unsigned vo = (unsigned)(((rt * 16 + wr * 4 + ((l >> 4) & 1)) * KB_ALL + 48) * 1024 + (2 * ct + hg) * 2048 + h * 256 + (l & 15) * 16);
    const unsigned vx = vo + 16384 - (2 * ct + hg) * 1536;
    bf16x8 af[4][2];
#pragma unroll
    for (int hd = 0; hd < 4; ++hd)
#pragma unroll
      for (int t = 0; t < 2; ++t) af[hd][t] = lds_b128(lds + (KVT + hg * KVT_SZ + l31 * KS + h * 16) + 32 * hd * KS + t * 32);
    float gv[2];
#pragma unroll
    for (int s = 0; s < 2; ++s) gv[s] = *(LAS float*)(lds + M_G + (64 * wr + 32 * s + l31) * 4);
    FST(18);
    SCHED_FENCE();
    f32x16 acc[2][4];
#pragma unroll
    for (int s = 0; s < 2; ++s)
#pragma unroll
      for (int hd = 3; hd >= 0; --hd) acc[s][hd] = mfma32(af[hd][1], pq[5 * s + hd + 1], mfma32(af[hd][0], pq[5 * s + hd], f32x16{}));
#pragma unroll
    for (int s = 0; s < 2; ++s) {
      u32x4 pc[4];
      unsigned ex[4][2];
#pragma unroll
      for (int hd = 3; hd >= 0; --hd) {
        const f32x16& c = acc[s][hd];
        float d0 = c[12], d1 = c[8];
        asm volatile("" : "+v"(d0), "+v"(d1));
        const float f = gv[s] * __builtin_amdgcn_rcpf((h ? d1 : d0) + 1e-6f);
        const f32x2 ff = {f, f};
        unsigned r[6];
#pragma unroll
        for (int i = 0; i < 6; ++i) { const f32x2 y = f32x2{c[2 * i], c[2 * i + 1]} * ff; r[i] = pk2(y[0], y[1]); }
        pc[hd] = u32x4{r[0], r[1], r[2], r[3]};
        ex[hd][0] = r[4]; ex[hd][1] = r[5];
        if (hd > 0) st128_sc1(ra, vo + s * 2 * KB_ALL * 1024 + hd * 512, pc[hd]);
      }
      u32x4 x;
#pragma unroll
      for (int i = 0; i < 4; ++i) x[i] = __builtin_amdgcn_permlane32_swap(ex[i >> 1][i & 1], ex[2 + (i >> 1)][i & 1], false, false)[0];
      st128_sc1(ra, vx + s * 2 * KB_ALL * 1024, x);
      st128_sc1(ra, vo + s * 2 * KB_ALL * 1024, pc[0]);
    }
    STAMP(5);
    STAMP(6);
    FDUMP();
    return;
  }
  }
  const int ct = ctn;
  u32x4 held[15];
  if (__builtin_expect(loader, 1)) {
    u32x4 pre[30];
    p1_loader<false>(p, lds, lw, tm, rt, ct, rot0, t2, pre, [&](int) {
      st128_sc1(mk_rsrc(p.ws + KVP_OFF + (rt * 8 + ct) * 3360, 3360), lw * 1024 + l * 16, u32x4{UNSET, UNSET, UNSET, UNSET});
    }, [&]() {
      lds_tag();
      if (lw == 0 && l == 0) __hip_atomic_store(flag_ptr(p, FL_KV + rt * 8 + ct), tag, __ATOMIC_RELAXED, __HIP_MEMORY_SCOPE_AGENT);
    }, [&]() { lds_tag(); });
    const W2Src src(p, lw, t2);
    src.fill<0, 24>(lds + lw * 1024);
    if (ty == T_KV) {
#pragma unroll
      for (int i = 0; i < 30; ++i) pre[i] = pre_load(src, i);
      SCHED_FENCE();
      BARRIER(); BARRIER();
    } else {
      lds_wait(lds + M_END, 4);
    }
    WAIT_VM(0);
#pragma unroll
    for (int i = 0; i < 15; ++i) *(LAS u32x4*)(lds + (lw * 1024 + l * 16) + w2_off(24 + i)) = pre[i];
#pragma unroll
    for (int i = 0; i < 15; ++i) held[i] = pre[15 + i];
    WAIT_LGKM0();
  } else {
    mfma_start();
    if (spare) {
      BARRIER();
      lds_tag();
      const int l15 = l & 15, r0 = cg * 512 + w * 64 + (l >> 4);
      const rsrc_t rx = mk_rsrc(p.x, S * D * 2), rgw = mk_rsrc(p.gw, D * 2), rg_ = mk_rsrc(p.ws + G_OFF, G_BYTES);
      u32x4 gw[5], v[2][4][5];
#pragma unroll
      for (int c = 0; c < 5; ++c) gw[c] = __builtin_amdgcn_raw_buffer_load_b128(rgw, l15 * 16 + c * 256, 0, 0);
      auto ld = [&](int it) {
#pragma unroll
        for (int u = 0; u < 4; ++u)
#pragma unroll
          for (int c = 0; c < 5; ++c) v[it & 1][u][c] = __builtin_amdgcn_raw_buffer_load_b128(rx, (r0 + 4 * u) * 1280 + l15 * 16 + c * 256, it * 16 * 1280, 0);
      };
      ld(0);
#pragma unroll
      for (int it = 0; it < 4; ++it) {
        if (it < 3) ld(it + 1);
        SCHED_FENCE();
        float keep = 0.f;
#pragma unroll
        for (int u = 0; u < 4; ++u) {
          float sacc = 0.f;
#pragma unroll
          for (int c = 0; c < 5; ++c)
#pragma unroll
            for (int e = 0; e < 4; ++e) {
              const unsigned xw = v[it & 1][u][c][e], ge = gw[c][e];
              sacc = __builtin_amdgcn_fdot2_f32_bf16(__builtin_bit_cast(bf16x2, xw), __builtin_bit_cast(bf16x2, ge), sacc, false);
            }
          sacc += __shfl_xor(sacc, 1); sacc += __shfl_xor(sacc, 2); sacc += __shfl_xor(sacc, 4); sacc += __shfl_xor(sacc, 8);
          if (l15 == u) keep = sacc;
        }
        if (l15 < 4) __builtin_amdgcn_raw_buffer_store_b32(__builtin_bit_cast(unsigned, sigmoidf(keep)), rg_, (r0 + 16 * it + 4 * l15) * 4, 0, SC1);
        SCHED_FENCE();
      }
      publish(p, lds, FL_G + cg, tag, 8);
      STAMP(6);
      FDUMP();
      if (cg == 0 && w == 0) {
#pragma unroll 1
        for (int i = 0; i < 4; ++i) poll(flag_ptr(p, i * 64 + l), tag);
        if (l == 0) __hip_atomic_store((GAS u64*)(p.ws + EPOCH_OFF), tag, __ATOMIC_RELAXED, __HIP_MEMORY_SCOPE_AGENT);
      }
      return;
    }
      f32x4 acc[4][10];
#pragma unroll
      for (int i = 0; i < 4; ++i)
#pragma unroll
        for (int j = 0; j < 10; ++j) acc[i][j] = f32x4{};
      if (ty == T_GLU) {
        p1_main16<T_GLU>(lds, w, acc);
        STAMP(1);
        const Consts cn(-1.f);
        const rsrc_t ra = mk_rsrc(p.ws, A2_BYTES);
        const unsigned vo = (unsigned)((rt * 10 * KB_ALL + ct * 4 + w) * 1024 + l * 16);
#pragma unroll
        for (int j = 0; j < 10; ++j) {
          st128_sc1(ra, vo + j * KB_ALL * 1024, __builtin_bit_cast(u32x4, pack44(glu4(acc[0][j], acc[2][j], cn), glu4(acc[1][j], acc[3][j], cn))));
          SCHED_FENCE();
        }
        STAMP(2);
        lds_tag();
        publish(p, lds, FL_H + rt * 12 + ct, tag, 4);
      } else {
        int goff[2];
#pragma unroll
        for (int i = 0; i < 2; ++i) {
          const int en = min(2 * (int)threadIdx.x + i, 419), row = en / 5, dq = en % 5;
          const int hd = row < 80 ? row / 20 : row - 80, d0 = 20 * hd + 4 * dq, ti = d0 >> 4, tj = row >> 4;
          const int tile = row >= 80 ? 9 : ti == tj ? ti : (ti == 0 || ti == 4) ? 8 : 4 + ti;
          goff[i] = tile * 1024 + (((d0 & 15) >> 2) * 16 + (row < 80 ? row & 15 : ti)) * 16;
          asm volatile("" : "+v"(goff[i]));
        }
        p1_main16<T_KV>(lds, w, acc);
        STAMP(1);
        const Consts cn(1.f);
        const int l15 = l & 15, g = l >> 4;
        ldsp tp = lds + (w * 10240 + l * 16);
        bf16x8 vf[2][5];
#pragma unroll
        for (int u = 0; u < 2; ++u)
#pragma unroll
          for (int j = 0; j < 5; ++j) vf[u][j] = pack44(acc[2 * u][5 + j], acc[2 * u + 1][5 + j]);
        f32x4 z = {}, dg[5], lo[5], hi[5];
        SCHED_FENCE();
#pragma unroll
        for (int i = 0; i < 5; ++i) {
          const unsigned o1 = l15 == i ? 0x3F803F80u : 0u;
          const bf16x8 one = __builtin_bit_cast(bf16x8, u32x4{o1, o1, o1, o1});
          dg[i] = f32x4{}; lo[i] = f32x4{}; hi[i] = f32x4{};
#pragma unroll
          for (int u = 0; u < 2; ++u) {
            const bf16x8 kf = pack44(phi4(acc[2 * u][i], cn), phi4(acc[2 * u + 1][i], cn));
            if (i > 0) lo[i] = mfma16(kf, vf[u][i - 1], lo[i]);
            if (i < 4) hi[i] = mfma16(kf, vf[u][i + 1], hi[i]);
            dg[i] = mfma16(kf, vf[u][i], dg[i]);
            z = mfma16(kf, one, z);
            if (i > 0) {
              if (u == 0) *(LAS f32x4*)(tp + (i - 1) * 1024) = dg[i - 1];
              else if (i > 1) *(LAS f32x4*)(tp + (3 + i) * 1024) = g < i - 1 ? lo[i - 1] : hi[i - 1];
            }
            SCHED_FENCE();
          }
          if (i == 0) { FST(18); lds_wait(lds + M_END, 4); }
        }
        FST(19);
        *(LAS f32x4*)(tp + 8 * 1024) = l15 < 8 ? hi[0] : lo[4];
        *(LAS f32x4*)(tp + 4 * 1024) = dg[4];
        *(LAS f32x4*)(tp + 9 * 1024) = z;
        WAIT_LGKM0();
        FST(20);
        BARRIER();
        FST(21);
        const rsrc_t rk = mk_rsrc(p.ws + KVP_OFF + (rt * 8 + ct) * 3360, 3360);
        f32x4 part[2][4];
#pragma unroll
        for (int i = 0; i < 2; ++i)
#pragma unroll
          for (int ww = 0; ww < 4; ++ww) part[i][ww] = *(LAS f32x4*)(lds + goff[i] + ww * 10240);
        SCHED_FENCE();
        u32x4 res = __builtin_bit_cast(u32x4, pack44((part[0][0] + part[0][1]) + (part[0][2] + part[0][3]), (part[1][0] + part[1][1]) + (part[1][2] + part[1][3])));
#pragma unroll
        for (int e = 0; e < 4; ++e) res[e] = min(res[e], UNSET - 1);
        st128_sc1(rk, threadIdx.x * 16, res);
        WAIT_LGKM0();
        FST(22);
        BARRIER();
        STAMP(2);
      }
      STAMP(3);
      const W2Src src(p, w, t2);
#pragma unroll
      for (int i = 0; i < 15; ++i) held[i] = src.load(39 + 2 * i);
      SCHED_FENCE();
  }
  int t2l = __builtin_amdgcn_readfirstlane(t2);
  asm volatile("" : "+s"(t2l));
  const int rt2 = (t2l * 205) >> 11;
  p2_main(p, lds, w, rt2, t2l - 10 * rt2, tag, held);
  STAMP(6);
  FDUMP();
}

__global__ void never() { asm volatile(".rept 50000\n s_nop 0\n .endr"); }
#define STAMP_PTR nullptr
extern "C" void kernel_launch(void* const* d_in, const int* in_sizes, int n_in, void* d_out, int out_size, void* d_ws, size_t ws_size,
                              hipStream_t stream) {
  Params p;
  p.x = (const u16*)d_in[0]; p.wqkv = (const u16*)d_in[1]; p.wo = (const u16*)d_in[2];
  p.wg = (const u16*)d_in[4]; p.wu = (const u16*)d_in[5]; p.wd = (const u16*)d_in[6]; p.gw = (const u16*)d_in[7];
  p.out = (u16*)d_out; p.ws = (char*)d_ws; p.stamps = STAMP_PTR;
  (void)hipFuncSetAttribute((const void*)fused, hipFuncAttributeMaxDynamicSharedMemorySize, LDS_BYTES);
  hipLaunchKernelGGL(fused, dim3(256), dim3(512), LDS_BYTES, stream, p);
}
```

```cpp
#include <hip/hip_runtime.h>
#include <hip/hip_bf16.h>

typedef unsigned short u16;
typedef unsigned long long u64;
typedef __bf16 bf16x8 __attribute__((ext_vector_type(8)));
typedef __bf16 bf16x4 __attribute__((ext_vector_type(4)));
typedef __bf16 bf16x2 __attribute__((ext_vector_type(2)));
typedef float f32x16 __attribute__((ext_vector_type(16)));
typedef float f32x4 __attribute__((ext_vector_type(4)));
typedef unsigned u32x4 __attribute__((ext_vector_type(4)));
typedef unsigned u32x2 __attribute__((ext_vector_type(2)));
#define LAS __attribute__((address_space(3)))
#define GAS __attribute__((address_space(1)))
typedef LAS char* ldsp;
typedef __amdgpu_buffer_rsrc_t rsrc_t;

constexpr int S = 2048, D = 640, F = 1536;
constexpr float LOG2E = 1.4426950408889634f;
constexpr int KB_ALL = 68;
constexpr unsigned A2_BYTES = 128u * KB_ALL * 1024;
constexpr unsigned KVP_OFF = A2_BYTES, KVP_BYTES = 8 * 8 * 3360;
constexpr unsigned G_OFF = KVP_OFF + KVP_BYTES, G_BYTES = S * 4;
constexpr unsigned FLAG_OFF = G_OFF + G_BYTES;
constexpr unsigned EPOCH_OFF = FLAG_OFF + 256 * 128;
constexpr unsigned WS_USED = EPOCH_OFF + 128;
constexpr int FL_H = 0, FL_KV = 156, FL_O = 220, FL_G = 252;
constexpr int SC1 = 16;
constexpr unsigned UNSET = 0xFFFFFFFFu;
constexpr int SLOT = 53248, MISC = 3 * SLOT, LDS_BYTES = 163840;
constexpr int M_DUMMY = MISC, M_CNT = MISC + 256  , M_END = MISC + 260  , M_RND = MISC + 320  , M_TAG = MISC + 384, M_Z = MISC + 512  , M_G = MISC + 2048  ;
constexpr int KS = 80, KVT = SLOT, KVT_SZ = 4 * 32 * KS;
constexpr int NST1 = 10;
constexpr int SPIN_MAX = 1 << 16;
#define A2AUX SC1
#define GLU_PACE 0
#define OUT_AUX 0
#define P2_AW 3
#define P2_X3 99
#define P2_PRIO 2
#define P2_A0 1
#define P2_RAMP 2
#define P2_G0 2
#define P2_G1 1
#define GATE_HG 3
constexpr unsigned CODE_BYTES = 49500;
#define GLU_PEEL 1
#define P2_GATE 10
#define P2_POS 0
#define ROT (2 * (cg & 1) + 5 * (((cg >> 1) ^ rg) & 1))
#define W2_ST 4

struct Params {
  const u16 *x, *wqkv, *wo, *wg, *wu, *wd, *gw;
  u16* out;
  char* ws;
  u64* stamps;
};

#define STAMP(i) do {} while (0)
#define FST(i) do {} while (0)
#define FSTD(i) do {} while (0)
#define FINIT() do {} while (0)
#define FDUMP() do {} while (0)

#define WAIT_VM(n) asm volatile("s_waitcnt vmcnt(" #n ")" ::: "memory")
#define WAIT_LGKM0() asm volatile("s_waitcnt lgkmcnt(0)" ::: "memory")
#define BARRIER() do { asm volatile("" ::: "memory"); __builtin_amdgcn_s_barrier(); asm volatile("" ::: "memory"); } while (0)
#define SCHED_FENCE() __builtin_amdgcn_sched_barrier(0)
#define ORDER_FENCE() do { asm volatile("" ::: "memory"); __builtin_amdgcn_sched_barrier(0); } while (0)
#define SG_MFMA(n) __builtin_amdgcn_sched_group_barrier(0x8, n, 0)
#define SG_DS(n) __builtin_amdgcn_sched_group_barrier(0x100, n, 0)
#define SG_VALU(n) __builtin_amdgcn_sched_group_barrier(0x2, n, 0)
#define SG_DW(n) __builtin_amdgcn_sched_group_barrier(0x200, n, 0)
#define SG_VM(n) __builtin_amdgcn_sched_group_barrier(0x20, n, 0)

__device__ __forceinline__ rsrc_t mk_rsrc(const void* p, unsigned bytes) { return __builtin_amdgcn_make_buffer_rsrc((void*)p, 0, bytes, 0x00020000); }
__device__ __forceinline__ void dma16(rsrc_t r, ldsp dst, unsigned voff, unsigned soff) { __builtin_amdgcn_raw_ptr_buffer_load_lds(r, dst, 16, voff, soff, 0, 0); }
__device__ __forceinline__ bf16x8 lds_b128(ldsp a) { return *(LAS bf16x8*)a; }
__device__ __forceinline__ bf16x8 lds_tr2(ldsp a0, ldsp a1) {
  bf16x4 lo = __builtin_amdgcn_ds_read_tr16_b64_v4bf16((LAS bf16x4*)a0);
  bf16x4 hi = __builtin_amdgcn_ds_read_tr16_b64_v4bf16((LAS bf16x4*)a1);
  return __builtin_shufflevector(lo, hi, 0, 1, 2, 3, 4, 5, 6, 7);
}
__device__ __forceinline__ f32x16 mfma32(bf16x8 a, bf16x8 b, f32x16 c) { return __builtin_amdgcn_mfma_f32_32x32x16_bf16(a, b, c, 0, 0, 0); }
__device__ __forceinline__ f32x4 mfma16(bf16x8 a, bf16x8 b, f32x4 c) { return __builtin_amdgcn_mfma_f32_16x16x32_bf16(a, b, c, 0, 0, 0); }
__device__ __forceinline__ unsigned pk2(float a, float b) {
  bf16x2 v = {(__bf16)a, (__bf16)b};
  return __builtin_bit_cast(unsigned, v);
}
__device__ __forceinline__ bf16x8 pack8(const f32x16& c, int o) {
  u32x4 r = {pk2(c[o], c[o + 1]), pk2(c[o + 2], c[o + 3]), pk2(c[o + 4], c[o + 5]), pk2(c[o + 6], c[o + 7])};
  return __builtin_bit_cast(bf16x8, r);
}
__device__ __forceinline__ bf16x8 pack44(const f32x4& a, const f32x4& b) {
  u32x4 r = {pk2(a[0], a[1]), pk2(a[2], a[3]), pk2(b[0], b[1]), pk2(b[2], b[3])};
  return __builtin_bit_cast(bf16x8, r);
}
__device__ __forceinline__ float phi(float t) {
  const float e = __builtin_amdgcn_exp2f(t * LOG2E);
  return __builtin_amdgcn_fmed3f(t, 1e-6f, __builtin_inff()) + __builtin_amdgcn_fmed3f(e, 0.f, 1.f);
}
__device__ __forceinline__ float sigmoidf(float t) { return __builtin_amdgcn_rcpf(1.f + __builtin_amdgcn_exp2f(-LOG2E * t)); }
typedef float f32x2 __attribute__((ext_vector_type(2)));
struct Consts {
  f32x2 l2e;
  float eps, big;
  __device__ __forceinline__ Consts(float sign) : l2e{sign * LOG2E, sign * LOG2E}, eps(1e-6f), big(3.4028234664e38f) {
    asm volatile("" : "+s"(l2e), "+s"(eps), "+v"(big));
  }
};
__device__ __forceinline__ f32x4 phi4(f32x4 t, const Consts& c) {
  const f32x2 a = f32x2{t[0], t[1]} * c.l2e, b = f32x2{t[2], t[3]} * c.l2e;
  f32x2 m0, m1, e0, e1;
#pragma unroll
  for (int i = 0; i < 2; ++i) {
    m0[i] = __builtin_amdgcn_fmed3f(t[i], c.eps, c.big);
    m1[i] = __builtin_amdgcn_fmed3f(t[2 + i], c.eps, c.big);
    e0[i] = __builtin_amdgcn_fmed3f(__builtin_amdgcn_exp2f(a[i]), 0.f, 1.f);
    e1[i] = __builtin_amdgcn_fmed3f(__builtin_amdgcn_exp2f(b[i]), 0.f, 1.f);
  }
  const f32x2 r0 = m0 + e0, r1 = m1 + e1;
  return f32x4{r0[0], r0[1], r1[0], r1[1]};
}
__device__ __forceinline__ f32x4 glu4(f32x4 g, f32x4 u, const Consts& c) {
  f32x2 a = f32x2{g[0], g[1]} * c.l2e, b = f32x2{g[2], g[3]} * c.l2e;
#pragma unroll
  for (int i = 0; i < 2; ++i) { a[i] = __builtin_amdgcn_exp2f(a[i]); b[i] = __builtin_amdgcn_exp2f(b[i]); }
  a += 1.f; b += 1.f;
#pragma unroll
  for (int i = 0; i < 2; ++i) { a[i] = __builtin_amdgcn_rcpf(a[i]); b[i] = __builtin_amdgcn_rcpf(b[i]); }
  a *= f32x2{u[0], u[1]}; b *= f32x2{u[2], u[3]};
  return f32x4{a[0], a[1], b[0], b[1]};
}
__device__ __forceinline__ void st128_sc1(rsrc_t r, unsigned off, u32x4 v) { __builtin_amdgcn_raw_buffer_store_b128(v, r, off, 0, SC1); }

__device__ __forceinline__ u64 make_tag(u64 e) { return ((e ^ 0x9E3779B97F4A7C15ull) * 0xBF58476D1CE4E5B9ull) | 1ull; }
__device__ __forceinline__ GAS u64* flag_ptr(const Params& p, int f) { return (GAS u64*)(p.ws + FLAG_OFF + (size_t)f * 128); }
__device__ __forceinline__ u64 ld_flag(GAS u64* f) { return __hip_atomic_load(f, __ATOMIC_RELAXED, __HIP_MEMORY_SCOPE_AGENT); }
__device__ __forceinline__ void poll(GAS u64* f, u64 tag) {
#pragma unroll 1
  for (int spin = 0; spin < SPIN_MAX; ++spin) {
    if (__all(ld_flag(f) == tag)) break;
    __builtin_amdgcn_s_sleep(2);
  }
  __builtin_amdgcn_fence(__ATOMIC_ACQUIRE, "wavefront");
}
__device__ __forceinline__ void lds_arrive(ldsp c) {
  asm volatile("s_mov_b64 exec, 1\n\tds_add_u32 %0, %1\n\ts_mov_b64 exec, -1" ::"v"((unsigned)(size_t)c), "v"(1u) : "memory");
}
__device__ __forceinline__ void lds_wait(ldsp c, unsigned n) {
#pragma unroll 1
  for (int spin = 0; spin < SPIN_MAX; ++spin) {
    unsigned v;
    asm volatile("ds_read_b32 %0, %1\n\ts_waitcnt lgkmcnt(0)" : "=v"(v) : "v"((unsigned)(size_t)c) : "memory");
    if (__builtin_amdgcn_readfirstlane(v) >= n) break;
    __builtin_amdgcn_s_sleep(1);
  }
}
__device__ __forceinline__ void publish(const Params& p, ldsp lds, int f, u64 tag, unsigned n) {
  WAIT_VM(0);
  if ((threadIdx.x & 63) == 0) {
    const unsigned old = __hip_atomic_fetch_add((LAS unsigned*)(lds + M_CNT), 1u, __ATOMIC_RELAXED, __HIP_MEMORY_SCOPE_WORKGROUP);
    if (old == n - 1) __hip_atomic_store(flag_ptr(p, f), tag, __ATOMIC_RELAXED, __HIP_MEMORY_SCOPE_AGENT);
  }
}

enum { T_Q = 0, T_KV = 1, T_GLU = 2 };

__device__ __forceinline__ constexpr int w2_off(int s) { return (SLOT + (s % 39) * 4096) % (3 * SLOT); }

struct W2Src {
  rsrc_t rd, ro;
  unsigned wv, wv4, ws0, wo0, wo1;
  __device__ __forceinline__ W2Src(const Params& p, int piece, int t2) {
    int l = threadIdx.x & 63;
    t2 = __builtin_amdgcn_readfirstlane(t2);
    asm volatile("" : "+s"(piece), "+s"(t2), "+v"(l));
    const int ct = t2 - 10 * ((t2 * 205) >> 11);
    rd = mk_rsrc(p.wd, F * D * 2);
    ro = mk_rsrc(p.wo, D * D * 2);
    wv = (l >> 3) * 1280 + (((l & 7) ^ ((l >> 4) << 1)) << 4);
    wv4 = wv + (l >> 5) * 36 * 1280;
    ws0 = (unsigned)(8 * piece) * 1280 + ct * 128;
    wo0 = (unsigned)(20 * (piece >> 1) + 8 * (piece & 1)) * 1280 + ct * 128;
    wo1 = (unsigned)(80 * (piece >> 1) + 16 + 20 * (piece & 1)) * 1280 + ct * 128;
  }
  __device__ __forceinline__ u32x4 load(int s) const {
    if (s < 48) return __builtin_amdgcn_raw_buffer_load_b128(rd, wv, ws0 + (unsigned)s * 32 * 1280, 0);
    if (s < 64) return __builtin_amdgcn_raw_buffer_load_b128(ro, wv, wo0 + (unsigned)(s - 48) * 40 * 1280, 0);
    return __builtin_amdgcn_raw_buffer_load_b128(ro, wv4, wo1 + (unsigned)(s - 64) * 160 * 1280, 0);
  }
  template <int S0, int S1> __device__ __forceinline__ void fill(ldsp dst) const {
#pragma unroll
    for (int s = S0; s < S1; ++s) dma16(rd, dst + w2_off(s), wv, ws0 + (unsigned)s * 32 * 1280);
  }
};

__device__ __forceinline__ u32x4 pre_load(const W2Src& src, int i) { return i < 15 ? src.load(24 + i) : src.load(40 + 2 * (i - 15)); }

template <bool ISQ, class Reset, class Raise, class Gap>
__device__ __forceinline__ void p1_loader(const Params& p, ldsp lds, int lw, int tm, int rt, int ct, int k0, int t2, u32x4 (&pre)[30],
                                          Reset reset, Raise raise, Gap gap) {
  const bool tb = !ISQ && tm != 0;
  auto sel = [&](int a, int b) { return ISQ ? b : b ^ ((a ^ b) & tm); };
  auto selp = [&](const u16* a, const u16* b) { return ISQ ? b : (const u16*)((u64)b ^ (((u64)a ^ (u64)b) & (u64)(long long)tm)); };
  const int l = threadIdx.x & 63;
  const int i8 = sel(5 * lw, 8 * lw + 3);
  const unsigned xe = (l >> 3) * 1280 + (((l & 7) ^ (l >> 4)) << 4);
  const unsigned xa = (xe ^ ((i8 & 1) << 6)) + (unsigned)(sel(160, 256) * rt + 8 * i8) * 1280, xb = (xa ^ 64) + 8 * 1280;
  const int d8 = i8 * 1024;
  const rsrc_t rx = mk_rsrc(p.x, S * D * 2);
  FST(0);
#pragma unroll
  for (int j = 0; j < 5; ++j) dma16(rx, lds + d8 + j * 1024, (j & 1) ? xb : xa, k0 * 128 + (j >> 1) * 16 * 1280);
  SCHED_FENCE();
  const rsrc_t r0 = mk_rsrc(selp(p.wg, p.wqkv), sel(D * F * 2, D * 3 * D * 2));
  const rsrc_t r1 = mk_rsrc(selp(p.wu, p.wqkv), sel(D * F * 2, D * 3 * D * 2));
  const rsrc_t r2 = mk_rsrc(selp(p.wu, p.x), sel(D * F * 2, S * D * 2));
  unsigned v[8];
  {
    const int r = l >> 4;
    const unsigned g0 = (16 * lw + r) * 3072 + ct * 256 + (((l & 15) ^ (r << 1)) << 4);
    const int colA = ISQ ? 320 * ct : 1280 + 160 * ct, colB = ISQ ? 160 : 1280;
#pragma unroll
    for (int j = 0; j < 5; ++j) {
      const int c = 64 * j + l, rr = (c * 3277) >> 16;
      int cc = c - 20 * rr;
      if (!ISQ) cc ^= (rr >> 2) & 2;
      const unsigned a = (16 * lw + rr) * 3840 + colA + cc * 16 + (cc < 10 ? 0 : colB - 160);
      v[j] = sel((g0 ^ ((j & 2) << 6)) + (j & 3) * 4 * 3072, a);
    }
    v[5] = sel(v[1], xb - 4 * 8 * 1280);
    v[6] = sel(v[2], xa - 2 * 8 * 1280);
    v[7] = sel(v[3], xb - 2 * 8 * 1280);
  }
  const int d0 = sel(20480 + 4096 * lw, 32768 + 5120 * lw), d4 = d0 + sel(16384, 4096), d5 = sel(d4 + 1024, d8 - 3072);
  const unsigned wstep = sel(64 * 3072, 64 * 3840), mstep = sel(64 * 3072, 128);
  auto issue_a = [&](int st, int so) {
    ldsp sb = lds + so;
#pragma unroll
    for (int j = 0; j < 5; ++j) dma16(rx, sb + d8 + j * 1024, (j & 1) ? xb : xa, st * 128 + (j >> 1) * 16 * 1280);
#pragma unroll
    for (int j = 0; j < 2; ++j) dma16(r2, sb + d5 + j * 1024, v[5 + j], st * mstep);
    ORDER_FENCE();
  };
  auto issue_b = [&](int st, int so) {
    ldsp sb = lds + so;
    dma16(r2, sb + d5 + 2048, v[7], st * mstep);
#pragma unroll
    for (int j = 0; j < 4; ++j) dma16(r0, sb + d0 + j * 1024, v[j], st * wstep);
    dma16(r1, sb + d4, v[4], st * wstep);
    ORDER_FENCE();
  };
  auto rot = [&](int j) { const int t = k0 + j; return t >= NST1 ? t - NST1 : t; };
#pragma unroll
  for (int j = 0; j < 2; ++j) dma16(r2, lds + d5 + j * 1024, v[5 + j], k0 * mstep);
  issue_b(k0, 0);
  FST(1);
  issue_a(rot(1), SLOT);
  FST(2);
  WAIT_VM(7);
  FST(3);
  BARRIER();
  issue_b(rot(1), SLOT); issue_a(rot(2), 2 * SLOT);
  WAIT_VM(7);
  BARRIER();
  issue_b(rot(2), 2 * SLOT); issue_a(rot(3), 0); issue_b(rot(3), 0);
  WAIT_VM(13);
  if (tb) {
    const W2Src src(p, lw, t2);
#pragma unroll
    for (int j = 1; j < NST1 - 3; ++j) {
      BARRIER();
      issue_a(rot(j + 3), (j % 3) * SLOT); issue_b(rot(j + 3), (j % 3) * SLOT);
#pragma unroll
      for (int i = 5 * (j - 1); i < 5 * j; ++i) pre[i] = pre_load(src, i);
      SCHED_FENCE();
      if (j == 1) WAIT_VM(18); else WAIT_VM(23);
    }
    BARRIER();
    gap();
    WAIT_VM(5);
  } else {
    int so = SLOT;
#pragma unroll 1
    for (int j = 1; j < NST1 - 3; ++j) {
      BARRIER();
      if (!ISQ && j == 3) raise();
      issue_a(rot(j + 3), so); issue_b(rot(j + 3), so);
      so = so == 2 * SLOT ? 0 : so + SLOT;
      if (ISQ) {
        if (j < 6) { reset(j - 1); if (j == 1) WAIT_VM(17); else WAIT_VM(21); } else WAIT_VM(13);
      } else {
        if (j == 1) { reset(0); WAIT_VM(14); } else WAIT_VM(13);
      }
    }
    BARRIER();
    if (ISQ) raise();
    gap();
    WAIT_VM(0);
  }
  BARRIER();
}

struct Warm {
  unsigned sink;
  __device__ __forceinline__ Warm(ldsp lds) : sink((unsigned)(size_t)(lds + M_DUMMY)) {}
  __device__ __forceinline__ void touch(const GAS char* t) {
    unsigned keep;
    asm volatile("s_mov_b32 %0, m0\n\ts_mov_b32 m0, %2\n\ts_nop 0\n\tglobal_load_lds_dword %1, off\n\ts_mov_b32 m0, %0" : "=&s"(keep) : "v"(t), "s"(sink) : "memory");
  }
};

__device__ __forceinline__ void stage_barrier(int st) {
  asm volatile("s_waitcnt lgkmcnt(0)\n\ts_cmp_eq_u32 %0, %1\n\ts_cbranch_scc1 1\n\ts_barrier" ::"s"(st), "i"(NST1 - 1) : "memory", "scc");
}
struct FragQ { bf16x8 w[5], x[2]; };
__device__ __forceinline__ void p1_main_q(ldsp lds, int w, f32x16 (&acc)[5][2]) {
  const int l = threadIdx.x & 63, l31 = l & 31, h = l >> 5;
  const int xa = (64 * w + l31) * 128 + ((h ^ ((l31 >> 1) & 7)) << 4);
  const int wa = 32768 + (8 * h + ((l & 15) >> 2)) * 320 + ((l >> 4) & 1) * 32 + (l & 3) * 8;
  auto load = [&](FragQ& f, ldsp sb, int ks) {
    ldsp xp = sb + (xa ^ (ks << 5));
    ldsp wp = sb + wa + ks * 16 * 320;
    f.x[0] = lds_b128(xp);
    f.w[0] = lds_tr2(wp, wp + 1280);
    f.x[1] = lds_b128(xp + 4096);
#pragma unroll
    for (int n = 1; n < 5; ++n) f.w[n] = lds_tr2(wp + n * 64, wp + n * 64 + 1280);
  };
  auto mma = [&](const FragQ& f) {
#pragma unroll
    for (int n = 0; n < 5; ++n)
#pragma unroll
      for (int s = 0; s < 2; ++s) acc[n][s] = mfma32(f.w[n], f.x[s], acc[n][s]);
#pragma unroll
    for (int i = 0; i < 6; ++i) { SG_MFMA(1); SG_DS(2); }
    SG_MFMA(4);
    SCHED_FENCE();
  };
  FST(0);
  BARRIER();
  FST(1);
  FragQ f0, f1;
  int so = 0;
  load(f0, lds, 0);
  SCHED_FENCE();
#pragma unroll 1
  for (int j = 0; j < NST1; ++j) {
    ldsp sb = lds + so;
    load(f1, sb, 1); mma(f0);
    load(f0, sb, 2); mma(f1);
    load(f1, sb, 3); mma(f0);
    so = so == 2 * SLOT ? 0 : so + SLOT;
    stage_barrier(j);
    FSTD(2 + j);
    SCHED_FENCE();
    load(f0, lds + so, 0); mma(f1);
  }
  WAIT_LGKM0();
}

template <int TY, bool PEEL, class Tail>
__device__ __forceinline__ void p1_main16(ldsp lds, int w, f32x4 (&acc)[4][10], Tail tail) {
  constexpr bool TB = TY == T_GLU;
  const int l = threadIdx.x & 63, l15 = l & 15, g = l >> 4, q = l15 >> 2;
  const int xa = ((TB ? 0 : 64 * w) + l15) * 128 + ((g ^ ((l15 >> 1) & 7)) << 4);
  int wa0, wa1;
  if (TB) {
    wa0 = 20480 + (8 * g + q) * 256 + (((2 * w) ^ (q + 4 * (g & 1))) << 5) + (l & 3) * 8;
    wa1 = wa0 ^ 32;
  } else {
    wa0 = 32768 + (8 * g + q) * 320 + (l & 3) * 8 + 32 * (g & 1);
    wa1 = wa0 - 64 * (g & 1);
  }
  ldsp px0 = lds + xa, px1 = lds + (xa ^ 64), pw0 = lds + wa0, pw1 = lds + wa1;
  auto ldx = [&](int u, int t) { return lds_b128((u ? px1 : px0) + t * 2048); };
  auto ldw = [&](int u, int t) {
    ldsp a = ((t & 1) ? pw1 : pw0) + (TB ? u * 8192 + (t >> 1) * 16384 : u * 10240 + t * 32);
    return lds_tr2(a, a + (TB ? 1024 : 1280));
  };
  auto lda = [&](int u, int i) { return TB ? ldw(u, i) : ldx(u, i); };
  auto ldb = [&](int u, int j) { return TB ? ldx(u, j) : ldw(u, j); };
  auto mma = [&](const bf16x8 (&a)[4], const bf16x8& b, int j) {
#pragma unroll
    for (int i = 0; i < 4; ++i) acc[i][j] = mfma16(a[i], b, acc[i][j]);
    SG_MFMA(1); SG_DS(2); SG_MFMA(1); SG_DS(2); SG_MFMA(1); SG_DS(2); SG_MFMA(1);
    SCHED_FENCE();
  };
  FST(0);
  BARRIER();
  FST(1);
  bf16x8 a0[4], a1[4], b0[10], b1[10];
#pragma unroll
  for (int i = 0; i < 4; ++i) a0[i] = lda(0, i);
#pragma unroll
  for (int j = 0; j < 5; ++j) b0[j] = ldb(0, j);
  SCHED_FENCE();
  int ph = 0;
#pragma unroll 1
  for (int st = 0; st < (PEEL ? NST1 - 1 : NST1); ++st) {
#pragma unroll
    for (int j = 0; j < 10; ++j) {
      if (j < 5) b0[j + 5] = ldb(0, j + 5); else b1[j - 5] = ldb(1, j - 5);
      if (j >= 2 && j < 6) a1[j - 2] = lda(1, j - 2);
      mma(a0, b0[j], j);
    }
#pragma unroll
    for (int j = 0; j < 5; ++j) {
      b1[j + 5] = ldb(1, j + 5);
      mma(a1, b1[j], j);
    }
    {
      const int dlt = ph == 2 ? -2 * SLOT : SLOT;
      ph = ph == 2 ? 0 : ph + 1;
      px0 += dlt; px1 += dlt; pw0 += dlt; pw1 += dlt;
      asm volatile("" : "+v"(px0), "+v"(px1), "+v"(pw0), "+v"(pw1));
#pragma unroll
      for (int i = 0; i < 4; ++i) acc[i][5] = mfma16(a1[i], b1[5], acc[i][5]);
      SG_MFMA(1); SG_VALU(2); SG_MFMA(1); SG_VALU(2); SG_MFMA(2);
      SCHED_FENCE();
    }
    stage_barrier(st);
    FSTD(2 + st);
    SCHED_FENCE();
    a0[0] = lda(0, 0); b0[0] = ldb(0, 0); a0[1] = lda(0, 1); mma(a1, b1[6], 6);
    a0[2] = lda(0, 2); a0[3] = lda(0, 3); b0[1] = ldb(0, 1); mma(a1, b1[7], 7);
    b0[2] = ldb(0, 2); b0[3] = ldb(0, 3); mma(a1, b1[8], 8);
    b0[4] = ldb(0, 4); mma(a1, b1[9], 9);
  }
  if (PEEL) {
#pragma unroll
    for (int j = 0; j < 10; ++j) {
      if (j < 5) b0[j + 5] = ldb(0, j + 5); else b1[j - 5] = ldb(1, j - 5);
      if (j >= 2 && j < 6) a1[j - 2] = lda(1, j - 2);
      mma(a0, b0[j], j);
    }
#pragma unroll
    for (int j = 0; j < 10; ++j) {
      if (j < 5) b1[j + 5] = ldb(1, j + 5);
#pragma unroll
      for (int i = 0; i < 4; ++i) acc[i][j] = mfma16(a1[i], b1[j], acc[i][j]);
      if (j > 0) tail(j - 1);
      SG_MFMA(1); SG_DS(2); SG_VALU(10); SG_MFMA(1); SG_VALU(10); SG_MFMA(1); SG_VALU(10); SG_MFMA(1);
      SCHED_FENCE();
      if (j == 4) lds_arrive(lds + M_END);
    }
    tail(9);
  } else {
    lds_arrive(lds + M_END);
    WAIT_LGKM0();
  }
}

__device__ __forceinline__ void p2_main(const Params& p, ldsp lds, int w, int rt, int ct, u64 tag, const u32x4 (&held)[15]) {
  constexpr int LAG = 3, AW = P2_AW, NR = 17, RH = 12, GATE = P2_GATE;
  const int l = threadIdx.x & 63, l15 = l & 15, kg = l >> 4, q = l15 >> 2;
  const int sg = w & 1, kq = w >> 1, hf = w >> 2;
  const int rlo = rt * 96, rhi = min(rlo + 95, S - 1);
  const rsrc_t ra = mk_rsrc(p.ws, A2_BYTES);
  const unsigned av = (unsigned)(((rt * 6 + sg * 3) * KB_ALL + kq) * 1024 + l * 16);
  const int rh = 4 * kg + q, ro = 16 * (kg >> 1) + 4 * (kg & 1) + q;
  const int wh = rh * 128 + (((rh >> 1) & 3) << 5) + (l & 3) * 8;
  const int wo = ro * 128 + (((ro >> 1) & 3) << 5) + (l & 3) * 8;
  ldsp hdst = lds + ((w & 3) * 1024 + hf * 4096 + l * 16);
  f32x4 acc[4][3];
#pragma unroll
  for (int n = 0; n < 4; ++n)
#pragma unroll
    for (int s = 0; s < 3; ++s) acc[n][s] = f32x4{};
#define PIN_ACC(text) \
  asm volatile(text : "+v"(acc[0][0]), "+v"(acc[1][0]), "+v"(acc[2][0]), "+v"(acc[3][0]), "+v"(acc[0][1]), "+v"(acc[1][1]), "+v"(acc[2][1]), \
               "+v"(acc[3][1]), "+v"(acc[0][2]), "+v"(acc[1][2]), "+v"(acc[2][2]), "+v"(acc[3][2]))
  PIN_ACC("");
  u32x4 a[NR][3];
  bf16x8 wf[2][4];
  float gi[3];
  auto lda = [&](int t) {
#pragma unroll
    for (int s = 0; s < 3; ++s) {
      a[t][s] = __builtin_amdgcn_raw_buffer_load_b128(ra, av + s * KB_ALL * 1024, t * 4096, A2AUX);
    }
  };
  ldsp wb[4];
  auto ldw = [&](int t) {
    const int o0 = w2_off(4 * t), r0 = o0 & ~0x7FFF;
    auto straight = [](int u) { return w2_off(4 * u + 3) == w2_off(4 * u) + 3 * 4096; };
    if (t == 0 || t == RH || r0 != (w2_off(4 * t - 4) & ~0x7FFF) || !straight(t) || !straight(t - 1)) {
      int rel = kq * 4096;
      if (!straight(t)) rel = kq == 0 ? 0 : kq == 1 ? w2_off(4 * t + 1) - o0 : kq == 2 ? w2_off(4 * t + 2) - o0 : w2_off(4 * t + 3) - o0;
#pragma unroll
      for (int n = 0; n < 4; ++n) {
        wb[n] = lds + (((t < RH ? wh : wo) ^ ((n ^ kq) << 5)) + r0 + rel);
        asm volatile("" : "+v"(wb[n]));
      }
    }
#pragma unroll
    for (int n = 0; n < 4; ++n) wf[t & 1][n] = lds_tr2(wb[n] + (o0 - r0), wb[n] + (o0 - r0) + (t < RH ? 2048 : 1024));
  };
  if (w == 4)
    poll(flag_ptr(p, l < 32 ? FL_H + ((l & 16) ? rhi : rlo) / 160 * 12 + min(l15, 11)
                     : l < 48 ? FL_G + (((l & 1) ? rhi : rlo) >> 9) : FL_O + (((l & 4) ? rhi : rlo) >> 8) * 4 + (l & 3)), tag);
  const unsigned cnt = (unsigned)(size_t)(lds + M_RND);
  auto arrive = [&](int r) {
    asm volatile("s_mov_b64 exec, 1\n\tds_add_u32 %0, %1 offset:%2\n\ts_mov_b64 exec, -1" ::"v"(cnt), "v"(1u), "i"(4 * r) : "memory");
  };
  auto counted = [](int r) { return (r >= 0 && r < GATE - LAG) || (r >= GATE && r < RH); };
  unsigned peek = 0;
  auto check = [&](int r) {
    unsigned sv, n;
    asm volatile(
        "s_nop 0\n\tv_readfirstlane_b32 %0, %2\n\ts_cmp_ge_u32 %0, 8\n\ts_cbranch_scc1 .Lok%=\n\ts_mov_b32 %1, 0x100000\n"
        ".Lagain%=:\n\tds_read_b32 %2, %3 offset:%4\n\ts_waitcnt lgkmcnt(0)\n\tv_readfirstlane_b32 %0, %2\n\ts_sub_u32 %1, %1, 1\n\t"
        "s_cbranch_scc1 .Lok%=\n\ts_cmp_ge_u32 %0, 8\n\ts_cbranch_scc0 .Lagain%=\n.Lok%=:"
        : "=&s"(sv), "=&s"(n), "+v"(peek) : "v"(cnt), "i"(4 * r) : "memory", "scc");
  };
  auto prio = [](int v) {
    if (v >= 3) __builtin_amdgcn_s_setprio(3); else if (v == 2) __builtin_amdgcn_s_setprio(2);
    else if (v == 1) __builtin_amdgcn_s_setprio(1); else __builtin_amdgcn_s_setprio(0);
  };
  auto upto = [](int t) {
    if (t < GATE) return min(min(t + 1 + AW, P2_A0 + P2_RAMP * (t + 1)), RH);
    return min(RH + P2_G0 + P2_G1 * (t - GATE), NR);
  };
  BARRIER();
  STAMP(4);
  {
    const rsrc_t rg = mk_rsrc(p.ws + G_OFF, G_BYTES);
#pragma unroll
    for (int t = 0; t < upto(-1); ++t) {
      prio(3 - t); lda(t);
      if (t == 0) {
        ldw(0);
#pragma unroll
        for (int s = 0; s < 3; ++s) gi[s] = __builtin_bit_cast(float, __builtin_amdgcn_raw_buffer_load_b32(rg, (rt * 96 + sg * 48 + s * 16 + l15) * 4, 0, SC1));
      }
      SCHED_FENCE();
    }
  }
#pragma unroll
  for (int t = 0; t < NR; ++t) {
    if (t == GATE) {
      if (w == 7) {
        const unsigned co = (unsigned)(((rt * 6 + min(l >> 3, 5)) * KB_ALL + 48) * 1024 + (l & GATE_HG) * 2048);
#pragma unroll 1
        for (int spin = 0; spin < SPIN_MAX; ++spin) {
          if (__all(__builtin_amdgcn_raw_buffer_load_b32(ra, co, 0, SC1) != UNSET)) break;
          asm volatile("s_sleep 1" ::: "memory");
        }
      }
      WAIT_LGKM0();
      FST(16);
      BARRIER();
      FST(17);
      SCHED_FENCE();
    }
    if (counted(t - LAG)) check(t - LAG);
    if (counted(t + 1 - LAG)) peek = __hip_atomic_load((LAS unsigned*)(lds + M_RND + 4 * (t + 1 - LAG)), __ATOMIC_RELAXED, __HIP_MEMORY_SCOPE_WORKGROUP);
    SCHED_FENCE();
    prio(3 - (t & 3));
#pragma unroll
    for (int u = upto(t - 1); u < upto(t); ++u) lda(u);
    if (t + 1 < NR) ldw(t + 1);
    if (t >= LAG) {
#pragma unroll
      for (int i = 2 * (t - LAG); i < min(2 * (t - LAG) + 2, 15); ++i) {
        *(LAS u32x4*)(hdst + w2_off(39 + 2 * i)) = held[i];
      }
    }
    if (t >= RH) {
      auto unset = [&]() {
        unsigned m = 0u;
#pragma unroll
        for (int s = 0; s < 3; ++s) m = max(max(m, max(a[t][s][0], a[t][s][1])), max(a[t][s][2], a[t][s][3]));
        return __any(m == UNSET);
      };
      if (__builtin_expect(unset(), 0)) {
        int spin = 0;
#pragma unroll 1
        do { lda(t); __builtin_amdgcn_s_waitcnt(0x0F70); } while (unset() && ++spin < SPIN_MAX);
      }
      SCHED_FENCE();
    }
    if (t == RH) {
      PIN_ACC("s_nop 7");
#pragma unroll
      for (int n = 0; n < 4; ++n)
#pragma unroll
        for (int s = 0; s < 3; ++s) acc[n][s] *= 1.f - gi[s];
    }
    if (t >= RH) PIN_ACC("s_nop 1");
#pragma unroll
    for (int s = 0; s < 3; ++s)
#pragma unroll
      for (int n = 0; n < 4; ++n) {
        asm volatile("v_mfma_f32_16x16x32_bf16 %0, %1, %2, %0" : "+v"(acc[n][s]) : "v"(wf[t & 1][n]), "v"(a[t][s]));
      }
    if (counted(t)) arrive(t);
    SCHED_FENCE();
  }
  PIN_ACC("s_nop 7");
  STAMP(5);
  constexpr int XB = 16384;
  static_assert(w2_off(30) == XB && w2_off(39) == XB + 9 * 4096, "ring layout");
  {
    ldsp wp = lds + (XB + w * 9 * 1024 + l * 16);
#pragma unroll
    for (int d = 1; d < 4; ++d)
#pragma unroll
      for (int s = 0; s < 3; ++s) {
        *(LAS f32x4*)(wp + ((d - 1) * 3 + s) * 1024) = acc[d][s];
      }
  }
  const rsrc_t rout = mk_rsrc(p.out, S * D * 2);
  unsigned vo = (unsigned)((rt * 96 + sg * 48 + l15) * 1280 + (ct * 64 + kq * 16 + 4 * kg) * 2);
  asm volatile("" : "+v"(vo));
  WAIT_LGKM0();
  BARRIER();
  f32x4 part[3][4];
#pragma unroll
  for (int s = 0; s < 3; ++s)
#pragma unroll
    for (int d = 1; d < 4; ++d) part[s][d] = *(LAS f32x4*)(lds + XB + ((w ^ (2 * d)) * 9 + (d - 1) * 3 + s) * 1024 + l * 16);
  SCHED_FENCE();
#pragma unroll
  for (int s = 0; s < 3; ++s) {
    const f32x4 v = (acc[0][s] + part[s][1]) + (part[s][2] + part[s][3]);
    __builtin_amdgcn_raw_buffer_store_b64(u32x2{pk2(v[0], v[1]), pk2(v[2], v[3])}, rout, vo + s * 16 * 1280, 0, OUT_AUX);
  }
}

__global__ __launch_bounds__(512, 2) void fused(Params p) {
  extern __shared__ __attribute__((aligned(16))) char smem[];
  ldsp lds = (ldsp)smem;
  u64 pc;
  asm volatile("s_getpc_b64 %0" : "=s"(pc));
  constexpr unsigned rest = CODE_BYTES;
  const int b = blockIdx.x, xcd = b & 7, idx = b >> 3, cg = xcd & 3, rg = xcd >> 2;
  const int w = __builtin_amdgcn_readfirstlane(threadIdx.x >> 6), lw = w - 4;
  const int l = threadIdx.x & 63;
  FINIT();
  STAMP(0);
  const int tm = (11 - idx) >> 31;
  const int jj = idx + ((20 * rg - 12) & tm), j3 = (jj * 43) >> 7, jr = jj - 3 * j3;
  const bool spare = jj == 39;
  const int ty = tm ? T_GLU : (jr == 0 ? T_Q : T_KV);
  const int rt = j3 + (4 * rg & ~tm);
  const int ctn = 2 * cg + jr - 1 + ((cg + 1) & tm);
  const int t2 = 28 * xcd - ((xcd - 4) & -rg) + ((idx - 4) & tm) + ((2 * j3 + jr - 1) & ~tm);
  const int rot0 = ROT;
  auto get_tag = [&]() {
    u64 e;
    asm volatile("s_load_dwordx2 %0, %1, 0x0\n\ts_waitcnt lgkmcnt(0)" : "=s"(e) : "s"(p.ws + EPOCH_OFF) : "memory");
    return make_tag(e);
  };

  constexpr int PQX = 2 * SLOT;
  const bool loader = w >= 4 && !spare;
  Warm wm(lds);
  u64 tag;
  auto mfma_start = [&]() {
    if (idx < 2) {
      const unsigned o = (idx * 256 + w * 64 + l) * 128;
      wm.touch((const GAS char*)pc + (o < rest ? o : 0u));
    }
    if (w == 0) {
      tag = get_tag();
      if (l < 32) *(LAS unsigned*)(lds + M_CNT + l * 4) = 0u;
      if (l == 0) *(LAS u64*)(lds + M_TAG) = tag;
      WAIT_LGKM0();
    }
  };
  auto lds_tag = [&]() {
    u32x2 v;
    asm volatile("ds_read_b64 %0, %1\n\ts_waitcnt lgkmcnt(0)" : "=v"(v) : "v"((unsigned)(size_t)(lds + M_TAG)) : "memory");
    const unsigned lo = v[0], hi = v[1];
    tag = (u64)(unsigned)__builtin_amdgcn_readfirstlane(hi) << 32 | (unsigned)__builtin_amdgcn_readfirstlane(lo);
  };
  if (__builtin_expect(ty == T_Q, 0)) {
    const int ct = cg;
    bf16x8 pq[10];
    if (loader) {
      u32x4 none[30];
      p1_loader<true>(p, lds, lw, 0, rt, ct, rot0, 0, none, [&](int kb) {
        const rsrc_t ra = mk_rsrc(p.ws, A2_BYTES);
        const unsigned vo = (unsigned)(((rt * 16 + lw * 4) * KB_ALL + 48 + (kb < 4 ? 4 * ct + kb : 16 + ct)) * 1024 + l * 16);
#pragma unroll
        for (int i = 0; i < 4; ++i) st128_sc1(ra, vo + i * KB_ALL * 1024, u32x4{UNSET, UNSET, UNSET, UNSET});
      }, [&]() {
        lds_tag();
        if (lw == 0 && l == 0) __hip_atomic_store(flag_ptr(p, FL_O + rt * 4 + ct), tag, __ATOMIC_RELAXED, __HIP_MEMORY_SCOPE_AGENT);
      }, [&]() {
        {
          const unsigned o = ((j3 * 4 + lw) * 26 + min(l, 25)) * 128;
          wm.touch((const GAS char*)pc + (o < rest ? o : 0u));
        }
        for (int i = lw * 64 + l; i < 2 * KVT_SZ / 16; i += 256)
          asm volatile("ds_write_b128 %0, %1" ::"v"((unsigned)(size_t)(lds + KVT) + i * 16), "v"(u32x4{0u, 0u, 0u, 0u}) : "memory");
        if (lw == 0) poll(flag_ptr(p, FL_KV + (l & 7) * 8 + 2 * ct + ((l >> 3) & 1)), tag);
        WAIT_LGKM0();
      });
      poll(flag_ptr(p, FL_G + (rt >> 1)), tag);
      const int tl = lw * 64 + l;
      *(LAS unsigned*)(lds + M_G + tl * 4) = __builtin_amdgcn_raw_buffer_load_b32(mk_rsrc(p.ws + G_OFF, G_BYTES), (rt * 256 + tl) * 4, 0, SC1);
      lds_wait(lds + M_END, 4);
#pragma unroll
      for (int t = 0; t < 10; ++t) { pq[t] = lds_b128(lds + PQX + (lw * 10 + t) * 1024 + l * 16); asm volatile("" : "+v"(pq[t])); }
    } else {
    mfma_start();
    f32x16 acc[5][2];
#pragma unroll
    for (int n = 0; n < 5; ++n) { acc[n][0] = f32x16{}; acc[n][1] = f32x16{}; }
    p1_main_q(lds, w, acc);
    STAMP(1);
    const Consts cn(1.f);
#pragma unroll
    for (int k = 9; k >= 0; --k)
#pragma unroll
      for (int s = 0; s < 2; ++s) {
        const f32x16& c = acc[k >> 1][s];
        const int o = 8 * (k & 1);
        const bf16x8 f = pack44(phi4(f32x4{c[o], c[o + 1], c[o + 2], c[o + 3]}, cn), phi4(f32x4{c[o + 4], c[o + 5], c[o + 6], c[o + 7]}, cn));
        if (k >= 5) *(LAS bf16x8*)(lds + PQX + (w * 10 + 5 * s + k - 5) * 1024 + l * 16) = f;
        else { pq[5 * s + k] = f; asm volatile("" : "+v"(pq[5 * s + k])); }
        if (k == 5 && s == 1) { WAIT_LGKM0(); lds_arrive(lds + M_END); }
      }
    STAMP(2);
    }
    {
      const int ch = min((int)threadIdx.x, 419), hg = ch >= 210;
      ldsp dp[2];
      bool dup[2];
#pragma unroll
      for (int u = 0; u < 2; ++u) {
        const int un = 2 * (ch - 210 * hg) + u, row = un / 5, dq = un % 5, hd = row < 80 ? row / 20 : row - 80, run = 5 * hd + dq;
        dp[u] = lds + KVT + hg * KVT_SZ + (32 * hd + (row < 80 ? row - 20 * hd : 20)) * KS + ((run >> 2) - hd) * 32 + (run & 1) * 16 + (run & 2) * 4;
        dup[u] = row >= 80;
      }
      const rsrc_t rk = mk_rsrc(p.ws + KVP_OFF, KVP_BYTES);
      u32x4 pv[8];
      unsigned todo = 0xFFu;
#pragma unroll 1
      for (int spin = 0; spin < SPIN_MAX && todo; ++spin) {
#pragma unroll
        for (int r = 0; r < 8; ++r)
          if (todo >> r & 1) pv[r] = __builtin_amdgcn_raw_buffer_load_b128(rk, 2 * ct * 3360 + ch * 16, r * 8 * 3360, SC1);
#pragma unroll
        for (int r = 0; r < 8; ++r)
          if (__all(max(max(pv[r][0], pv[r][1]), max(pv[r][2], pv[r][3])) != UNSET)) todo &= ~(1u << r);
        asm volatile("s_sleep 1" ::: "memory");
      }
      STAMP(3);
      f32x2 sum[4];
#pragma unroll
      for (int e = 0; e < 4; ++e) sum[e] = f32x2{0.f, 0.f};
#pragma unroll
      for (int r = 0; r < 8; ++r)
#pragma unroll
        for (int e = 0; e < 4; ++e) {
          const unsigned x = pv[r][e];
          sum[e] += f32x2{__uint_as_float(x << 16), __uint_as_float(x & 0xFFFF0000u)};
        }
#pragma unroll
      for (int u = 0; u < 2; ++u) {
        const u32x2 v = {pk2(sum[2 * u][0], sum[2 * u][1]), pk2(sum[2 * u + 1][0], sum[2 * u + 1][1])};
        *(LAS u32x2*)dp[u] = v;
        if (dup[u]) *(LAS u32x2*)(dp[u] + 4 * KS) = v;
      }
      WAIT_LGKM0();
      BARRIER();
      STAMP(4);
    }
  {
    const int l31 = l & 31, h = l >> 5, hg = w >> 2, wr = w & 3;
    const rsrc_t ra = mk_rsrc(p.ws, A2_BYTES);
    const unsigned vo = (unsigned)(((rt * 16 + wr * 4 + ((l >> 4) & 1)) * KB_ALL + 48) * 1024 + (2 * ct + hg) * 2048 + h * 256 + (l & 15) * 16);
    const unsigned vx = vo + 16384 - (2 * ct + hg) * 1536;
    bf16x8 af[4][2];
#pragma unroll
    for (int hd = 0; hd < 4; ++hd)
#pragma unroll
      for (int t = 0; t < 2; ++t) af[hd][t] = lds_b128(lds + (KVT + hg * KVT_SZ + l31 * KS + h * 16) + 32 * hd * KS + t * 32);
    float gv[2];
#pragma unroll
    for (int s = 0; s < 2; ++s) gv[s] = *(LAS float*)(lds + M_G + (64 * wr + 32 * s + l31) * 4);
    FST(18);
    SCHED_FENCE();
    f32x16 acc[2][4];
#pragma unroll
    for (int s = 0; s < 2; ++s)
#pragma unroll
      for (int hd = 3; hd >= 0; --hd) acc[s][hd] = mfma32(af[hd][1], pq[5 * s + hd + 1], mfma32(af[hd][0], pq[5 * s + hd], f32x16{}));
    u32x4 xs[2];
#pragma unroll
    for (int s = 0; s < 2; ++s) {
      u32x4 pc[4];
      unsigned ex[4][2];
#pragma unroll
      for (int hd = 3; hd >= 0; --hd) {
        const f32x16& c = acc[s][hd];
        float d0 = c[12], d1 = c[8];
        asm volatile("" : "+v"(d0), "+v"(d1));
        const float f = gv[s] * __builtin_amdgcn_rcpf((h ? d1 : d0) + 1e-6f);
        const f32x2 ff = {f, f};
        unsigned r[6];
#pragma unroll
        for (int i = 0; i < 6; ++i) { const f32x2 y = f32x2{c[2 * i], c[2 * i + 1]} * ff; r[i] = pk2(y[0], y[1]); }
        pc[hd] = u32x4{r[0], r[1], r[2], r[3]};
        ex[hd][0] = r[4]; ex[hd][1] = r[5];
        if (hd > 0) st128_sc1(ra, vo + s * 2 * KB_ALL * 1024 + hd * 512, pc[hd]);
      }
      u32x4 x;
#pragma unroll
      for (int i = 0; i < 4; ++i) x[i] = __builtin_amdgcn_permlane32_swap(ex[i >> 1][i & 1], ex[2 + (i >> 1)][i & 1], false, false)[0];
      st128_sc1(ra, vo + s * 2 * KB_ALL * 1024, pc[0]);
      xs[s] = x;
    }
    st128_sc1(ra, vx, xs[0]);
    st128_sc1(ra, vx + 2 * KB_ALL * 1024, xs[1]);
    STAMP(5);
    STAMP(6);
    FDUMP();
    return;
  }
  }
  const int ct = ctn;
  u32x4 held[15];
  if (__builtin_expect(loader, 1)) {
    u32x4 pre[30];
    p1_loader<false>(p, lds, lw, tm, rt, ct, rot0, t2, pre, [&](int) {
      st128_sc1(mk_rsrc(p.ws + KVP_OFF + (rt * 8 + ct) * 3360, 3360), lw * 1024 + l * 16, u32x4{UNSET, UNSET, UNSET, UNSET});
    }, [&]() {
      lds_tag();
      if (lw == 0 && l == 0) __hip_atomic_store(flag_ptr(p, FL_KV + rt * 8 + ct), tag, __ATOMIC_RELAXED, __HIP_MEMORY_SCOPE_AGENT);
    }, [&]() { lds_tag(); });
    const W2Src src(p, lw, t2);
    src.fill<0, 24>(lds + lw * 1024);
    if (ty == T_KV) {
#pragma unroll
      for (int i = 0; i < 30; ++i) pre[i] = pre_load(src, i);
      SCHED_FENCE();
      BARRIER(); BARRIER();
    } else {
      lds_wait(lds + M_END, 4);
    }
    WAIT_VM(0);
#pragma unroll
    for (int i = 0; i < 15; ++i) *(LAS u32x4*)(lds + (lw * 1024 + l * 16) + w2_off(24 + i)) = pre[i];
#pragma unroll
    for (int i = 0; i < 15; ++i) held[i] = pre[15 + i];
    WAIT_LGKM0();
  } else {
    mfma_start();
    if (spare) {
      BARRIER();
      lds_tag();
      const int l15 = l & 15, r0 = cg * 512 + w * 64 + (l >> 4);
      const rsrc_t rx = mk_rsrc(p.x, S * D * 2), rgw = mk_rsrc(p.gw, D * 2), rg_ = mk_rsrc(p.ws + G_OFF, G_BYTES);
      u32x4 gw[5], v[2][4][5];
#pragma unroll
      for (int c = 0; c < 5; ++c) gw[c] = __builtin_amdgcn_raw_buffer_load_b128(rgw, l15 * 16 + c * 256, 0, 0);
      auto ld = [&](int it) {
#pragma unroll
        for (int u = 0; u < 4; ++u)
#pragma unroll
          for (int c = 0; c < 5; ++c) v[it & 1][u][c] = __builtin_amdgcn_raw_buffer_load_b128(rx, (r0 + 4 * u) * 1280 + l15 * 16 + c * 256, it * 16 * 1280, 0);
      };
      ld(0);
#pragma unroll
      for (int it = 0; it < 4; ++it) {
        if (it < 3) ld(it + 1);
        SCHED_FENCE();
        float keep = 0.f;
#pragma unroll
        for (int u = 0; u < 4; ++u) {
          float sacc = 0.f;
#pragma unroll
          for (int c = 0; c < 5; ++c)
#pragma unroll
            for (int e = 0; e < 4; ++e) {
              const unsigned xw = v[it & 1][u][c][e], ge = gw[c][e];
              sacc = __builtin_amdgcn_fdot2_f32_bf16(__builtin_bit_cast(bf16x2, xw), __builtin_bit_cast(bf16x2, ge), sacc, false);
            }
          sacc += __shfl_xor(sacc, 1); sacc += __shfl_xor(sacc, 2); sacc += __shfl_xor(sacc, 4); sacc += __shfl_xor(sacc, 8);
          if (l15 == u) keep = sacc;
        }
        if (l15 < 4) __builtin_amdgcn_raw_buffer_store_b32(__builtin_bit_cast(unsigned, sigmoidf(keep)), rg_, (r0 + 16 * it + 4 * l15) * 4, 0, SC1);
        SCHED_FENCE();
      }
      publish(p, lds, FL_G + cg, tag, 8);
      STAMP(6);
      FDUMP();
      if (cg == 0 && w == 0) {
#pragma unroll 1
        for (int i = 0; i < 4; ++i) poll(flag_ptr(p, i * 64 + l), tag);
        if (l == 0) __hip_atomic_store((GAS u64*)(p.ws + EPOCH_OFF), tag, __ATOMIC_RELAXED, __HIP_MEMORY_SCOPE_AGENT);
      }
      return;
    }
      f32x4 acc[4][10];
#pragma unroll
      for (int i = 0; i < 4; ++i)
#pragma unroll
        for (int j = 0; j < 10; ++j) acc[i][j] = f32x4{};
      if (ty == T_GLU) {
        const Consts cn(-1.f);
        const rsrc_t ra = mk_rsrc(p.ws, A2_BYTES);
        const unsigned vo = (unsigned)((rt * 10 * KB_ALL + ct * 4 + w) * 1024 + l * 16);
        auto fin = [&](int j) {
          st128_sc1(ra, vo + j * KB_ALL * 1024, __builtin_bit_cast(u32x4, pack44(glu4(acc[0][j], acc[2][j], cn), glu4(acc[1][j], acc[3][j], cn))));
        };
        p1_main16<T_GLU, true>(lds, w, acc, fin);
        STAMP(2);
        lds_tag();
        publish(p, lds, FL_H + rt * 12 + ct, tag, 4);
      } else {
        int goff[2];
#pragma unroll
        for (int i = 0; i < 2; ++i) {
          const int en = min(2 * (int)threadIdx.x + i, 419), row = en / 5, dq = en % 5;
          const int hd = row < 80 ? row / 20 : row - 80, d0 = 20 * hd + 4 * dq, ti = d0 >> 4, tj = row >> 4;
          const int tile = row >= 80 ? 9 : ti == tj ? ti : (ti == 0 || ti == 4) ? 8 : 4 + ti;
          goff[i] = tile * 1024 + (((d0 & 15) >> 2) * 16 + (row < 80 ? row & 15 : ti)) * 16;
          asm volatile("" : "+v"(goff[i]));
        }
        p1_main16<T_KV, false>(lds, w, acc, [](int) {});
        STAMP(1);
        const Consts cn(1.f);
        const int l15 = l & 15, g = l >> 4;
        ldsp tp = lds + (w * 10240 + l * 16);
        bf16x8 vf[2][5];
#pragma unroll
        for (int u = 0; u < 2; ++u)
#pragma unroll
          for (int j = 0; j < 5; ++j) vf[u][j] = pack44(acc[2 * u][5 + j], acc[2 * u + 1][5 + j]);
        f32x4 z = {}, dg[5], lo[5], hi[5];
        SCHED_FENCE();
#pragma unroll
        for (int i = 0; i < 5; ++i) {
          const unsigned o1 = l15 == i ? 0x3F803F80u : 0u;
          const bf16x8 one = __builtin_bit_cast(bf16x8, u32x4{o1, o1, o1, o1});
          dg[i] = f32x4{}; lo[i] = f32x4{}; hi[i] = f32x4{};
#pragma unroll
          for (int u = 0; u < 2; ++u) {
            const bf16x8 kf = pack44(phi4(acc[2 * u][i], cn), phi4(acc[2 * u + 1][i], cn));
            if (i > 0) lo[i] = mfma16(kf, vf[u][i - 1], lo[i]);
            if (i < 4) hi[i] = mfma16(kf, vf[u][i + 1], hi[i]);
            dg[i] = mfma16(kf, vf[u][i], dg[i]);
            z = mfma16(kf, one, z);
            if (i > 0) {
              if (u == 0) *(LAS f32x4*)(tp + (i - 1) * 1024) = dg[i - 1];
              else if (i > 1) *(LAS f32x4*)(tp + (3 + i) * 1024) = g < i - 1 ? lo[i - 1] : hi[i - 1];
            }
            SCHED_FENCE();
          }
          if (i == 0) { FST(18); lds_wait(lds + M_END, 4); }
        }
        FST(19);
        *(LAS f32x4*)(tp + 8 * 1024) = l15 < 8 ? hi[0] : lo[4];
        *(LAS f32x4*)(tp + 4 * 1024) = dg[4];
        *(LAS f32x4*)(tp + 9 * 1024) = z;
        WAIT_LGKM0();
        FST(20);
        BARRIER();
        FST(21);
        const rsrc_t rk = mk_rsrc(p.ws + KVP_OFF + (rt * 8 + ct) * 3360, 3360);
        f32x4 part[2][4];
#pragma unroll
        for (int i = 0; i < 2; ++i)
#pragma unroll
          for (int ww = 0; ww < 4; ++ww) part[i][ww] = *(LAS f32x4*)(lds + goff[i] + ww * 10240);
        SCHED_FENCE();
        u32x4 res = __builtin_bit_cast(u32x4, pack44((part[0][0] + part[0][1]) + (part[0][2] + part[0][3]), (part[1][0] + part[1][1]) + (part[1][2] + part[1][3])));
#pragma unroll
        for (int e = 0; e < 4; ++e) res[e] = min(res[e], UNSET - 1);
        st128_sc1(rk, threadIdx.x * 16, res);
        WAIT_LGKM0();
        FST(22);
        BARRIER();
        STAMP(2);
      }
      STAMP(3);
      const W2Src src(p, w, t2);
#pragma unroll
      for (int i = 0; i < 15; ++i) held[i] = src.load(39 + 2 * i);
      SCHED_FENCE();
  }
  int t2l = __builtin_amdgcn_readfirstlane(t2);
  asm volatile("" : "+s"(t2l));
  const int rt2 = (t2l * 205) >> 11;
  p2_main(p, lds, w, rt2, t2l - 10 * rt2, tag, held);
  STAMP(6);
  FDUMP();
}

#define STAMP_PTR nullptr
extern "C" void kernel_launch(void* const* d_in, const int* in_sizes, int n_in, void* d_out, int out_size, void* d_ws, size_t ws_size,
                              hipStream_t stream) {
  Params p;
  p.x = (const u16*)d_in[0]; p.wqkv = (const u16*)d_in[1]; p.wo = (const u16*)d_in[2];
  p.wg = (const u16*)d_in[4]; p.wu = (const u16*)d_in[5]; p.wd = (const u16*)d_in[6]; p.gw = (const u16*)d_in[7];
  p.out = (u16*)d_out; p.ws = (char*)d_ws; p.stamps = STAMP_PTR;
  (void)hipFuncSetAttribute((const void*)fused, hipFuncAttributeMaxDynamicSharedMemorySize, LDS_BYTES);
  hipLaunchKernelGGL(fused, dim3(256), dim3(512), LDS_BYTES, stream, p);
}
```

```cpp
#include <hip/hip_runtime.h>
#include <hip/hip_bf16.h>

typedef unsigned short u16;
typedef unsigned long long u64;
typedef __bf16 bf16x8 __attribute__((ext_vector_type(8)));
typedef __bf16 bf16x4 __attribute__((ext_vector_type(4)));
typedef __bf16 bf16x2 __attribute__((ext_vector_type(2)));
typedef float f32x16 __attribute__((ext_vector_type(16)));
typedef float f32x4 __attribute__((ext_vector_type(4)));
typedef unsigned u32x4 __attribute__((ext_vector_type(4)));
typedef unsigned u32x2 __attribute__((ext_vector_type(2)));
#define LAS __attribute__((address_space(3)))
#define GAS __attribute__((address_space(1)))
typedef LAS char* ldsp;
typedef __amdgpu_buffer_rsrc_t rsrc_t;

constexpr int S = 2048, D = 640, F = 1536;
constexpr float LOG2E = 1.4426950408889634f;
constexpr int KB_ALL = 68;
constexpr unsigned A2_BYTES = 128u * KB_ALL * 1024;
constexpr unsigned KVP_OFF = A2_BYTES, KVP_BYTES = 8 * 8 * 3360;
constexpr unsigned G_OFF = KVP_OFF + KVP_BYTES, G_BYTES = S * 4;
constexpr unsigned FLAG_OFF = G_OFF + G_BYTES;
constexpr int NFLAG = 284;
constexpr unsigned EPOCH_OFF = FLAG_OFF + NFLAG * 128;
constexpr unsigned WS_USED = EPOCH_OFF + 128;
constexpr int FL_H = 0, FL_KV = 156, FL_O = 220, FL_G = 252;
constexpr int SC1 = 16;
constexpr unsigned UNSET = 0xFFFFFFFFu;
constexpr int SLOT = 53248, MISC = 3 * SLOT, LDS_BYTES = 163840;
constexpr int M_DUMMY = MISC, M_CNT = MISC + 256  , M_END = MISC + 260  , M_RND = MISC + 320  , M_TAG = MISC + 384, M_GW = MISC + 512  , M_G = MISC + 2048  ;
constexpr int KS = 80, KVT = SLOT, KVT_SZ = 4 * 32 * KS;
constexpr int NST1 = 10;
constexpr int SPIN_MAX = 1 << 16;
#define A2AUX SC1
#define GLU_PACE 0
#define OUT_AUX 0
#define P2_AW 3
#define P2_X3 99
#define P2_PRIO 2
#define P2_A0 1
#define P2_RAMP 2
#define P2_G0 2
#define P2_G1 1
#define GATE_HG 3
constexpr unsigned CODE_BYTES = 47000;
#define GLU_PEEL 1
#define P2_GATE 10
#define P2_POS 0
#define ROT ((2 * (cg & 1) + 5 * (((cg >> 1) ^ rg) & 1) + rt % 3) % 10)
#define W2_ST 4

struct Params {
  const u16 *x, *wqkv, *wo, *wg, *wu, *wd, *gw;
  u16* out;
  char* ws;
  u64* stamps;
};

#define STAMP(i) do {} while (0)
#define FST(i) do {} while (0)
#define FSTD(i) do {} while (0)
#define FINIT() do {} while (0)
#define FDUMP() do {} while (0)

#define WAIT_VM(n) asm volatile("s_waitcnt vmcnt(" #n ")" ::: "memory")
#define WAIT_LGKM0() asm volatile("s_waitcnt lgkmcnt(0)" ::: "memory")
#define BARRIER() do { asm volatile("" ::: "memory"); __builtin_amdgcn_s_barrier(); asm volatile("" ::: "memory"); } while (0)
#define SCHED_FENCE() __builtin_amdgcn_sched_barrier(0)
#define ORDER_FENCE() do { asm volatile("" ::: "memory"); __builtin_amdgcn_sched_barrier(0); } while (0)
#define SG_MFMA(n) __builtin_amdgcn_sched_group_barrier(0x8, n, 0)
#define SG_DS(n) __builtin_amdgcn_sched_group_barrier(0x100, n, 0)
#define SG_VALU(n) __builtin_amdgcn_sched_group_barrier(0x2, n, 0)
#define SG_DW(n) __builtin_amdgcn_sched_group_barrier(0x200, n, 0)
#define SG_VM(n) __builtin_amdgcn_sched_group_barrier(0x20, n, 0)

__device__ __forceinline__ rsrc_t mk_rsrc(const void* p, unsigned bytes) { return __builtin_amdgcn_make_buffer_rsrc((void*)p, 0, bytes, 0x00020000); }
#define DMA_AUX 0
__device__ __forceinline__ void dma16(rsrc_t r, ldsp dst, unsigned voff, unsigned soff) { __builtin_amdgcn_raw_ptr_buffer_load_lds(r, dst, 16, voff, soff, 0, DMA_AUX); }
__device__ __forceinline__ bf16x8 lds_b128(ldsp a) { return *(LAS bf16x8*)a; }
__device__ __forceinline__ bf16x8 lds_tr2(ldsp a0, ldsp a1) {
  bf16x4 lo = __builtin_amdgcn_ds_read_tr16_b64_v4bf16((LAS bf16x4*)a0);
  bf16x4 hi = __builtin_amdgcn_ds_read_tr16_b64_v4bf16((LAS bf16x4*)a1);
  return __builtin_shufflevector(lo, hi, 0, 1, 2, 3, 4, 5, 6, 7);
}
__device__ __forceinline__ f32x16 mfma32(bf16x8 a, bf16x8 b, f32x16 c) { return __builtin_amdgcn_mfma_f32_32x32x16_bf16(a, b, c, 0, 0, 0); }
__device__ __forceinline__ f32x4 mfma16(bf16x8 a, bf16x8 b, f32x4 c) { return __builtin_amdgcn_mfma_f32_16x16x32_bf16(a, b, c, 0, 0, 0); }
__device__ __forceinline__ unsigned pk2(float a, float b) {
  bf16x2 v = {(__bf16)a, (__bf16)b};
  return __builtin_bit_cast(unsigned, v);
}
__device__ __forceinline__ bf16x8 pack8(const f32x16& c, int o) {
  u32x4 r = {pk2(c[o], c[o + 1]), pk2(c[o + 2], c[o + 3]), pk2(c[o + 4], c[o + 5]), pk2(c[o + 6], c[o + 7])};
  return __builtin_bit_cast(bf16x8, r);
}
__device__ __forceinline__ bf16x8 pack44(const f32x4& a, const f32x4& b) {
  u32x4 r = {pk2(a[0], a[1]), pk2(a[2], a[3]), pk2(b[0], b[1]), pk2(b[2], b[3])};
  return __builtin_bit_cast(bf16x8, r);
}
__device__ __forceinline__ float phi(float t) {
  const float e = __builtin_amdgcn_exp2f(t * LOG2E);
  return __builtin_amdgcn_fmed3f(t, 1e-6f, __builtin_inff()) + __builtin_amdgcn_fmed3f(e, 0.f, 1.f);
}
__device__ __forceinline__ float sigmoidf(float t) { return __builtin_amdgcn_rcpf(1.f + __builtin_amdgcn_exp2f(-LOG2E * t)); }
typedef float f32x2 __attribute__((ext_vector_type(2)));
struct Consts {
  f32x2 l2e;
  float eps, big;
  __device__ __forceinline__ Consts(float sign) : l2e{sign * LOG2E, sign * LOG2E}, eps(1e-6f), big(3.4028234664e38f) {
    asm volatile("" : "+s"(l2e), "+s"(eps), "+v"(big));
  }
};
__device__ __forceinline__ f32x4 phi4(f32x4 t, const Consts& c) {
  const f32x2 a = f32x2{t[0], t[1]} * c.l2e, b = f32x2{t[2], t[3]} * c.l2e;
  f32x2 m0, m1, e0, e1;
#pragma unroll
  for (int i = 0; i < 2; ++i) {
    m0[i] = __builtin_amdgcn_fmed3f(t[i], c.eps, c.big);
    m1[i] = __builtin_amdgcn_fmed3f(t[2 + i], c.eps, c.big);
    e0[i] = __builtin_amdgcn_fmed3f(__builtin_amdgcn_exp2f(a[i]), 0.f, 1.f);
    e1[i] = __builtin_amdgcn_fmed3f(__builtin_amdgcn_exp2f(b[i]), 0.f, 1.f);
  }
  const f32x2 r0 = m0 + e0, r1 = m1 + e1;
  return f32x4{r0[0], r0[1], r1[0], r1[1]};
}
__device__ __forceinline__ f32x4 glu4(f32x4 g, f32x4 u, const Consts& c) {
  f32x2 a = f32x2{g[0], g[1]} * c.l2e, b = f32x2{g[2], g[3]} * c.l2e;
#pragma unroll
  for (int i = 0; i < 2; ++i) { a[i] = __builtin_amdgcn_exp2f(a[i]); b[i] = __builtin_amdgcn_exp2f(b[i]); }
  a += 1.f; b += 1.f;
#pragma unroll
  for (int i = 0; i < 2; ++i) { a[i] = __builtin_amdgcn_rcpf(a[i]); b[i] = __builtin_amdgcn_rcpf(b[i]); }
  a *= f32x2{u[0], u[1]}; b *= f32x2{u[2], u[3]};
  return f32x4{a[0], a[1], b[0], b[1]};
}
__device__ __forceinline__ void st128_sc1(rsrc_t r, unsigned off, u32x4 v) { __builtin_amdgcn_raw_buffer_store_b128(v, r, off, 0, SC1); }

__device__ __forceinline__ u64 make_tag(u64 e) { return ((e ^ 0x9E3779B97F4A7C15ull) * 0xBF58476D1CE4E5B9ull) | 1ull; }
__device__ __forceinline__ GAS u64* flag_ptr(const Params& p, int f) { return (GAS u64*)(p.ws + FLAG_OFF + (size_t)f * 128); }
__device__ __forceinline__ u64 ld_flag(GAS u64* f) { return __hip_atomic_load(f, __ATOMIC_RELAXED, __HIP_MEMORY_SCOPE_AGENT); }
__device__ __forceinline__ void poll(GAS u64* f, u64 tag) {
#pragma unroll 1
  for (int spin = 0; spin < SPIN_MAX; ++spin) {
    if (__all(ld_flag(f) == tag)) break;
    __builtin_amdgcn_s_sleep(2);
  }
  __builtin_amdgcn_fence(__ATOMIC_ACQUIRE, "wavefront");
}
__device__ __forceinline__ void lds_arrive(ldsp c) {
  asm volatile("s_mov_b64 exec, 1\n\tds_add_u32 %0, %1\n\ts_mov_b64 exec, -1" ::"v"((unsigned)(size_t)c), "v"(1u) : "memory");
}
__device__ __forceinline__ void lds_wait(ldsp c, unsigned n) {
#pragma unroll 1
  for (int spin = 0; spin < SPIN_MAX; ++spin) {
    unsigned v;
    asm volatile("ds_read_b32 %0, %1\n\ts_waitcnt lgkmcnt(0)" : "=v"(v) : "v"((unsigned)(size_t)c) : "memory");
    if (__builtin_amdgcn_readfirstlane(v) >= n) break;
    __builtin_amdgcn_s_sleep(1);
  }
}
__device__ __forceinline__ void publish(const Params& p, ldsp lds, int f, u64 tag, unsigned n) {
  WAIT_VM(0);
  if ((threadIdx.x & 63) == 0) {
    const unsigned old = __hip_atomic_fetch_add((LAS unsigned*)(lds + M_CNT), 1u, __ATOMIC_RELAXED, __HIP_MEMORY_SCOPE_WORKGROUP);
    if (old == n - 1) __hip_atomic_store(flag_ptr(p, f), tag, __ATOMIC_RELAXED, __HIP_MEMORY_SCOPE_AGENT);
  }
}

enum { T_Q = 0, T_KV = 1, T_GLU = 2 };

__device__ __forceinline__ constexpr int w2_off(int s) { return (SLOT + (s % 39) * 4096) % (3 * SLOT); }

struct W2Src {
  rsrc_t rd, ro;
  unsigned wv, wv4, ws0, wo0, wo1;
  __device__ __forceinline__ W2Src(const Params& p, int piece, int t2) {
    int l = threadIdx.x & 63;
    t2 = __builtin_amdgcn_readfirstlane(t2);
    asm volatile("" : "+s"(piece), "+s"(t2), "+v"(l));
    const int ct = t2 - 10 * ((t2 * 205) >> 11);
    rd = mk_rsrc(p.wd, F * D * 2);
    ro = mk_rsrc(p.wo, D * D * 2);
    wv = (l >> 3) * 1280 + (((l & 7) ^ ((l >> 4) << 1)) << 4);
    wv4 = wv + (l >> 5) * 36 * 1280;
    ws0 = (unsigned)(8 * piece) * 1280 + ct * 128;
    wo0 = (unsigned)(20 * (piece >> 1) + 8 * (piece & 1)) * 1280 + ct * 128;
    wo1 = (unsigned)(80 * (piece >> 1) + 16 + 20 * (piece & 1)) * 1280 + ct * 128;
  }
  __device__ __forceinline__ u32x4 load(int s) const {
    if (s < 48) return __builtin_amdgcn_raw_buffer_load_b128(rd, wv, ws0 + (unsigned)s * 32 * 1280, 0);
    if (s < 64) return __builtin_amdgcn_raw_buffer_load_b128(ro, wv, wo0 + (unsigned)(s - 48) * 40 * 1280, 0);
    return __builtin_amdgcn_raw_buffer_load_b128(ro, wv4, wo1 + (unsigned)(s - 64) * 160 * 1280, 0);
  }
  template <int S0, int S1> __device__ __forceinline__ void fill(ldsp dst) const {
#pragma unroll
    for (int s = S0; s < S1; ++s) dma16(rd, dst + w2_off(s), wv, ws0 + (unsigned)s * 32 * 1280);
  }
};

__device__ __forceinline__ u32x4 pre_load(const W2Src& src, int i) { return i < 15 ? src.load(24 + i) : src.load(40 + 2 * (i - 15)); }

template <bool ISQ, class Reset, class Raise, class Gap>
__device__ __forceinline__ float p1_loader(const Params& p, ldsp lds, int lw, int tm, int rt, int ct, int k0, int t2, u32x4 (&pre)[30],
                                          Reset reset, Raise raise, Gap gap) {
  const bool tb = !ISQ && tm != 0;
  auto sel = [&](int a, int b) { return ISQ ? b : b ^ ((a ^ b) & tm); };
  auto selp = [&](const u16* a, const u16* b) { return ISQ ? b : (const u16*)((u64)b ^ (((u64)a ^ (u64)b) & (u64)(long long)tm)); };
  const int l = threadIdx.x & 63;
  const int i8 = sel(5 * lw, 8 * lw + 3);
  const unsigned xe = (l >> 3) * 1280 + (((l & 7) ^ (l >> 4)) << 4);
  const unsigned xa = (xe ^ ((i8 & 1) << 6)) + (unsigned)(sel(160, 256) * rt + 8 * i8) * 1280, xb = (xa ^ 64) + 8 * 1280;
  const int d8 = i8 * 1024;
  const rsrc_t rx = mk_rsrc(p.x, S * D * 2);
  FST(0);
#pragma unroll
  for (int j = 0; j < 5; ++j) dma16(rx, lds + d8 + j * 1024, (j & 1) ? xb : xa, k0 * 128 + (j >> 1) * 16 * 1280);
  SCHED_FENCE();
  const rsrc_t r0 = mk_rsrc(selp(p.wg, p.wqkv), sel(D * F * 2, D * 3 * D * 2));
  const rsrc_t r1 = mk_rsrc(selp(p.wu, p.wqkv), sel(D * F * 2, D * 3 * D * 2));
  const rsrc_t r2 = mk_rsrc(selp(p.wu, p.x), sel(D * F * 2, S * D * 2));
  unsigned v[8];
  {
    const int r = l >> 4;
    const unsigned g0 = (16 * lw + r) * 3072 + ct * 256 + (((l & 15) ^ (r << 1)) << 4);
    const int colA = ISQ ? 320 * ct : 1280 + 160 * ct, colB = ISQ ? 160 : 1280;
#pragma unroll
    for (int j = 0; j < 5; ++j) {
      const int c = 64 * j + l, rr = (c * 3277) >> 16;
      int cc = c - 20 * rr;
      if (!ISQ) cc ^= (rr >> 2) & 2;
      const unsigned a = (16 * lw + rr) * 3840 + colA + cc * 16 + (cc < 10 ? 0 : colB - 160);
      v[j] = sel((g0 ^ ((j & 2) << 6)) + (j & 3) * 4 * 3072, a);
    }
    v[5] = sel(v[1], xb - 4 * 8 * 1280);
    v[6] = sel(v[2], xa - 2 * 8 * 1280);
    v[7] = sel(v[3], xb - 2 * 8 * 1280);
  }
  const int d0 = sel(20480 + 4096 * lw, 32768 + 5120 * lw), d4 = d0 + sel(16384, 4096), d5 = sel(d4 + 1024, d8 - 3072);
  const unsigned wstep = sel(64 * 3072, 64 * 3840), mstep = sel(64 * 3072, 128);
  auto issue_a = [&](int st, int so) {
    ldsp sb = lds + so;
#pragma unroll
    for (int j = 0; j < 5; ++j) dma16(rx, sb + d8 + j * 1024, (j & 1) ? xb : xa, st * 128 + (j >> 1) * 16 * 1280);
#pragma unroll
    for (int j = 0; j < 2; ++j) dma16(r2, sb + d5 + j * 1024, v[5 + j], st * mstep);
    ORDER_FENCE();
  };
  auto issue_b = [&](int st, int so) {
    ldsp sb = lds + so;
    dma16(r2, sb + d5 + 2048, v[7], st * mstep);
#pragma unroll
    for (int j = 0; j < 4; ++j) dma16(r0, sb + d0 + j * 1024, v[j], st * wstep);
    dma16(r1, sb + d4, v[4], st * wstep);
    ORDER_FENCE();
  };
  auto rot = [&](int j) { const int t = k0 + j; return t >= NST1 ? t - NST1 : t; };
  float gs = 0.f;
  auto gate = [&](int st, int so) {
    if (!ISQ) return;
    const int row = 64 * ct + 16 * lw + (l >> 2);
    const unsigned a = (unsigned)(size_t)(lds + so) + row * 128 + (((2 * (l & 3)) ^ ((row >> 1) & 7)) << 4);
    const unsigned ga = (unsigned)(size_t)(lds + M_GW) + st * 128 + (l & 3) * 32;
    u32x4 x0, x1, g0, g1;
    asm volatile("ds_read_b128 %0, %4\n\tds_read_b128 %1, %5\n\tds_read_b128 %2, %6\n\tds_read_b128 %3, %6 offset:16\n\ts_waitcnt lgkmcnt(0)"
                 : "=&v"(x0), "=&v"(x1), "=&v"(g0), "=&v"(g1) : "v"(a), "v"(a ^ 16), "v"(ga) : "memory");
#pragma unroll
    for (int e = 0; e < 4; ++e) {
      const unsigned xa_ = x0[e], xb_ = x1[e], ge = g0[e], gf = g1[e];
      gs = __builtin_amdgcn_fdot2_f32_bf16(__builtin_bit_cast(bf16x2, xa_), __builtin_bit_cast(bf16x2, ge), gs, false);
      gs = __builtin_amdgcn_fdot2_f32_bf16(__builtin_bit_cast(bf16x2, xb_), __builtin_bit_cast(bf16x2, gf), gs, false);
    }
  };
  if (ISQ && lw < 2) dma16(mk_rsrc(p.gw, D * 2), lds + M_GW + lw * 1024, l * 16, lw * 1024);
#pragma unroll
  for (int j = 0; j < 2; ++j) dma16(r2, lds + d5 + j * 1024, v[5 + j], k0 * mstep);
  issue_b(k0, 0);
  FST(1);
  issue_a(rot(1), SLOT);
  FST(2);
  WAIT_VM(7);
  FST(3);
  BARRIER();
  issue_b(rot(1), SLOT); issue_a(rot(2), 2 * SLOT);
  gate(k0, 0);
  WAIT_VM(7);
  BARRIER();
  issue_b(rot(2), 2 * SLOT); issue_a(rot(3), 0); issue_b(rot(3), 0);
  gate(rot(1), SLOT);
  WAIT_VM(13);
  if (tb) {
    const W2Src src(p, lw, t2);
#pragma unroll
    for (int j = 1; j < NST1 - 3; ++j) {
      BARRIER();
      issue_a(rot(j + 3), (j % 3) * SLOT); issue_b(rot(j + 3), (j % 3) * SLOT);
#pragma unroll
      for (int i = 5 * (j - 1); i < 5 * j; ++i) pre[i] = pre_load(src, i);
      SCHED_FENCE();
      if (j == 1) WAIT_VM(18); else WAIT_VM(23);
    }
    BARRIER();
    gap();
    WAIT_VM(5);
  } else {
    int so = SLOT;
#pragma unroll 1
    for (int j = 1; j < NST1 - 3; ++j) {
      BARRIER();
      if (!ISQ && j == 3) raise();
      issue_a(rot(j + 3), so); issue_b(rot(j + 3), so);
      so = so == 2 * SLOT ? 0 : so + SLOT;
      if (ISQ) {
        if (j < 6) reset(j - 1);
        gate(rot(j + 1), so);
        if (j == 1) WAIT_VM(17); else if (j < 6) WAIT_VM(21); else WAIT_VM(13);
      } else {
        if (j == 1) { reset(0); WAIT_VM(14); } else WAIT_VM(13);
      }
    }
    BARRIER();
    if (ISQ) raise();
    gate(rot(8), 2 * SLOT);
    gap();
    WAIT_VM(0);
  }
  BARRIER();
  gate(rot(9), 0);
  return gs;
}

struct Warm {
  unsigned sink;
  __device__ __forceinline__ Warm(ldsp lds) : sink((unsigned)(size_t)(lds + M_DUMMY)) {}
  __device__ __forceinline__ void touch(const GAS char* t) {
    unsigned keep;
    asm volatile("s_mov_b32 %0, m0\n\ts_mov_b32 m0, %2\n\ts_nop 0\n\tglobal_load_lds_dword %1, off\n\ts_mov_b32 m0, %0" : "=&s"(keep) : "v"(t), "s"(sink) : "memory");
  }
};

__device__ __forceinline__ void stage_barrier(int st) {
  asm volatile("s_waitcnt lgkmcnt(0)\n\ts_cmp_eq_u32 %0, %1\n\ts_cbranch_scc1 1\n\ts_barrier" ::"s"(st), "i"(NST1 - 1) : "memory", "scc");
}
struct FragQ { bf16x8 w[5], x[2]; };
__device__ __forceinline__ void p1_main_q(ldsp lds, int w, f32x16 (&acc)[5][2]) {
  const int l = threadIdx.x & 63, l31 = l & 31, h = l >> 5;
  const int xa = (64 * w + l31) * 128 + ((h ^ ((l31 >> 1) & 7)) << 4);
  const int wa = 32768 + (8 * h + ((l & 15) >> 2)) * 320 + ((l >> 4) & 1) * 32 + (l & 3) * 8;
  auto load = [&](FragQ& f, ldsp sb, int ks) {
    ldsp xp = sb + (xa ^ (ks << 5));
    ldsp wp = sb + wa + ks * 16 * 320;
    f.x[0] = lds_b128(xp);
    f.w[0] = lds_tr2(wp, wp + 1280);
    f.x[1] = lds_b128(xp + 4096);
#pragma unroll
    for (int n = 1; n < 5; ++n) f.w[n] = lds_tr2(wp + n * 64, wp + n * 64 + 1280);
  };
  auto mma = [&](const FragQ& f) {
#pragma unroll
    for (int n = 0; n < 5; ++n)
#pragma unroll
      for (int s = 0; s < 2; ++s) acc[n][s] = mfma32(f.w[n], f.x[s], acc[n][s]);
#pragma unroll
    for (int i = 0; i < 6; ++i) { SG_MFMA(1); SG_DS(2); }
    SG_MFMA(4);
    SCHED_FENCE();
  };
  FST(0);
  BARRIER();
  FST(1);
  FragQ f0, f1;
  int so = 0;
  load(f0, lds, 0);
  SCHED_FENCE();
#pragma unroll 1
  for (int j = 0; j < NST1; ++j) {
    ldsp sb = lds + so;
    load(f1, sb, 1); mma(f0);
    load(f0, sb, 2); mma(f1);
    load(f1, sb, 3); mma(f0);
    so = so == 2 * SLOT ? 0 : so + SLOT;
    stage_barrier(j);
    FSTD(2 + j);
    SCHED_FENCE();
    load(f0, lds + so, 0); mma(f1);
  }
  WAIT_LGKM0();
}

template <int TY, bool PEEL, class Tail>
__device__ __forceinline__ void p1_main16(ldsp lds, int w, f32x4 (&acc)[4][10], Tail tail) {
  constexpr bool TB = TY == T_GLU;
  const int l = threadIdx.x & 63, l15 = l & 15, g = l >> 4, q = l15 >> 2;
  const int xa = ((TB ? 0 : 64 * w) + l15) * 128 + ((g ^ ((l15 >> 1) & 7)) << 4);
  int wa0, wa1;
  if (TB) {
    wa0 = 20480 + (8 * g + q) * 256 + (((2 * w) ^ (q + 4 * (g & 1))) << 5) + (l & 3) * 8;
    wa1 = wa0 ^ 32;
  } else {
    wa0 = 32768 + (8 * g + q) * 320 + (l & 3) * 8 + 32 * (g & 1);
    wa1 = wa0 - 64 * (g & 1);
  }
  ldsp px0 = lds + xa, px1 = lds + (xa ^ 64), pw0 = lds + wa0, pw1 = lds + wa1;
  auto ldx = [&](int u, int t) { return lds_b128((u ? px1 : px0) + t * 2048); };
  auto ldw = [&](int u, int t) {
    ldsp a = ((t & 1) ? pw1 : pw0) + (TB ? u * 8192 + (t >> 1) * 16384 : u * 10240 + t * 32);
    return lds_tr2(a, a + (TB ? 1024 : 1280));
  };
  auto lda = [&](int u, int i) { return TB ? ldw(u, i) : ldx(u, i); };
  auto ldb = [&](int u, int j) { return TB ? ldx(u, j) : ldw(u, j); };
  auto mma = [&](const bf16x8 (&a)[4], const bf16x8& b, int j) {
#pragma unroll
    for (int i = 0; i < 4; ++i) acc[i][j] = mfma16(a[i], b, acc[i][j]);
    SG_MFMA(1); SG_DS(2); SG_MFMA(1); SG_DS(2); SG_MFMA(1); SG_DS(2); SG_MFMA(1);
    SCHED_FENCE();
  };
  FST(0);
  BARRIER();
  FST(1);
  bf16x8 a0[4], a1[4], b0[10], b1[10];
#pragma unroll
  for (int i = 0; i < 4; ++i) a0[i] = lda(0, i);
#pragma unroll
  for (int j = 0; j < 5; ++j) b0[j] = ldb(0, j);
  SCHED_FENCE();
  int ph = 0;
#pragma unroll 1
  for (int st = 0; st < (PEEL ? NST1 - 1 : NST1); ++st) {
#pragma unroll
    for (int j = 0; j < 10; ++j) {
      if (j < 5) b0[j + 5] = ldb(0, j + 5); else b1[j - 5] = ldb(1, j - 5);
      if (j >= 2 && j < 6) a1[j - 2] = lda(1, j - 2);
      mma(a0, b0[j], j);
    }
#pragma unroll
    for (int j = 0; j < 5; ++j) {
      b1[j + 5] = ldb(1, j + 5);
      mma(a1, b1[j], j);
    }
    {
      const int dlt = ph == 2 ? -2 * SLOT : SLOT;
      ph = ph == 2 ? 0 : ph + 1;
      px0 += dlt; px1 += dlt; pw0 += dlt; pw1 += dlt;
      asm volatile("" : "+v"(px0), "+v"(px1), "+v"(pw0), "+v"(pw1));
#pragma unroll
      for (int i = 0; i < 4; ++i) acc[i][5] = mfma16(a1[i], b1[5], acc[i][5]);
      SG_MFMA(1); SG_VALU(2); SG_MFMA(1); SG_VALU(2); SG_MFMA(2);
      SCHED_FENCE();
    }
    stage_barrier(st);
    FSTD(2 + st);
    SCHED_FENCE();
    a0[0] = lda(0, 0); b0[0] = ldb(0, 0); a0[1] = lda(0, 1); mma(a1, b1[6], 6);
    a0[2] = lda(0, 2); a0[3] = lda(0, 3); b0[1] = ldb(0, 1); mma(a1, b1[7], 7);
    b0[2] = ldb(0, 2); b0[3] = ldb(0, 3); mma(a1, b1[8], 8);
    b0[4] = ldb(0, 4); mma(a1, b1[9], 9);
  }
  if (PEEL) {
#pragma unroll
    for (int j = 0; j < 10; ++j) {
      if (j < 5) b0[j + 5] = ldb(0, j + 5); else b1[j - 5] = ldb(1, j - 5);
      if (j >= 2 && j < 6) a1[j - 2] = lda(1, j - 2);
      mma(a0, b0[j], j);
    }
#pragma unroll
    for (int j = 0; j < 10; ++j) {
      if (j < 5) b1[j + 5] = ldb(1, j + 5);
#pragma unroll
      for (int i = 0; i < 4; ++i) acc[i][j] = mfma16(a1[i], b1[j], acc[i][j]);
      if (j > 0) tail(j - 1);
      SG_MFMA(1); SG_DS(2); SG_VALU(10); SG_MFMA(1); SG_VALU(10); SG_MFMA(1); SG_VALU(10); SG_MFMA(1);
      SCHED_FENCE();
      if (j == 4) lds_arrive(lds + M_END);
    }
    tail(9);
  } else {
    lds_arrive(lds + M_END);
    WAIT_LGKM0();
  }
}

__device__ __forceinline__ void p2_main(const Params& p, ldsp lds, int w, int rt, int ct, u64 tag, const u32x4 (&held)[15]) {
  constexpr int LAG = 3, AW = P2_AW, NR = 17, RH = 12, GATE = P2_GATE;
  const int l = threadIdx.x & 63, l15 = l & 15, kg = l >> 4, q = l15 >> 2;
  const int sg = w & 1, kq = w >> 1, hf = w >> 2;
  const int rlo = rt * 96, rhi = min(rlo + 95, S - 1);
  const rsrc_t ra = mk_rsrc(p.ws, A2_BYTES);
  const unsigned av = (unsigned)(((rt * 6 + sg * 3) * KB_ALL + kq) * 1024 + l * 16);
  const int rh = 4 * kg + q, ro = 16 * (kg >> 1) + 4 * (kg & 1) + q;
  const int wh = rh * 128 + (((rh >> 1) & 3) << 5) + (l & 3) * 8;
  const int wo = ro * 128 + (((ro >> 1) & 3) << 5) + (l & 3) * 8;
  ldsp hdst = lds + ((w & 3) * 1024 + hf * 4096 + l * 16);
  f32x4 acc[4][3];
#pragma unroll
  for (int n = 0; n < 4; ++n)
#pragma unroll
    for (int s = 0; s < 3; ++s) acc[n][s] = f32x4{};
#define PIN_ACC(text) \
  asm volatile(text : "+v"(acc[0][0]), "+v"(acc[1][0]), "+v"(acc[2][0]), "+v"(acc[3][0]), "+v"(acc[0][1]), "+v"(acc[1][1]), "+v"(acc[2][1]), \
               "+v"(acc[3][1]), "+v"(acc[0][2]), "+v"(acc[1][2]), "+v"(acc[2][2]), "+v"(acc[3][2]))
  PIN_ACC("");
  u32x4 a[NR][3];
  bf16x8 wf[2][4];
  float gi[3];
  auto lda = [&](int t) {
#pragma unroll
    for (int s = 0; s < 3; ++s) {
      a[t][s] = __builtin_amdgcn_raw_buffer_load_b128(ra, av + s * KB_ALL * 1024, t * 4096, A2AUX);
    }
  };
  ldsp wb[4];
  auto ldw = [&](int t) {
    const int o0 = w2_off(4 * t), r0 = o0 & ~0x7FFF;
    auto straight = [](int u) { return w2_off(4 * u + 3) == w2_off(4 * u) + 3 * 4096; };
    if (t == 0 || t == RH || r0 != (w2_off(4 * t - 4) & ~0x7FFF) || !straight(t) || !straight(t - 1)) {
      int rel = kq * 4096;
      if (!straight(t)) rel = kq == 0 ? 0 : kq == 1 ? w2_off(4 * t + 1) - o0 : kq == 2 ? w2_off(4 * t + 2) - o0 : w2_off(4 * t + 3) - o0;
#pragma unroll
      for (int n = 0; n < 4; ++n) {
        wb[n] = lds + (((t < RH ? wh : wo) ^ ((n ^ kq) << 5)) + r0 + rel);
        asm volatile("" : "+v"(wb[n]));
      }
    }
#pragma unroll
    for (int n = 0; n < 4; ++n) wf[t & 1][n] = lds_tr2(wb[n] + (o0 - r0), wb[n] + (o0 - r0) + (t < RH ? 2048 : 1024));
  };
  if (w == 4)
    poll(flag_ptr(p, l < 32 ? FL_H + ((l & 16) ? rhi : rlo) / 160 * 12 + min(l15, 11)
                     : (l < 48 ? FL_G : FL_O) + (((l & 4) ? rhi : rlo) >> 8) * 4 + (l & 3)), tag);
  const unsigned cnt = (unsigned)(size_t)(lds + M_RND);
  auto arrive = [&](int r) {
    asm volatile("s_mov_b64 exec, 1\n\tds_add_u32 %0, %1 offset:%2\n\ts_mov_b64 exec, -1" ::"v"(cnt), "v"(1u), "i"(4 * r) : "memory");
  };
  auto counted = [](int r) { return (r >= 0 && r < GATE - LAG) || (r >= GATE && r < RH); };
  unsigned peek = 0;
  auto check = [&](int r) {
    unsigned sv, n;
    asm volatile(
        "s_nop 0\n\tv_readfirstlane_b32 %0, %2\n\ts_cmp_ge_u32 %0, 8\n\ts_cbranch_scc1 .Lok%=\n\ts_mov_b32 %1, 0x100000\n"
        ".Lagain%=:\n\tds_read_b32 %2, %3 offset:%4\n\ts_waitcnt lgkmcnt(0)\n\tv_readfirstlane_b32 %0, %2\n\ts_sub_u32 %1, %1, 1\n\t"
        "s_cbranch_scc1 .Lok%=\n\ts_cmp_ge_u32 %0, 8\n\ts_cbranch_scc0 .Lagain%=\n.Lok%=:"
        : "=&s"(sv), "=&s"(n), "+v"(peek) : "v"(cnt), "i"(4 * r) : "memory", "scc");
  };
  auto prio = [](int v) {
    if (v >= 3) __builtin_amdgcn_s_setprio(3); else if (v == 2) __builtin_amdgcn_s_setprio(2);
    else if (v == 1) __builtin_amdgcn_s_setprio(1); else __builtin_amdgcn_s_setprio(0);
  };
  auto upto = [](int t) {
    if (t < GATE) return min(min(t + 1 + AW, P2_A0 + P2_RAMP * (t + 1)), RH);
    return min(RH + P2_G0 + P2_G1 * (t - GATE), NR);
  };
  BARRIER();
  STAMP(4);
  {
    const rsrc_t rg = mk_rsrc(p.ws + G_OFF, G_BYTES);
#pragma unroll
    for (int t = 0; t < upto(-1); ++t) {
      prio(3 - t); lda(t);
      if (t == 0) {
        ldw(0);
#pragma unroll
        for (int s = 0; s < 3; ++s) gi[s] = __builtin_bit_cast(float, __builtin_amdgcn_raw_buffer_load_b32(rg, (rt * 96 + sg * 48 + s * 16 + l15) * 4, 0, SC1));
      }
      SCHED_FENCE();
    }
  }
#pragma unroll
  for (int t = 0; t < NR; ++t) {
    if (t == GATE) {
      if (w == 7) {
        const unsigned co = (unsigned)(((rt * 6 + min(l >> 3, 5)) * KB_ALL + 48) * 1024 + (l & GATE_HG) * 2048);
#pragma unroll 1
        for (int spin = 0; spin < SPIN_MAX; ++spin) {
          if (__all(__builtin_amdgcn_raw_buffer_load_b32(ra, co, 0, SC1) != UNSET)) break;
          asm volatile("s_sleep 1" ::: "memory");
        }
      }
      WAIT_LGKM0();
      FST(16);
      BARRIER();
      FST(17);
      SCHED_FENCE();
    }
    if (counted(t - LAG)) check(t - LAG);
    if (counted(t + 1 - LAG)) peek = __hip_atomic_load((LAS unsigned*)(lds + M_RND + 4 * (t + 1 - LAG)), __ATOMIC_RELAXED, __HIP_MEMORY_SCOPE_WORKGROUP);
    SCHED_FENCE();
    prio(3 - (t & 3));
#pragma unroll
    for (int u = upto(t - 1); u < upto(t); ++u) lda(u);
    if (t + 1 < NR) ldw(t + 1);
    if (t >= LAG) {
#pragma unroll
      for (int i = 2 * (t - LAG); i < min(2 * (t - LAG) + 2, 15); ++i) {
        *(LAS u32x4*)(hdst + w2_off(39 + 2 * i)) = held[i];
      }
    }
    if (t >= RH) {
      auto unset = [&]() {
        unsigned m = 0u;
#pragma unroll
        for (int s = 0; s < 3; ++s) m = max(max(m, max(a[t][s][0], a[t][s][1])), max(a[t][s][2], a[t][s][3]));
        return __any(m == UNSET);
      };
      if (__builtin_expect(unset(), 0)) {
        int spin = 0;
#pragma unroll 1
        do { lda(t); __builtin_amdgcn_s_waitcnt(0x0F70); } while (unset() && ++spin < SPIN_MAX);
      }
      SCHED_FENCE();
    }
    if (t == RH) {
      PIN_ACC("s_nop 7");
#pragma unroll
      for (int n = 0; n < 4; ++n)
#pragma unroll
        for (int s = 0; s < 3; ++s) acc[n][s] *= 1.f - gi[s];
    }
    if (t >= RH) PIN_ACC("s_nop 1");
#pragma unroll
    for (int s = 0; s < 3; ++s)
#pragma unroll
      for (int n = 0; n < 4; ++n) {
        asm volatile("v_mfma_f32_16x16x32_bf16 %0, %1, %2, %0" : "+v"(acc[n][s]) : "v"(wf[t & 1][n]), "v"(a[t][s]));
      }
    if (counted(t)) arrive(t);
    SCHED_FENCE();
  }
  PIN_ACC("s_nop 7");
  STAMP(5);
  constexpr int XB = 16384;
  static_assert(w2_off(30) == XB && w2_off(39) == XB + 9 * 4096, "ring layout");
  {
    ldsp wp = lds + (XB + w * 9 * 1024 + l * 16);
#pragma unroll
    for (int d = 1; d < 4; ++d)
#pragma unroll
      for (int s = 0; s < 3; ++s) {
        *(LAS f32x4*)(wp + ((d - 1) * 3 + s) * 1024) = acc[d][s];
      }
  }
  const rsrc_t rout = mk_rsrc(p.out, S * D * 2);
  unsigned vo = (unsigned)((rt * 96 + sg * 48 + l15) * 1280 + (ct * 64 + kq * 16 + 4 * kg) * 2);
  asm volatile("" : "+v"(vo));
  WAIT_LGKM0();
  BARRIER();
  f32x4 part[3][4];
#pragma unroll
  for (int s = 0; s < 3; ++s)
#pragma unroll
    for (int d = 1; d < 4; ++d) part[s][d] = *(LAS f32x4*)(lds + XB + ((w ^ (2 * d)) * 9 + (d - 1) * 3 + s) * 1024 + l * 16);
  SCHED_FENCE();
#pragma unroll
  for (int s = 0; s < 3; ++s) {
    const f32x4 v = (acc[0][s] + part[s][1]) + (part[s][2] + part[s][3]);
    __builtin_amdgcn_raw_buffer_store_b64(u32x2{pk2(v[0], v[1]), pk2(v[2], v[3])}, rout, vo + s * 16 * 1280, 0, OUT_AUX);
  }
}

__global__ __launch_bounds__(512, 2) void fused(Params p) {
  extern __shared__ __attribute__((aligned(16))) char smem[];
  ldsp lds = (ldsp)smem;
  u64 pc;
  asm volatile("s_getpc_b64 %0" : "=s"(pc));
  constexpr unsigned rest = CODE_BYTES;
  const int b = blockIdx.x, xcd = b & 7, idx = b >> 3, cg = xcd & 3, rg = xcd >> 2;
  const int w = __builtin_amdgcn_readfirstlane(threadIdx.x >> 6), lw = w - 4;
  const int l = threadIdx.x & 63;
  FINIT();
  STAMP(0);
  const int tm = (11 - idx) >> 31;
  const int jj = idx + ((20 * rg - 12) & tm), j3 = (jj * 43) >> 7, jr = jj - 3 * j3;
  const bool spare = jj == 39;
  const int ty = tm ? T_GLU : (jr == 0 ? T_Q : T_KV);
  const int rt = j3 + (4 * rg & ~tm);
  const int ctn = 2 * cg + jr - 1 + ((cg + 1) & tm);
  const int t2 = 28 * xcd - ((xcd - 4) & -rg) + ((idx - 4) & tm) + ((2 * j3 + jr - 1) & ~tm);
  const int rot0 = ROT;
  auto get_tag = [&]() {
    u64 e;
    asm volatile("s_load_dwordx2 %0, %1, 0x0\n\ts_waitcnt lgkmcnt(0)" : "=s"(e) : "s"(p.ws + EPOCH_OFF) : "memory");
    return make_tag(e);
  };

  constexpr int PQX = 2 * SLOT;
  const bool loader = w >= 4 && !spare;
  Warm wm(lds);
  u64 tag;
  auto mfma_start = [&]() {
    if (idx < 2) {
      const unsigned o = (idx * 256 + w * 64 + l) * 128;
      wm.touch((const GAS char*)pc + (o < rest ? o : 0u));
    }
    if (w == 0) {
      tag = get_tag();
      if (l < 32) *(LAS unsigned*)(lds + M_CNT + l * 4) = 0u;
      if (l == 0) *(LAS u64*)(lds + M_TAG) = tag;
      WAIT_LGKM0();
    }
  };
  auto lds_tag = [&]() {
    u32x2 v;
    asm volatile("ds_read_b64 %0, %1\n\ts_waitcnt lgkmcnt(0)" : "=v"(v) : "v"((unsigned)(size_t)(lds + M_TAG)) : "memory");
    const unsigned lo = v[0], hi = v[1];
    tag = (u64)(unsigned)__builtin_amdgcn_readfirstlane(hi) << 32 | (unsigned)__builtin_amdgcn_readfirstlane(lo);
  };
  if (__builtin_expect(ty == T_Q, 0)) {
    const int ct = cg;
    bf16x8 pq[10];
    if (loader) {
      u32x4 none[30];
      float gs = p1_loader<true>(p, lds, lw, 0, rt, ct, rot0, 0, none, [&](int kb) {
        const rsrc_t ra = mk_rsrc(p.ws, A2_BYTES);
        const unsigned vo = (unsigned)(((rt * 16 + lw * 4) * KB_ALL + 48 + (kb < 4 ? 4 * ct + kb : 16 + ct)) * 1024 + l * 16);
#pragma unroll
        for (int i = 0; i < 4; ++i) st128_sc1(ra, vo + i * KB_ALL * 1024, u32x4{UNSET, UNSET, UNSET, UNSET});
      }, [&]() {
        lds_tag();
        if (lw == 0 && l == 0) __hip_atomic_store(flag_ptr(p, FL_O + rt * 4 + ct), tag, __ATOMIC_RELAXED, __HIP_MEMORY_SCOPE_AGENT);
      }, [&]() {
        {
          const unsigned o = ((j3 * 4 + lw) * 26 + min(l, 25)) * 128;
          wm.touch((const GAS char*)pc + (o < rest ? o : 0u));
        }
        for (int i = lw * 64 + l; i < 2 * KVT_SZ / 16; i += 256)
          asm volatile("ds_write_b128 %0, %1" ::"v"((unsigned)(size_t)(lds + KVT) + i * 16), "v"(u32x4{0u, 0u, 0u, 0u}) : "memory");
        if (lw == 0) poll(flag_ptr(p, FL_KV + (l & 7) * 8 + 2 * ct + ((l >> 3) & 1)), tag);
        WAIT_LGKM0();
      });
      gs += __shfl_xor(gs, 1); gs += __shfl_xor(gs, 2);
      if ((l & 3) == 0)
        __builtin_amdgcn_raw_buffer_store_b32(__builtin_bit_cast(unsigned, sigmoidf(gs)), mk_rsrc(p.ws + G_OFF, G_BYTES), (rt * 256 + 64 * ct + 16 * lw + (l >> 2)) * 4, 0, SC1);
      publish(p, lds, FL_G + rt * 4 + ct, tag, 4);
      poll(flag_ptr(p, FL_G + rt * 4 + (l & 3)), tag);
      const int tl = lw * 64 + l;
      *(LAS unsigned*)(lds + M_G + tl * 4) = __builtin_amdgcn_raw_buffer_load_b32(mk_rsrc(p.ws + G_OFF, G_BYTES), (rt * 256 + tl) * 4, 0, SC1);
      lds_wait(lds + M_END, 4);
#pragma unroll
      for (int t = 0; t < 10; ++t) { pq[t] = lds_b128(lds + PQX + (lw * 10 + t) * 1024 + l * 16); asm volatile("" : "+v"(pq[t])); }
    } else {
    mfma_start();
    f32x16 acc[5][2];
#pragma unroll
    for (int n = 0; n < 5; ++n) { acc[n][0] = f32x16{}; acc[n][1] = f32x16{}; }
    p1_main_q(lds, w, acc);
    STAMP(1);
    const Consts cn(1.f);
#pragma unroll
    for (int k = 9; k >= 0; --k)
#pragma unroll
      for (int s = 0; s < 2; ++s) {
        const f32x16& c = acc[k >> 1][s];
        const int o = 8 * (k & 1);
        const bf16x8 f = pack44(phi4(f32x4{c[o], c[o + 1], c[o + 2], c[o + 3]}, cn), phi4(f32x4{c[o + 4], c[o + 5], c[o + 6], c[o + 7]}, cn));
        if (k >= 5) *(LAS bf16x8*)(lds + PQX + (w * 10 + 5 * s + k - 5) * 1024 + l * 16) = f;
        else { pq[5 * s + k] = f; asm volatile("" : "+v"(pq[5 * s + k])); }
        if (k == 5 && s == 1) { WAIT_LGKM0(); lds_arrive(lds + M_END); }
      }
    STAMP(2);
    }
    {
      const int ch = min((int)threadIdx.x, 419), hg = ch >= 210;
      ldsp dp[2];
      bool dup[2];
#pragma unroll
      for (int u = 0; u < 2; ++u) {
        const int un = 2 * (ch - 210 * hg) + u, row = un / 5, dq = un % 5, hd = row < 80 ? row / 20 : row - 80, run = 5 * hd + dq;
        dp[u] = lds + KVT + hg * KVT_SZ + (32 * hd + (row < 80 ? row - 20 * hd : 20)) * KS + ((run >> 2) - hd) * 32 + (run & 1) * 16 + (run & 2) * 4;
        dup[u] = row >= 80;
      }
      const rsrc_t rk = mk_rsrc(p.ws + KVP_OFF, KVP_BYTES);
      u32x4 pv[8];
      unsigned todo = 0xFFu;
#pragma unroll 1
      for (int spin = 0; spin < SPIN_MAX && todo; ++spin) {
#pragma unroll
        for (int r = 0; r < 8; ++r)
          if (todo >> r & 1) pv[r] = __builtin_amdgcn_raw_buffer_load_b128(rk, 2 * ct * 3360 + ch * 16, r * 8 * 3360, SC1);
#pragma unroll
        for (int r = 0; r < 8; ++r)
          if (__all(max(max(pv[r][0], pv[r][1]), max(pv[r][2], pv[r][3])) != UNSET)) todo &= ~(1u << r);
        asm volatile("s_sleep 1" ::: "memory");
      }
      STAMP(3);
      f32x2 sum[4];
#pragma unroll
      for (int e = 0; e < 4; ++e) sum[e] = f32x2{0.f, 0.f};
#pragma unroll
      for (int r = 0; r < 8; ++r)
#pragma unroll
        for (int e = 0; e < 4; ++e) {
          const unsigned x = pv[r][e];
          sum[e] += f32x2{__uint_as_float(x << 16), __uint_as_float(x & 0xFFFF0000u)};
        }
#pragma unroll
      for (int u = 0; u < 2; ++u) {
        const u32x2 v = {pk2(sum[2 * u][0], sum[2 * u][1]), pk2(sum[2 * u + 1][0], sum[2 * u + 1][1])};
        *(LAS u32x2*)dp[u] = v;
        if (dup[u]) *(LAS u32x2*)(dp[u] + 4 * KS) = v;
      }
      WAIT_LGKM0();
      BARRIER();
      STAMP(4);
    }
  {
    const int l31 = l & 31, h = l >> 5, hg = w >> 2, wr = w & 3;
    const rsrc_t ra = mk_rsrc(p.ws, A2_BYTES);
    const unsigned vo = (unsigned)(((rt * 16 + wr * 4 + ((l >> 4) & 1)) * KB_ALL + 48) * 1024 + (2 * ct + hg) * 2048 + h * 256 + (l & 15) * 16);
    const unsigned vx = vo + 16384 - (2 * ct + hg) * 1536;
    bf16x8 af[4][2];
#pragma unroll
    for (int hd = 0; hd < 4; ++hd)
#pragma unroll
      for (int t = 0; t < 2; ++t) af[hd][t] = lds_b128(lds + (KVT + hg * KVT_SZ + l31 * KS + h * 16) + 32 * hd * KS + t * 32);
    float gv[2];
#pragma unroll
    for (int s = 0; s < 2; ++s) gv[s] = *(LAS float*)(lds + M_G + (64 * wr + 32 * s + l31) * 4);
    FST(18);
    SCHED_FENCE();
    f32x16 acc[2][4];
#pragma unroll
    for (int s = 0; s < 2; ++s)
#pragma unroll
      for (int hd = 3; hd >= 0; --hd) acc[s][hd] = mfma32(af[hd][1], pq[5 * s + hd + 1], mfma32(af[hd][0], pq[5 * s + hd], f32x16{}));
    u32x4 xs[2];
#pragma unroll
    for (int s = 0; s < 2; ++s) {
      u32x4 pc[4];
      unsigned ex[4][2];
#pragma unroll
      for (int hd = 3; hd >= 0; --hd) {
        const f32x16& c = acc[s][hd];
        float d0 = c[12], d1 = c[8];
        asm volatile("" : "+v"(d0), "+v"(d1));
        const float f = gv[s] * __builtin_amdgcn_rcpf((h ? d1 : d0) + 1e-6f);
        const f32x2 ff = {f, f};
        unsigned r[6];
#pragma unroll
        for (int i = 0; i < 6; ++i) { const f32x2 y = f32x2{c[2 * i], c[2 * i + 1]} * ff; r[i] = pk2(y[0], y[1]); }
        pc[hd] = u32x4{r[0], r[1], r[2], r[3]};
        ex[hd][0] = r[4]; ex[hd][1] = r[5];
        if (hd > 0) st128_sc1(ra, vo + s * 2 * KB_ALL * 1024 + hd * 512, pc[hd]);
      }
      u32x4 x;
#pragma unroll
      for (int i = 0; i < 4; ++i) x[i] = __builtin_amdgcn_permlane32_swap(ex[i >> 1][i & 1], ex[2 + (i >> 1)][i & 1], false, false)[0];
      st128_sc1(ra, vo + s * 2 * KB_ALL * 1024, pc[0]);
      xs[s] = x;
    }
    st128_sc1(ra, vx, xs[0]);
    st128_sc1(ra, vx + 2 * KB_ALL * 1024, xs[1]);
    STAMP(5);
    STAMP(6);
    FDUMP();
    return;
  }
  }
  const int ct = ctn;
  u32x4 held[15];
  if (__builtin_expect(loader, 1)) {
    u32x4 pre[30];
    p1_loader<false>(p, lds, lw, tm, rt, ct, rot0, t2, pre, [&](int) {
      st128_sc1(mk_rsrc(p.ws + KVP_OFF + (rt * 8 + ct) * 3360, 3360), lw * 1024 + l * 16, u32x4{UNSET, UNSET, UNSET, UNSET});
    }, [&]() {
      lds_tag();
      if (lw == 0 && l == 0) __hip_atomic_store(flag_ptr(p, FL_KV + rt * 8 + ct), tag, __ATOMIC_RELAXED, __HIP_MEMORY_SCOPE_AGENT);
    }, [&]() { lds_tag(); });
    const W2Src src(p, lw, t2);
    src.fill<0, 24>(lds + lw * 1024);
    if (ty == T_KV) {
#pragma unroll
      for (int i = 0; i < 30; ++i) pre[i] = pre_load(src, i);
      SCHED_FENCE();
      BARRIER(); BARRIER();
    } else {
      lds_wait(lds + M_END, 4);
    }
    WAIT_VM(0);
#pragma unroll
    for (int i = 0; i < 15; ++i) *(LAS u32x4*)(lds + (lw * 1024 + l * 16) + w2_off(24 + i)) = pre[i];
#pragma unroll
    for (int i = 0; i < 15; ++i) held[i] = pre[15 + i];
    WAIT_LGKM0();
  } else {
    mfma_start();
    if (spare) {
#pragma unroll 1
      for (int n = cg * 8 + w; n < 340; n += 32) {
        const int k = n / 5, line = (n - 5 * k) * 64 + l, r = line / 10;
        const int st = k < 15 ? 24 + k : k < 29 ? 10 + 2 * k : k < 53 ? k - 29 : 2 * k - 67;
        const int row = st < 48 ? 32 * st + r : st < 64 ? 40 * (st - 48) + 20 * (r >> 4) + (r & 15)
                                             : 80 * (2 * (st - 64) + (r >> 4)) + 16 + 20 * ((r >> 3) & 1) + 40 * ((r >> 2) & 1) + (r & 3);
        wm.touch((const GAS char*)(st < 48 ? p.wd : p.wo) + row * 1280 + (line - 10 * r) * 128);
        WAIT_VM(1);
      }
      STAMP(6);
      FDUMP();
      if (cg == 0 && w == 0) {
#pragma unroll 1
        for (int i = 0; i < 5; ++i) poll(flag_ptr(p, min(i * 64 + l, NFLAG - 1)), tag);
        if (l == 0) __hip_atomic_store((GAS u64*)(p.ws + EPOCH_OFF), tag, __ATOMIC_RELAXED, __HIP_MEMORY_SCOPE_AGENT);
      }
      return;
    }
      f32x4 acc[4][10];
#pragma unroll
      for (int i = 0; i < 4; ++i)
#pragma unroll
        for (int j = 0; j < 10; ++j) acc[i][j] = f32x4{};
      if (ty == T_GLU) {
        const Consts cn(-1.f);
        const rsrc_t ra = mk_rsrc(p.ws, A2_BYTES);
        const unsigned vo = (unsigned)((rt * 10 * KB_ALL + ct * 4 + w) * 1024 + l * 16);
        auto fin = [&](int j) {
          st128_sc1(ra, vo + j * KB_ALL * 1024, __builtin_bit_cast(u32x4, pack44(glu4(acc[0][j], acc[2][j], cn), glu4(acc[1][j], acc[3][j], cn))));
        };
        p1_main16<T_GLU, true>(lds, w, acc, fin);
        STAMP(2);
        lds_tag();
        publish(p, lds, FL_H + rt * 12 + ct, tag, 4);
      } else {
        int goff[2];
#pragma unroll
        for (int i = 0; i < 2; ++i) {
          const int en = min(2 * (int)threadIdx.x + i, 419), row = en / 5, dq = en % 5;
          const int hd = row < 80 ? row / 20 : row - 80, d0 = 20 * hd + 4 * dq, ti = d0 >> 4, tj = row >> 4;
          const int tile = row >= 80 ? 9 : ti == tj ? ti : (ti == 0 || ti == 4) ? 8 : 4 + ti;
          goff[i] = tile * 1024 + (((d0 & 15) >> 2) * 16 + (row < 80 ? row & 15 : ti)) * 16;
          asm volatile("" : "+v"(goff[i]));
        }
        p1_main16<T_KV, false>(lds, w, acc, [](int) {});
        STAMP(1);
        const Consts cn(1.f);
        const int l15 = l & 15, g = l >> 4;
        ldsp tp = lds + (w * 10240 + l * 16);
        bf16x8 vf[2][5];
#pragma unroll
        for (int u = 0; u < 2; ++u)
#pragma unroll
          for (int j = 0; j < 5; ++j) vf[u][j] = pack44(acc[2 * u][5 + j], acc[2 * u + 1][5 + j]);
        f32x4 z = {}, dg[5], lo[5], hi[5];
        SCHED_FENCE();
#pragma unroll
        for (int i = 0; i < 5; ++i) {
          const unsigned o1 = l15 == i ? 0x3F803F80u : 0u;
          const bf16x8 one = __builtin_bit_cast(bf16x8, u32x4{o1, o1, o1, o1});
          dg[i] = f32x4{}; lo[i] = f32x4{}; hi[i] = f32x4{};
#pragma unroll
          for (int u = 0; u < 2; ++u) {
            const bf16x8 kf = pack44(phi4(acc[2 * u][i], cn), phi4(acc[2 * u + 1][i], cn));
            if (i > 0) lo[i] = mfma16(kf, vf[u][i - 1], lo[i]);
            if (i < 4) hi[i] = mfma16(kf, vf[u][i + 1], hi[i]);
            dg[i] = mfma16(kf, vf[u][i], dg[i]);
            z = mfma16(kf, one, z);
            if (i > 0) {
              if (u == 0) *(LAS f32x4*)(tp + (i - 1) * 1024) = dg[i - 1];
              else if (i > 1) *(LAS f32x4*)(tp + (3 + i) * 1024) = g < i - 1 ? lo[i - 1] : hi[i - 1];
            }
            SCHED_FENCE();
          }
          if (i == 0) { FST(18); lds_wait(lds + M_END, 4); }
        }
        FST(19);
        *(LAS f32x4*)(tp + 8 * 1024) = l15 < 8 ? hi[0] : lo[4];
        *(LAS f32x4*)(tp + 4 * 1024) = dg[4];
        *(LAS f32x4*)(tp + 9 * 1024) = z;
        WAIT_LGKM0();
        FST(20);
        BARRIER();
        FST(21);
        const rsrc_t rk = mk_rsrc(p.ws + KVP_OFF + (rt * 8 + ct) * 3360, 3360);
        f32x4 part[2][4];
#pragma unroll
        for (int i = 0; i < 2; ++i)
#pragma unroll
          for (int ww = 0; ww < 4; ++ww) part[i][ww] = *(LAS f32x4*)(lds + goff[i] + ww * 10240);
        SCHED_FENCE();
        u32x4 res = __builtin_bit_cast(u32x4, pack44((part[0][0] + part[0][1]) + (part[0][2] + part[0][3]), (part[1][0] + part[1][1]) + (part[1][2] + part[1][3])));
#pragma unroll
        for (int e = 0; e < 4; ++e) res[e] = min(res[e], UNSET - 1);
        st128_sc1(rk, threadIdx.x * 16, res);
        WAIT_LGKM0();
        FST(22);
        BARRIER();
        STAMP(2);
      }
      STAMP(3);
      const W2Src src(p, w, t2);
#pragma unroll
      for (int i = 0; i < 15; ++i) held[i] = src.load(39 + 2 * i);
      SCHED_FENCE();
  }
  int t2l = __builtin_amdgcn_readfirstlane(t2);
  asm volatile("" : "+s"(t2l));
  const int rt2 = (t2l * 205) >> 11;
  p2_main(p, lds, w, rt2, t2l - 10 * rt2, tag, held);
  STAMP(6);
  FDUMP();
}

#define STAMP_PTR nullptr
extern "C" void kernel_launch(void* const* d_in, const int* in_sizes, int n_in, void* d_out, int out_size, void* d_ws, size_t ws_size,
                              hipStream_t stream) {
  Params p;
  p.x = (const u16*)d_in[0]; p.wqkv = (const u16*)d_in[1]; p.wo = (const u16*)d_in[2];
  p.wg = (const u16*)d_in[4]; p.wu = (const u16*)d_in[5]; p.wd = (const u16*)d_in[6]; p.gw = (const u16*)d_in[7];
  p.out = (u16*)d_out; p.ws = (char*)d_ws; p.stamps = STAMP_PTR;
  (void)hipFuncSetAttribute((const void*)fused, hipFuncAttributeMaxDynamicSharedMemorySize, LDS_BYTES);
  hipLaunchKernelGGL(fused, dim3(256), dim3(512), LDS_BYTES, stream, p);
}
```

```cpp
#include <hip/hip_runtime.h>
#include <hip/hip_bf16.h>

typedef unsigned short u16;
typedef unsigned long long u64;
typedef __bf16 bf16x8 __attribute__((ext_vector_type(8)));
typedef __bf16 bf16x4 __attribute__((ext_vector_type(4)));
typedef __bf16 bf16x2 __attribute__((ext_vector_type(2)));
typedef float f32x16 __attribute__((ext_vector_type(16)));
typedef float f32x4 __attribute__((ext_vector_type(4)));
typedef unsigned u32x4 __attribute__((ext_vector_type(4)));
typedef unsigned u32x2 __attribute__((ext_vector_type(2)));
#define LAS __attribute__((address_space(3)))
#define GAS __attribute__((address_space(1)))
typedef LAS char* ldsp;
typedef __amdgpu_buffer_rsrc_t rsrc_t;

constexpr int S = 2048, D = 640, F = 1536;
constexpr float LOG2E = 1.4426950408889634f;
constexpr int KB_ALL = 68;
constexpr unsigned A2_BYTES = 128u * KB_ALL * 1024;
constexpr unsigned KVP_OFF = A2_BYTES, KVP_BYTES = 8 * 8 * 3360;
constexpr unsigned G_OFF = KVP_OFF + KVP_BYTES, G_BYTES = S * 4;
constexpr unsigned FLAG_OFF = G_OFF + G_BYTES;
constexpr int NFLAG = 284;
constexpr unsigned EPOCH_OFF = FLAG_OFF + NFLAG * 128;
constexpr unsigned WS_USED = EPOCH_OFF + 128;
constexpr int FL_H = 0, FL_KV = 156, FL_O = 220, FL_G = 252;
constexpr int SC1 = 16;
constexpr unsigned UNSET = 0xFFFFFFFFu;
constexpr int SLOT = 53248, MISC = 3 * SLOT, LDS_BYTES = 163840;
constexpr int M_DUMMY = MISC, M_CNT = MISC + 256  , M_END = MISC + 260  , M_RND = MISC + 320  , M_TAG = MISC + 384, M_GW = MISC + 512  , M_G = MISC + 2048  ;
constexpr int KS = 80, KVT = SLOT, KVT_SZ = 4 * 32 * KS;
constexpr int NST1 = 10;
constexpr int SPIN_MAX = 1 << 16;
#define A2AUX SC1
#define GLU_PACE 0
#define OUT_AUX 0
#define P2_AW 3
#define P2_X3 99
#define P2_PRIO 2
#define P2_A0 1
#define P2_RAMP 2
#define P2_G0 2
#define P2_G1 1
#define GATE_HG 3
constexpr unsigned CODE_BYTES = 47000;
#define W0_AT 4
#define GLU_PEEL 1
#define P2_GATE 10
#define P2_POS 0
#define ROT (2 * (cg & 1) + 5 * (((cg >> 1) ^ rg) & 1) + 2 * (rt & 1))
#define W2_ST 4

struct Params {
  const u16 *x, *wqkv, *wo, *wg, *wu, *wd, *gw;
  u16* out;
  char* ws;
  u64* stamps;
};

#define STAMP(i) do {} while (0)
#define FST(i) do {} while (0)
#define FSTD(i) do {} while (0)
#define FINIT() do {} while (0)
#define FDUMP() do {} while (0)

#define WAIT_VM(n) asm volatile("s_waitcnt vmcnt(" #n ")" ::: "memory")
#define WAIT_LGKM0() asm volatile("s_waitcnt lgkmcnt(0)" ::: "memory")
#define BARRIER() do { asm volatile("" ::: "memory"); __builtin_amdgcn_s_barrier(); asm volatile("" ::: "memory"); } while (0)
#define SCHED_FENCE() __builtin_amdgcn_sched_barrier(0)
#define ORDER_FENCE() do { asm volatile("" ::: "memory"); __builtin_amdgcn_sched_barrier(0); } while (0)
#define SG_MFMA(n) __builtin_amdgcn_sched_group_barrier(0x8, n, 0)
#define SG_DS(n) __builtin_amdgcn_sched_group_barrier(0x100, n, 0)
#define SG_VALU(n) __builtin_amdgcn_sched_group_barrier(0x2, n, 0)
#define SG_DW(n) __builtin_amdgcn_sched_group_barrier(0x200, n, 0)
#define SG_VM(n) __builtin_amdgcn_sched_group_barrier(0x20, n, 0)

__device__ __forceinline__ rsrc_t mk_rsrc(const void* p, unsigned bytes) { return __builtin_amdgcn_make_buffer_rsrc((void*)p, 0, bytes, 0x00020000); }
#define DMA_AUX 0
__device__ __forceinline__ void dma16(rsrc_t r, ldsp dst, unsigned voff, unsigned soff) { __builtin_amdgcn_raw_ptr_buffer_load_lds(r, dst, 16, voff, soff, 0, DMA_AUX); }
__device__ __forceinline__ bf16x8 lds_b128(ldsp a) { return *(LAS bf16x8*)a; }
__device__ __forceinline__ bf16x8 lds_tr2(ldsp a0, ldsp a1) {
  bf16x4 lo = __builtin_amdgcn_ds_read_tr16_b64_v4bf16((LAS bf16x4*)a0);
  bf16x4 hi = __builtin_amdgcn_ds_read_tr16_b64_v4bf16((LAS bf16x4*)a1);
  return __builtin_shufflevector(lo, hi, 0, 1, 2, 3, 4, 5, 6, 7);
}
__device__ __forceinline__ f32x16 mfma32(bf16x8 a, bf16x8 b, f32x16 c) { return __builtin_amdgcn_mfma_f32_32x32x16_bf16(a, b, c, 0, 0, 0); }
__device__ __forceinline__ f32x4 mfma16(bf16x8 a, bf16x8 b, f32x4 c) { return __builtin_amdgcn_mfma_f32_16x16x32_bf16(a, b, c, 0, 0, 0); }
__device__ __forceinline__ unsigned pk2(float a, float b) {
  bf16x2 v = {(__bf16)a, (__bf16)b};
  return __builtin_bit_cast(unsigned, v);
}
__device__ __forceinline__ bf16x8 pack8(const f32x16& c, int o) {
  u32x4 r = {pk2(c[o], c[o + 1]), pk2(c[o + 2], c[o + 3]), pk2(c[o + 4], c[o + 5]), pk2(c[o + 6], c[o + 7])};
  return __builtin_bit_cast(bf16x8, r);
}
__device__ __forceinline__ bf16x8 pack44(const f32x4& a, const f32x4& b) {
  u32x4 r = {pk2(a[0], a[1]), pk2(a[2], a[3]), pk2(b[0], b[1]), pk2(b[2], b[3])};
  return __builtin_bit_cast(bf16x8, r);
}
__device__ __forceinline__ float phi(float t) {
  const float e = __builtin_amdgcn_exp2f(t * LOG2E);
  return __builtin_amdgcn_fmed3f(t, 1e-6f, __builtin_inff()) + __builtin_amdgcn_fmed3f(e, 0.f, 1.f);
}
__device__ __forceinline__ float sigmoidf(float t) { return __builtin_amdgcn_rcpf(1.f + __builtin_amdgcn_exp2f(-LOG2E * t)); }
typedef float f32x2 __attribute__((ext_vector_type(2)));
struct Consts {
  f32x2 l2e;
  float eps, big;
  __device__ __forceinline__ Consts(float sign) : l2e{sign * LOG2E, sign * LOG2E}, eps(1e-6f), big(3.4028234664e38f) {
    asm volatile("" : "+s"(l2e), "+s"(eps), "+v"(big));
  }
};
__device__ __forceinline__ f32x4 phi4(f32x4 t, const Consts& c) {
  const f32x2 a = f32x2{t[0], t[1]} * c.l2e, b = f32x2{t[2], t[3]} * c.l2e;
  f32x2 m0, m1, e0, e1;
#pragma unroll
  for (int i = 0; i < 2; ++i) {
    m0[i] = __builtin_amdgcn_fmed3f(t[i], c.eps, c.big);
    m1[i] = __builtin_amdgcn_fmed3f(t[2 + i], c.eps, c.big);
    e0[i] = __builtin_amdgcn_fmed3f(__builtin_amdgcn_exp2f(a[i]), 0.f, 1.f);
    e1[i] = __builtin_amdgcn_fmed3f(__builtin_amdgcn_exp2f(b[i]), 0.f, 1.f);
  }
  const f32x2 r0 = m0 + e0, r1 = m1 + e1;
  return f32x4{r0[0], r0[1], r1[0], r1[1]};
}
__device__ __forceinline__ f32x4 glu4(f32x4 g, f32x4 u, const Consts& c) {
  f32x2 a = f32x2{g[0], g[1]} * c.l2e, b = f32x2{g[2], g[3]} * c.l2e;
#pragma unroll
  for (int i = 0; i < 2; ++i) { a[i] = __builtin_amdgcn_exp2f(a[i]); b[i] = __builtin_amdgcn_exp2f(b[i]); }
  a += 1.f; b += 1.f;
#pragma unroll
  for (int i = 0; i < 2; ++i) { a[i] = __builtin_amdgcn_rcpf(a[i]); b[i] = __builtin_amdgcn_rcpf(b[i]); }
  a *= f32x2{u[0], u[1]}; b *= f32x2{u[2], u[3]};
  return f32x4{a[0], a[1], b[0], b[1]};
}
__device__ __forceinline__ void st128_sc1(rsrc_t r, unsigned off, u32x4 v) { __builtin_amdgcn_raw_buffer_store_b128(v, r, off, 0, SC1); }

__device__ __forceinline__ u64 make_tag(u64 e) { return ((e ^ 0x9E3779B97F4A7C15ull) * 0xBF58476D1CE4E5B9ull) | 1ull; }
__device__ __forceinline__ GAS u64* flag_ptr(const Params& p, int f) { return (GAS u64*)(p.ws + FLAG_OFF + (size_t)f * 128); }
__device__ __forceinline__ u64 ld_flag(GAS u64* f) { return __hip_atomic_load(f, __ATOMIC_RELAXED, __HIP_MEMORY_SCOPE_AGENT); }
__device__ __forceinline__ void poll(GAS u64* f, u64 tag) {
#pragma unroll 1
  for (int spin = 0; spin < SPIN_MAX; ++spin) {
    if (__all(ld_flag(f) == tag)) break;
    __builtin_amdgcn_s_sleep(2);
  }
  __builtin_amdgcn_fence(__ATOMIC_ACQUIRE, "wavefront");
}
__device__ __forceinline__ void lds_arrive(ldsp c) {
  asm volatile("s_mov_b64 exec, 1\n\tds_add_u32 %0, %1\n\ts_mov_b64 exec, -1" ::"v"((unsigned)(size_t)c), "v"(1u) : "memory");
}
__device__ __forceinline__ void lds_wait(ldsp c, unsigned n) {
#pragma unroll 1
  for (int spin = 0; spin < SPIN_MAX; ++spin) {
    unsigned v;
    asm volatile("ds_read_b32 %0, %1\n\ts_waitcnt lgkmcnt(0)" : "=v"(v) : "v"((unsigned)(size_t)c) : "memory");
    if (__builtin_amdgcn_readfirstlane(v) >= n) break;
    __builtin_amdgcn_s_sleep(1);
  }
}
__device__ __forceinline__ void publish(const Params& p, ldsp lds, int f, u64 tag, unsigned n) {
  WAIT_VM(0);
  if ((threadIdx.x & 63) == 0) {
    const unsigned old = __hip_atomic_fetch_add((LAS unsigned*)(lds + M_CNT), 1u, __ATOMIC_RELAXED, __HIP_MEMORY_SCOPE_WORKGROUP);
    if (old == n - 1) __hip_atomic_store(flag_ptr(p, f), tag, __ATOMIC_RELAXED, __HIP_MEMORY_SCOPE_AGENT);
  }
}

enum { T_Q = 0, T_KV = 1, T_GLU = 2 };

__device__ __forceinline__ constexpr int w2_off(int s) { return (SLOT + (s % 39) * 4096) % (3 * SLOT); }

struct W2Src {
  rsrc_t rd, ro;
  unsigned wv, wv4, ws0, wo0, wo1;
  __device__ __forceinline__ W2Src(const Params& p, int piece, int t2) {
    int l = threadIdx.x & 63;
    t2 = __builtin_amdgcn_readfirstlane(t2);
    asm volatile("" : "+s"(piece), "+s"(t2), "+v"(l));
    const int ct = t2 - 10 * ((t2 * 205) >> 11);
    rd = mk_rsrc(p.wd, F * D * 2);
    ro = mk_rsrc(p.wo, D * D * 2);
    wv = (l >> 3) * 1280 + (((l & 7) ^ ((l >> 4) << 1)) << 4);
    wv4 = wv + (l >> 5) * 36 * 1280;
    ws0 = (unsigned)(8 * piece) * 1280 + ct * 128;
    wo0 = (unsigned)(20 * (piece >> 1) + 8 * (piece & 1)) * 1280 + ct * 128;
    wo1 = (unsigned)(80 * (piece >> 1) + 16 + 20 * (piece & 1)) * 1280 + ct * 128;
  }
  __device__ __forceinline__ u32x4 load(int s) const {
    if (s < 48) return __builtin_amdgcn_raw_buffer_load_b128(rd, wv, ws0 + (unsigned)s * 32 * 1280, 0);
    if (s < 64) return __builtin_amdgcn_raw_buffer_load_b128(ro, wv, wo0 + (unsigned)(s - 48) * 40 * 1280, 0);
    return __builtin_amdgcn_raw_buffer_load_b128(ro, wv4, wo1 + (unsigned)(s - 64) * 160 * 1280, 0);
  }
  template <int S0, int S1> __device__ __forceinline__ void fill(ldsp dst) const {
#pragma unroll
    for (int s = S0; s < S1; ++s) dma16(rd, dst + w2_off(s), wv, ws0 + (unsigned)s * 32 * 1280);
  }
};

struct Warm {
  unsigned sink;
  __device__ __forceinline__ Warm(ldsp lds) : sink((unsigned)(size_t)(lds + M_DUMMY)) {}
  __device__ __forceinline__ void touch(const GAS char* t) {
    unsigned keep;
    asm volatile("s_mov_b32 %0, m0\n\ts_mov_b32 m0, %2\n\ts_nop 0\n\tglobal_load_lds_dword %1, off\n\ts_mov_b32 m0, %0" : "=&s"(keep) : "v"(t), "s"(sink) : "memory");
  }
};

__device__ __forceinline__ u32x4 pre_load(const W2Src& src, int i) { return i < 15 ? src.load(24 + i) : src.load(40 + 2 * (i - 15)); }

template <bool ISQ, class Reset, class Raise, class Gap>
__device__ __forceinline__ float p1_loader(const Params& p, ldsp lds, int lw, int tm, int rt, int ct, int k0, int t2, u32x4 (&pre)[30],
                                          Reset reset, Raise raise, Gap gap) {
  const bool tb = !ISQ && tm != 0;
  auto sel = [&](int a, int b) { return ISQ ? b : b ^ ((a ^ b) & tm); };
  auto selp = [&](const u16* a, const u16* b) { return ISQ ? b : (const u16*)((u64)b ^ (((u64)a ^ (u64)b) & (u64)(long long)tm)); };
  const int l = threadIdx.x & 63;
  const int i8 = sel(5 * lw, 8 * lw + 3);
  const unsigned xe = (l >> 3) * 1280 + (((l & 7) ^ (l >> 4)) << 4);
  const unsigned xa = (xe ^ ((i8 & 1) << 6)) + (unsigned)(sel(160, 256) * rt + 8 * i8) * 1280, xb = (xa ^ 64) + 8 * 1280;
  const int d8 = i8 * 1024;
  const rsrc_t rx = mk_rsrc(p.x, S * D * 2);
  FST(0);
#pragma unroll
  for (int j = 0; j < 5; ++j) dma16(rx, lds + d8 + j * 1024, (j & 1) ? xb : xa, k0 * 128 + (j >> 1) * 16 * 1280);
  SCHED_FENCE();
  const rsrc_t r0 = mk_rsrc(selp(p.wg, p.wqkv), sel(D * F * 2, D * 3 * D * 2));
  const rsrc_t r1 = mk_rsrc(selp(p.wu, p.wqkv), sel(D * F * 2, D * 3 * D * 2));
  const rsrc_t r2 = mk_rsrc(selp(p.wu, p.x), sel(D * F * 2, S * D * 2));
  unsigned v[8];
  {
    const int r = l >> 4;
    const unsigned g0 = (16 * lw + r) * 3072 + ct * 256 + (((l & 15) ^ (r << 1)) << 4);
    const int colA = ISQ ? 320 * ct : 1280 + 160 * ct, colB = ISQ ? 160 : 1280;
#pragma unroll
    for (int j = 0; j < 5; ++j) {
      const int c = 64 * j + l, rr = (c * 3277) >> 16;
      int cc = c - 20 * rr;
      if (!ISQ) cc ^= (rr >> 2) & 2;
      const unsigned a = (16 * lw + rr) * 3840 + colA + cc * 16 + (cc < 10 ? 0 : colB - 160);
      v[j] = sel((g0 ^ ((j & 2) << 6)) + (j & 3) * 4 * 3072, a);
    }
    v[5] = sel(v[1], xb - 4 * 8 * 1280);
    v[6] = sel(v[2], xa - 2 * 8 * 1280);
    v[7] = sel(v[3], xb - 2 * 8 * 1280);
  }
  const int d0 = sel(20480 + 4096 * lw, 32768 + 5120 * lw), d4 = d0 + sel(16384, 4096), d5 = sel(d4 + 1024, d8 - 3072);
  const unsigned wstep = sel(64 * 3072, 64 * 3840), mstep = sel(64 * 3072, 128);
  auto issue_a = [&](int st, int so) {
    ldsp sb = lds + so;
#pragma unroll
    for (int j = 0; j < 5; ++j) dma16(rx, sb + d8 + j * 1024, (j & 1) ? xb : xa, st * 128 + (j >> 1) * 16 * 1280);
#pragma unroll
    for (int j = 0; j < 2; ++j) dma16(r2, sb + d5 + j * 1024, v[5 + j], st * mstep);
    ORDER_FENCE();
  };
  auto issue_b = [&](int st, int so) {
    ldsp sb = lds + so;
    dma16(r2, sb + d5 + 2048, v[7], st * mstep);
#pragma unroll
    for (int j = 0; j < 4; ++j) dma16(r0, sb + d0 + j * 1024, v[j], st * wstep);
    dma16(r1, sb + d4, v[4], st * wstep);
    ORDER_FENCE();
  };
  auto rot = [&](int j) { const int t = k0 + j; return t >= NST1 ? t - NST1 : t; };
  float gs = 0.f;
  auto gate = [&](int st, int so) {
    if (!ISQ) return;
    const int row = 64 * ct + 16 * lw + (l >> 2);
    const unsigned a = (unsigned)(size_t)(lds + so) + row * 128 + (((2 * (l & 3)) ^ ((row >> 1) & 7)) << 4);
    const unsigned ga = (unsigned)(size_t)(lds + M_GW) + st * 128 + (l & 3) * 32;
    u32x4 x0, x1, g0, g1;
    asm volatile("ds_read_b128 %0, %4\n\tds_read_b128 %1, %5\n\tds_read_b128 %2, %6\n\tds_read_b128 %3, %6 offset:16\n\ts_waitcnt lgkmcnt(0)"
                 : "=&v"(x0), "=&v"(x1), "=&v"(g0), "=&v"(g1) : "v"(a), "v"(a ^ 16), "v"(ga) : "memory");
#pragma unroll
    for (int e = 0; e < 4; ++e) {
      const unsigned xa_ = x0[e], xb_ = x1[e], ge = g0[e], gf = g1[e];
      gs = __builtin_amdgcn_fdot2_f32_bf16(__builtin_bit_cast(bf16x2, xa_), __builtin_bit_cast(bf16x2, ge), gs, false);
      gs = __builtin_amdgcn_fdot2_f32_bf16(__builtin_bit_cast(bf16x2, xb_), __builtin_bit_cast(bf16x2, gf), gs, false);
    }
  };
  if (ISQ && lw < 2) dma16(mk_rsrc(p.gw, D * 2), lds + M_GW + lw * 1024, l * 16, lw * 1024);
#pragma unroll
  for (int j = 0; j < 2; ++j) dma16(r2, lds + d5 + j * 1024, v[5 + j], k0 * mstep);
  issue_b(k0, 0);
  FST(1);
  issue_a(rot(1), SLOT);
  FST(2);
  WAIT_VM(7);
  FST(3);
  BARRIER();
  issue_b(rot(1), SLOT); issue_a(rot(2), 2 * SLOT);
  gate(k0, 0);
  WAIT_VM(7);
  BARRIER();
  issue_b(rot(2), 2 * SLOT); issue_a(rot(3), 0); issue_b(rot(3), 0);
  gate(rot(1), SLOT);
  WAIT_VM(13);
  auto ask = [&]() {
    const int line = min((__builtin_amdgcn_readfirstlane(t2) * 4 + lw) * 9 + min(l, 8), 7679);
    Warm(lds).touch((const GAS char*)p.wd + line * 128);
  };
  if (tb) {
    const W2Src src(p, lw, t2);
#pragma unroll
    for (int j = 1; j < NST1 - 3; ++j) {
      BARRIER();
      issue_a(rot(j + 3), (j % 3) * SLOT); issue_b(rot(j + 3), (j % 3) * SLOT);
#pragma unroll
      for (int i = 5 * (j - 1); i < 5 * j; ++i) pre[i] = pre_load(src, i);
      ORDER_FENCE();
      if (j == W0_AT) ask();
      if (j == 1) WAIT_VM(18); else if (j == W0_AT || j == W0_AT + 1) WAIT_VM(24); else WAIT_VM(23);
    }
    BARRIER();
    gap();
    WAIT_VM(5);
  } else {
    int so = SLOT;
#pragma unroll 1
    for (int j = 1; j < NST1 - 3; ++j) {
      BARRIER();
      if (!ISQ && j == 3) raise();
      issue_a(rot(j + 3), so); issue_b(rot(j + 3), so);
      so = so == 2 * SLOT ? 0 : so + SLOT;
      if (ISQ) {
        if (j < 6) reset(j - 1);
        gate(rot(j + 1), so);
        if (j == 1) WAIT_VM(17); else if (j < 6) WAIT_VM(21); else WAIT_VM(13);
      } else {
        if (j == 1) reset(0);
        if (j == W0_AT) ask();
        if (j == 1 || j == W0_AT || j == W0_AT + 1) WAIT_VM(14); else WAIT_VM(13);
      }
    }
    BARRIER();
    if (ISQ) raise();
    gate(rot(8), 2 * SLOT);
    gap();
    WAIT_VM(0);
  }
  BARRIER();
  gate(rot(9), 0);
  return gs;
}

__device__ __forceinline__ void stage_barrier(int st) {
  asm volatile("s_waitcnt lgkmcnt(0)\n\ts_cmp_eq_u32 %0, %1\n\ts_cbranch_scc1 1\n\ts_barrier" ::"s"(st), "i"(NST1 - 1) : "memory", "scc");
}
struct FragQ { bf16x8 w[5], x[2]; };
__device__ __forceinline__ void p1_main_q(ldsp lds, int w, f32x16 (&acc)[5][2]) {
  const int l = threadIdx.x & 63, l31 = l & 31, h = l >> 5;
  const int xa = (64 * w + l31) * 128 + ((h ^ ((l31 >> 1) & 7)) << 4);
  const int wa = 32768 + (8 * h + ((l & 15) >> 2)) * 320 + ((l >> 4) & 1) * 32 + (l & 3) * 8;
  auto load = [&](FragQ& f, ldsp sb, int ks) {
    ldsp xp = sb + (xa ^ (ks << 5));
    ldsp wp = sb + wa + ks * 16 * 320;
    f.x[0] = lds_b128(xp);
    f.w[0] = lds_tr2(wp, wp + 1280);
    f.x[1] = lds_b128(xp + 4096);
#pragma unroll
    for (int n = 1; n < 5; ++n) f.w[n] = lds_tr2(wp + n * 64, wp + n * 64 + 1280);
  };
  auto mma = [&](const FragQ& f) {
#pragma unroll
    for (int n = 0; n < 5; ++n)
#pragma unroll
      for (int s = 0; s < 2; ++s) acc[n][s] = mfma32(f.w[n], f.x[s], acc[n][s]);
#pragma unroll
    for (int i = 0; i < 6; ++i) { SG_MFMA(1); SG_DS(2); }
    SG_MFMA(4);
    SCHED_FENCE();
  };
  FST(0);
  BARRIER();
  FST(1);
  FragQ f0, f1;
  int so = 0;
  load(f0, lds, 0);
  SCHED_FENCE();
#pragma unroll 1
  for (int j = 0; j < NST1; ++j) {
    ldsp sb = lds + so;
    load(f1, sb, 1); mma(f0);
    load(f0, sb, 2); mma(f1);
    load(f1, sb, 3); mma(f0);
    so = so == 2 * SLOT ? 0 : so + SLOT;
    stage_barrier(j);
    FSTD(2 + j);
    SCHED_FENCE();
    load(f0, lds + so, 0); mma(f1);
  }
  WAIT_LGKM0();
}

template <int TY, bool PEEL, class Tail>
__device__ __forceinline__ void p1_main16(ldsp lds, int w, f32x4 (&acc)[4][10], Tail tail) {
  constexpr bool TB = TY == T_GLU;
  const int l = threadIdx.x & 63, l15 = l & 15, g = l >> 4, q = l15 >> 2;
  const int xa = ((TB ? 0 : 64 * w) + l15) * 128 + ((g ^ ((l15 >> 1) & 7)) << 4);
  int wa0, wa1;
  if (TB) {
    wa0 = 20480 + (8 * g + q) * 256 + (((2 * w) ^ (q + 4 * (g & 1))) << 5) + (l & 3) * 8;
    wa1 = wa0 ^ 32;
  } else {
    wa0 = 32768 + (8 * g + q) * 320 + (l & 3) * 8 + 32 * (g & 1);
    wa1 = wa0 - 64 * (g & 1);
  }
  ldsp px0 = lds + xa, px1 = lds + (xa ^ 64), pw0 = lds + wa0, pw1 = lds + wa1;
  auto ldx = [&](int u, int t) { return lds_b128((u ? px1 : px0) + t * 2048); };
  auto ldw = [&](int u, int t) {
    ldsp a = ((t & 1) ? pw1 : pw0) + (TB ? u * 8192 + (t >> 1) * 16384 : u * 10240 + t * 32);
    return lds_tr2(a, a + (TB ? 1024 : 1280));
  };
  auto lda = [&](int u, int i) { return TB ? ldw(u, i) : ldx(u, i); };
  auto ldb = [&](int u, int j) { return TB ? ldx(u, j) : ldw(u, j); };
  auto mma = [&](const bf16x8 (&a)[4], const bf16x8& b, int j) {
#pragma unroll
    for (int i = 0; i < 4; ++i) acc[i][j] = mfma16(a[i], b, acc[i][j]);
    SG_MFMA(1); SG_DS(2); SG_MFMA(1); SG_DS(2); SG_MFMA(1); SG_DS(2); SG_MFMA(1);
    SCHED_FENCE();
  };
  FST(0);
  BARRIER();
  FST(1);
  bf16x8 a0[4], a1[4], b0[10], b1[10];
#pragma unroll
  for (int i = 0; i < 4; ++i) a0[i] = lda(0, i);
#pragma unroll
  for (int j = 0; j < 5; ++j) b0[j] = ldb(0, j);
  SCHED_FENCE();
  int ph = 0;
#pragma unroll 1
  for (int st = 0; st < (PEEL ? NST1 - 1 : NST1); ++st) {
#pragma unroll
    for (int j = 0; j < 10; ++j) {
      if (j < 5) b0[j + 5] = ldb(0, j + 5); else b1[j - 5] = ldb(1, j - 5);
      if (j >= 2 && j < 6) a1[j - 2] = lda(1, j - 2);
      mma(a0, b0[j], j);
    }
#pragma unroll
    for (int j = 0; j < 5; ++j) {
      b1[j + 5] = ldb(1, j + 5);
      mma(a1, b1[j], j);
    }
    {
      const int dlt = ph == 2 ? -2 * SLOT : SLOT;
      ph = ph == 2 ? 0 : ph + 1;
      px0 += dlt; px1 += dlt; pw0 += dlt; pw1 += dlt;
      asm volatile("" : "+v"(px0), "+v"(px1), "+v"(pw0), "+v"(pw1));
#pragma unroll
      for (int i = 0; i < 4; ++i) acc[i][5] = mfma16(a1[i], b1[5], acc[i][5]);
      SG_MFMA(1); SG_VALU(2); SG_MFMA(1); SG_VALU(2); SG_MFMA(2);
      SCHED_FENCE();
    }
    stage_barrier(st);
    FSTD(2 + st);
    SCHED_FENCE();
    a0[0] = lda(0, 0); b0[0] = ldb(0, 0); a0[1] = lda(0, 1); mma(a1, b1[6], 6);
    a0[2] = lda(0, 2); a0[3] = lda(0, 3); b0[1] = ldb(0, 1); mma(a1, b1[7], 7);
    b0[2] = ldb(0, 2); b0[3] = ldb(0, 3); mma(a1, b1[8], 8);
    b0[4] = ldb(0, 4); mma(a1, b1[9], 9);
  }
  if (PEEL) {
#pragma unroll
    for (int j = 0; j < 10; ++j) {
      if (j < 5) b0[j + 5] = ldb(0, j + 5); else b1[j - 5] = ldb(1, j - 5);
      if (j >= 2 && j < 6) a1[j - 2] = lda(1, j - 2);
      mma(a0, b0[j], j);
    }
#pragma unroll
    for (int j = 0; j < 10; ++j) {
      if (j < 5) b1[j + 5] = ldb(1, j + 5);
#pragma unroll
      for (int i = 0; i < 4; ++i) acc[i][j] = mfma16(a1[i], b1[j], acc[i][j]);
      if (j > 0) tail(j - 1);
      SG_MFMA(1); SG_DS(2); SG_VALU(10); SG_MFMA(1); SG_VALU(10); SG_MFMA(1); SG_VALU(10); SG_MFMA(1);
      SCHED_FENCE();
      if (j == 4) lds_arrive(lds + M_END);
    }
    tail(9);
  } else {
    lds_arrive(lds + M_END);
    WAIT_LGKM0();
  }
}

__device__ __forceinline__ void p2_main(const Params& p, ldsp lds, int w, int rt, int ct, u64 tag, const u32x4 (&held)[15]) {
  constexpr int LAG = 3, AW = P2_AW, NR = 17, RH = 12, GATE = P2_GATE;
  const int l = threadIdx.x & 63, l15 = l & 15, kg = l >> 4, q = l15 >> 2;
  const int sg = w & 1, kq = w >> 1, hf = w >> 2;
  const int rlo = rt * 96, rhi = min(rlo + 95, S - 1);
  const rsrc_t ra = mk_rsrc(p.ws, A2_BYTES);
  const unsigned av = (unsigned)(((rt * 6 + sg * 3) * KB_ALL + kq) * 1024 + l * 16);
  const int rh = 4 * kg + q, ro = 16 * (kg >> 1) + 4 * (kg & 1) + q;
  const int wh = rh * 128 + (((rh >> 1) & 3) << 5) + (l & 3) * 8;
  const int wo = ro * 128 + (((ro >> 1) & 3) << 5) + (l & 3) * 8;
  ldsp hdst = lds + ((w & 3) * 1024 + hf * 4096 + l * 16);
  f32x4 acc[4][3];
#pragma unroll
  for (int n = 0; n < 4; ++n)
#pragma unroll
    for (int s = 0; s < 3; ++s) acc[n][s] = f32x4{};
#define PIN_ACC(text) \
  asm volatile(text : "+v"(acc[0][0]), "+v"(acc[1][0]), "+v"(acc[2][0]), "+v"(acc[3][0]), "+v"(acc[0][1]), "+v"(acc[1][1]), "+v"(acc[2][1]), \
               "+v"(acc[3][1]), "+v"(acc[0][2]), "+v"(acc[1][2]), "+v"(acc[2][2]), "+v"(acc[3][2]))
  PIN_ACC("");
  u32x4 a[NR][3];
  bf16x8 wf[2][4];
  float gi[3];
  auto lda = [&](int t) {
#pragma unroll
    for (int s = 0; s < 3; ++s) {
      a[t][s] = __builtin_amdgcn_raw_buffer_load_b128(ra, av + s * KB_ALL * 1024, t * 4096, A2AUX);
    }
  };
  ldsp wb[4];
  auto ldw = [&](int t) {
    const int o0 = w2_off(4 * t), r0 = o0 & ~0x7FFF;
    auto straight = [](int u) { return w2_off(4 * u + 3) == w2_off(4 * u) + 3 * 4096; };
    if (t == 0 || t == RH || r0 != (w2_off(4 * t - 4) & ~0x7FFF) || !straight(t) || !straight(t - 1)) {
      int rel = kq * 4096;
      if (!straight(t)) rel = kq == 0 ? 0 : kq == 1 ? w2_off(4 * t + 1) - o0 : kq == 2 ? w2_off(4 * t + 2) - o0 : w2_off(4 * t + 3) - o0;
#pragma unroll
      for (int n = 0; n < 4; ++n) {
        wb[n] = lds + (((t < RH ? wh : wo) ^ ((n ^ kq) << 5)) + r0 + rel);
        asm volatile("" : "+v"(wb[n]));
      }
    }
#pragma unroll
    for (int n = 0; n < 4; ++n) wf[t & 1][n] = lds_tr2(wb[n] + (o0 - r0), wb[n] + (o0 - r0) + (t < RH ? 2048 : 1024));
  };
  if (w == 4)
    poll(flag_ptr(p, l < 32 ? FL_H + ((l & 16) ? rhi : rlo) / 160 * 12 + min(l15, 11)
                     : (l < 48 ? FL_G : FL_O) + (((l & 4) ? rhi : rlo) >> 8) * 4 + (l & 3)), tag);
  const unsigned cnt = (unsigned)(size_t)(lds + M_RND);
  auto arrive = [&](int r) {
    asm volatile("s_mov_b64 exec, 1\n\tds_add_u32 %0, %1 offset:%2\n\ts_mov_b64 exec, -1" ::"v"(cnt), "v"(1u), "i"(4 * r) : "memory");
  };
  auto counted = [](int r) { return (r >= 0 && r < GATE - LAG) || (r >= GATE && r < RH); };
  unsigned peek = 0;
  auto check = [&](int r) {
    unsigned sv, n;
    asm volatile(
        "s_nop 0\n\tv_readfirstlane_b32 %0, %2\n\ts_cmp_ge_u32 %0, 8\n\ts_cbranch_scc1 .Lok%=\n\ts_mov_b32 %1, 0x100000\n"
        ".Lagain%=:\n\tds_read_b32 %2, %3 offset:%4\n\ts_waitcnt lgkmcnt(0)\n\tv_readfirstlane_b32 %0, %2\n\ts_sub_u32 %1, %1, 1\n\t"
        "s_cbranch_scc1 .Lok%=\n\ts_cmp_ge_u32 %0, 8\n\ts_cbranch_scc0 .Lagain%=\n.Lok%=:"
        : "=&s"(sv), "=&s"(n), "+v"(peek) : "v"(cnt), "i"(4 * r) : "memory", "scc");
  };
  auto prio = [](int v) {
    if (v >= 3) __builtin_amdgcn_s_setprio(3); else if (v == 2) __builtin_amdgcn_s_setprio(2);
    else if (v == 1) __builtin_amdgcn_s_setprio(1); else __builtin_amdgcn_s_setprio(0);
  };
  auto upto = [](int t) {
    if (t < GATE) return min(min(t + 1 + AW, P2_A0 + P2_RAMP * (t + 1)), RH);
    return min(RH + P2_G0 + P2_G1 * (t - GATE), NR);
  };
  BARRIER();
  STAMP(4);
  {
    const rsrc_t rg = mk_rsrc(p.ws + G_OFF, G_BYTES);
#pragma unroll
    for (int t = 0; t < upto(-1); ++t) {
      prio(3 - t); lda(t);
      if (t == 0) {
        ldw(0);
#pragma unroll
        for (int s = 0; s < 3; ++s) gi[s] = __builtin_bit_cast(float, __builtin_amdgcn_raw_buffer_load_b32(rg, (rt * 96 + sg * 48 + s * 16 + l15) * 4, 0, SC1));
      }
      SCHED_FENCE();
    }
  }
#pragma unroll
  for (int t = 0; t < NR; ++t) {
    if (t == GATE) {
      if (w == 7) {
        const unsigned co = (unsigned)(((rt * 6 + min(l >> 3, 5)) * KB_ALL + 48) * 1024 + (l & GATE_HG) * 2048);
#pragma unroll 1
        for (int spin = 0; spin < SPIN_MAX; ++spin) {
          if (__all(__builtin_amdgcn_raw_buffer_load_b32(ra, co, 0, SC1) != UNSET)) break;
          asm volatile("s_sleep 1" ::: "memory");
        }
      }
      WAIT_LGKM0();
      FST(16);
      BARRIER();
      FST(17);
      SCHED_FENCE();
    }
    if (counted(t - LAG)) check(t - LAG);
    if (counted(t + 1 - LAG)) peek = __hip_atomic_load((LAS unsigned*)(lds + M_RND + 4 * (t + 1 - LAG)), __ATOMIC_RELAXED, __HIP_MEMORY_SCOPE_WORKGROUP);
    SCHED_FENCE();
    prio(3 - (t & 3));
#pragma unroll
    for (int u = upto(t - 1); u < upto(t); ++u) lda(u);
    if (t + 1 < NR) ldw(t + 1);
    if (t >= LAG) {
#pragma unroll
      for (int i = 2 * (t - LAG); i < min(2 * (t - LAG) + 2, 15); ++i) {
        *(LAS u32x4*)(hdst + w2_off(39 + 2 * i)) = held[i];
      }
    }
    if (t >= RH) {
      auto unset = [&]() {
        unsigned m = 0u;
#pragma unroll
        for (int s = 0; s < 3; ++s) m = max(max(m, max(a[t][s][0], a[t][s][1])), max(a[t][s][2], a[t][s][3]));
        return __any(m == UNSET);
      };
      if (__builtin_expect(unset(), 0)) {
        int spin = 0;
#pragma unroll 1
        do { lda(t); __builtin_amdgcn_s_waitcnt(0x0F70); } while (unset() && ++spin < SPIN_MAX);
      }
      SCHED_FENCE();
    }
    if (t == RH) {
      PIN_ACC("s_nop 7");
#pragma unroll
      for (int n = 0; n < 4; ++n)
#pragma unroll
        for (int s = 0; s < 3; ++s) acc[n][s] *= 1.f - gi[s];
    }
    if (t >= RH) PIN_ACC("s_nop 1");
#pragma unroll
    for (int s = 0; s < 3; ++s)
#pragma unroll
      for (int n = 0; n < 4; ++n) {
        asm volatile("v_mfma_f32_16x16x32_bf16 %0, %1, %2, %0" : "+v"(acc[n][s]) : "v"(wf[t & 1][n]), "v"(a[t][s]));
      }
    if (counted(t)) arrive(t);
    SCHED_FENCE();
  }
  PIN_ACC("s_nop 7");
  STAMP(5);
  constexpr int XB = 16384;
  static_assert(w2_off(30) == XB && w2_off(39) == XB + 9 * 4096, "ring layout");
  {
    ldsp wp = lds + (XB + w * 9 * 1024 + l * 16);
#pragma unroll
    for (int d = 1; d < 4; ++d)
#pragma unroll
      for (int s = 0; s < 3; ++s) {
        *(LAS f32x4*)(wp + ((d - 1) * 3 + s) * 1024) = acc[d][s];
      }
  }
  const rsrc_t rout = mk_rsrc(p.out, S * D * 2);
  unsigned vo = (unsigned)((rt * 96 + sg * 48 + l15) * 1280 + (ct * 64 + kq * 16 + 4 * kg) * 2);
  asm volatile("" : "+v"(vo));
  WAIT_LGKM0();
  BARRIER();
  f32x4 part[3][4];
#pragma unroll
  for (int s = 0; s < 3; ++s)
#pragma unroll
    for (int d = 1; d < 4; ++d) part[s][d] = *(LAS f32x4*)(lds + XB + ((w ^ (2 * d)) * 9 + (d - 1) * 3 + s) * 1024 + l * 16);
  SCHED_FENCE();
#pragma unroll
  for (int s = 0; s < 3; ++s) {
    const f32x4 v = (acc[0][s] + part[s][1]) + (part[s][2] + part[s][3]);
    __builtin_amdgcn_raw_buffer_store_b64(u32x2{pk2(v[0], v[1]), pk2(v[2], v[3])}, rout, vo + s * 16 * 1280, 0, OUT_AUX);
  }
}

__global__ __launch_bounds__(512, 2) void fused(Params p) {
  extern __shared__ __attribute__((aligned(16))) char smem[];
  ldsp lds = (ldsp)smem;
  u64 pc;
  asm volatile("s_getpc_b64 %0" : "=s"(pc));
  constexpr unsigned rest = CODE_BYTES;
  const int b = blockIdx.x, xcd = b & 7, idx = b >> 3, cg = xcd & 3, rg = xcd >> 2;
  const int w = __builtin_amdgcn_readfirstlane(threadIdx.x >> 6), lw = w - 4;
  const int l = threadIdx.x & 63;
  FINIT();
  STAMP(0);
  const int tm = (11 - idx) >> 31;
  const int jj = idx + ((20 * rg - 12) & tm), j3 = (jj * 43) >> 7, jr = jj - 3 * j3;
  const bool spare = jj == 39;
  const int ty = tm ? T_GLU : (jr == 0 ? T_Q : T_KV);
  const int rt = j3 + (4 * rg & ~tm);
  const int ctn = 2 * cg + jr - 1 + ((cg + 1) & tm);
  const int t2 = 28 * xcd - ((xcd - 4) & -rg) + ((idx - 4) & tm) + ((2 * j3 + jr - 1) & ~tm);
  const int rot0 = ROT;
  auto get_tag = [&]() {
    u64 e;
    asm volatile("s_load_dwordx2 %0, %1, 0x0\n\ts_waitcnt lgkmcnt(0)" : "=s"(e) : "s"(p.ws + EPOCH_OFF) : "memory");
    return make_tag(e);
  };

  constexpr int PQX = 2 * SLOT;
  const bool loader = w >= 4 && !spare;
  Warm wm(lds);
  u64 tag;
  auto mfma_start = [&]() {
    if (idx < 2) {
      const unsigned o = (idx * 256 + w * 64 + l) * 128;
      wm.touch((const GAS char*)pc + (o < rest ? o : 0u));
    }
    if (w == 0) {
      tag = get_tag();
      if (l < 32) *(LAS unsigned*)(lds + M_CNT + l * 4) = 0u;
      if (l == 0) *(LAS u64*)(lds + M_TAG) = tag;
      WAIT_LGKM0();
    }
  };
  auto lds_tag = [&]() {
    u32x2 v;
    asm volatile("ds_read_b64 %0, %1\n\ts_waitcnt lgkmcnt(0)" : "=v"(v) : "v"((unsigned)(size_t)(lds + M_TAG)) : "memory");
    const unsigned lo = v[0], hi = v[1];
    tag = (u64)(unsigned)__builtin_amdgcn_readfirstlane(hi) << 32 | (unsigned)__builtin_amdgcn_readfirstlane(lo);
  };
  if (__builtin_expect(ty == T_Q, 0)) {
    const int ct = cg;
    bf16x8 pq[10];
    if (loader) {
      u32x4 none[30];
      float gs = p1_loader<true>(p, lds, lw, 0, rt, ct, rot0, 0, none, [&](int kb) {
        const rsrc_t ra = mk_rsrc(p.ws, A2_BYTES);
        const unsigned vo = (unsigned)(((rt * 16 + lw * 4) * KB_ALL + 48 + (kb < 4 ? 4 * ct + kb : 16 + ct)) * 1024 + l * 16);
#pragma unroll
        for (int i = 0; i < 4; ++i) st128_sc1(ra, vo + i * KB_ALL * 1024, u32x4{UNSET, UNSET, UNSET, UNSET});
      }, [&]() {
        lds_tag();
        if (lw == 0 && l == 0) __hip_atomic_store(flag_ptr(p, FL_O + rt * 4 + ct), tag, __ATOMIC_RELAXED, __HIP_MEMORY_SCOPE_AGENT);
      }, [&]() {
        {
          const unsigned o = ((j3 * 4 + lw) * 26 + min(l, 25)) * 128;
          wm.touch((const GAS char*)pc + (o < rest ? o : 0u));
        }
        for (int i = lw * 64 + l; i < 2 * KVT_SZ / 16; i += 256)
          asm volatile("ds_write_b128 %0, %1" ::"v"((unsigned)(size_t)(lds + KVT) + i * 16), "v"(u32x4{0u, 0u, 0u, 0u}) : "memory");
        if (lw == 0) poll(flag_ptr(p, FL_KV + (l & 7) * 8 + 2 * ct + ((l >> 3) & 1)), tag);
        WAIT_LGKM0();
      });
      gs += __shfl_xor(gs, 1); gs += __shfl_xor(gs, 2);
      if ((l & 3) == 0)
        __builtin_amdgcn_raw_buffer_store_b32(__builtin_bit_cast(unsigned, sigmoidf(gs)), mk_rsrc(p.ws + G_OFF, G_BYTES), (rt * 256 + 64 * ct + 16 * lw + (l >> 2)) * 4, 0, SC1);
      publish(p, lds, FL_G + rt * 4 + ct, tag, 4);
      poll(flag_ptr(p, FL_G + rt * 4 + (l & 3)), tag);
      const int tl = lw * 64 + l;
      *(LAS unsigned*)(lds + M_G + tl * 4) = __builtin_amdgcn_raw_buffer_load_b32(mk_rsrc(p.ws + G_OFF, G_BYTES), (rt * 256 + tl) * 4, 0, SC1);
      lds_wait(lds + M_END, 4);
#pragma unroll
      for (int t = 0; t < 10; ++t) { pq[t] = lds_b128(lds + PQX + (lw * 10 + t) * 1024 + l * 16); asm volatile("" : "+v"(pq[t])); }
    } else {
    mfma_start();
    f32x16 acc[5][2];
#pragma unroll
    for (int n = 0; n < 5; ++n) { acc[n][0] = f32x16{}; acc[n][1] = f32x16{}; }
    p1_main_q(lds, w, acc);
    STAMP(1);
    const Consts cn(1.f);
#pragma unroll
    for (int k = 9; k >= 0; --k)
#pragma unroll
      for (int s = 0; s < 2; ++s) {
        const f32x16& c = acc[k >> 1][s];
        const int o = 8 * (k & 1);
        const bf16x8 f = pack44(phi4(f32x4{c[o], c[o + 1], c[o + 2], c[o + 3]}, cn), phi4(f32x4{c[o + 4], c[o + 5], c[o + 6], c[o + 7]}, cn));
        if (k >= 5) *(LAS bf16x8*)(lds + PQX + (w * 10 + 5 * s + k - 5) * 1024 + l * 16) = f;
        else { pq[5 * s + k] = f; asm volatile("" : "+v"(pq[5 * s + k])); }
        if (k == 5 && s == 1) { WAIT_LGKM0(); lds_arrive(lds + M_END); }
      }
    STAMP(2);
    }
    {
      const int ch = min((int)threadIdx.x, 419), hg = ch >= 210;
      ldsp dp[2];
      bool dup[2];
#pragma unroll
      for (int u = 0; u < 2; ++u) {
        const int un = 2 * (ch - 210 * hg) + u, row = un / 5, dq = un % 5, hd = row < 80 ? row / 20 : row - 80, run = 5 * hd + dq;
        dp[u] = lds + KVT + hg * KVT_SZ + (32 * hd + (row < 80 ? row - 20 * hd : 20)) * KS + ((run >> 2) - hd) * 32 + (run & 1) * 16 + (run & 2) * 4;
        dup[u] = row >= 80;
      }
      const rsrc_t rk = mk_rsrc(p.ws + KVP_OFF, KVP_BYTES);
      u32x4 pv[8];
      unsigned todo = 0xFFu;
#pragma unroll 1
      for (int spin = 0; spin < SPIN_MAX && todo; ++spin) {
#pragma unroll
        for (int r = 0; r < 8; ++r)
          if (todo >> r & 1) pv[r] = __builtin_amdgcn_raw_buffer_load_b128(rk, 2 * ct * 3360 + ch * 16, r * 8 * 3360, SC1);
#pragma unroll
        for (int r = 0; r < 8; ++r)
          if (__all(max(max(pv[r][0], pv[r][1]), max(pv[r][2], pv[r][3])) != UNSET)) todo &= ~(1u << r);
        asm volatile("s_sleep 1" ::: "memory");
      }
      STAMP(3);
      f32x2 sum[4];
#pragma unroll
      for (int e = 0; e < 4; ++e) sum[e] = f32x2{0.f, 0.f};
#pragma unroll
      for (int r = 0; r < 8; ++r)
#pragma unroll
        for (int e = 0; e < 4; ++e) {
          const unsigned x = pv[r][e];
          sum[e] += f32x2{__uint_as_float(x << 16), __uint_as_float(x & 0xFFFF0000u)};
        }
#pragma unroll
      for (int u = 0; u < 2; ++u) {
        const u32x2 v = {pk2(sum[2 * u][0], sum[2 * u][1]), pk2(sum[2 * u + 1][0], sum[2 * u + 1][1])};
        *(LAS u32x2*)dp[u] = v;
        if (dup[u]) *(LAS u32x2*)(dp[u] + 4 * KS) = v;
      }
      WAIT_LGKM0();
      BARRIER();
      STAMP(4);
    }
  {
    const int l31 = l & 31, h = l >> 5, hg = w >> 2, wr = w & 3;
    const rsrc_t ra = mk_rsrc(p.ws, A2_BYTES);
    const unsigned vo = (unsigned)(((rt * 16 + wr * 4 + ((l >> 4) & 1)) * KB_ALL + 48) * 1024 + (2 * ct + hg) * 2048 + h * 256 + (l & 15) * 16);
    const unsigned vx = vo + 16384 - (2 * ct + hg) * 1536;
    bf16x8 af[4][2];
#pragma unroll
    for (int hd = 0; hd < 4; ++hd)
#pragma unroll
      for (int t = 0; t < 2; ++t) af[hd][t] = lds_b128(lds + (KVT + hg * KVT_SZ + l31 * KS + h * 16) + 32 * hd * KS + t * 32);
    float gv[2];
#pragma unroll
    for (int s = 0; s < 2; ++s) gv[s] = *(LAS float*)(lds + M_G + (64 * wr + 32 * s + l31) * 4);
    FST(18);
    SCHED_FENCE();
    f32x16 acc[2][4];
#pragma unroll
    for (int s = 0; s < 2; ++s)
#pragma unroll
      for (int hd = 3; hd >= 0; --hd) acc[s][hd] = mfma32(af[hd][1], pq[5 * s + hd + 1], mfma32(af[hd][0], pq[5 * s + hd], f32x16{}));
    u32x4 xs[2];
#pragma unroll
    for (int s = 0; s < 2; ++s) {
      u32x4 pc[4];
      unsigned ex[4][2];
#pragma unroll
      for (int hd = 3; hd >= 0; --hd) {
        const f32x16& c = acc[s][hd];
        float d0 = c[12], d1 = c[8];
        asm volatile("" : "+v"(d0), "+v"(d1));
        const float f = gv[s] * __builtin_amdgcn_rcpf((h ? d1 : d0) + 1e-6f);
        const f32x2 ff = {f, f};
        unsigned r[6];
#pragma unroll
        for (int i = 0; i < 6; ++i) { const f32x2 y = f32x2{c[2 * i], c[2 * i + 1]} * ff; r[i] = pk2(y[0], y[1]); }
        pc[hd] = u32x4{r[0], r[1], r[2], r[3]};
        ex[hd][0] = r[4]; ex[hd][1] = r[5];
        if (hd > 0) st128_sc1(ra, vo + s * 2 * KB_ALL * 1024 + hd * 512, pc[hd]);
      }
      u32x4 x;
#pragma unroll
      for (int i = 0; i < 4; ++i) x[i] = __builtin_amdgcn_permlane32_swap(ex[i >> 1][i & 1], ex[2 + (i >> 1)][i & 1], false, false)[0];
      st128_sc1(ra, vo + s * 2 * KB_ALL * 1024, pc[0]);
      xs[s] = x;
    }
    st128_sc1(ra, vx, xs[0]);
    st128_sc1(ra, vx + 2 * KB_ALL * 1024, xs[1]);
    STAMP(5);
    STAMP(6);
    FDUMP();
    return;
  }
  }
  const int ct = ctn;
  u32x4 held[15];
  if (__builtin_expect(loader, 1)) {
    u32x4 pre[30];
    p1_loader<false>(p, lds, lw, tm, rt, ct, rot0, t2, pre, [&](int) {
      st128_sc1(mk_rsrc(p.ws + KVP_OFF + (rt * 8 + ct) * 3360, 3360), lw * 1024 + l * 16, u32x4{UNSET, UNSET, UNSET, UNSET});
    }, [&]() {
      lds_tag();
      if (lw == 0 && l == 0) __hip_atomic_store(flag_ptr(p, FL_KV + rt * 8 + ct), tag, __ATOMIC_RELAXED, __HIP_MEMORY_SCOPE_AGENT);
    }, [&]() { lds_tag(); });
    const W2Src src(p, lw, t2);
    src.fill<0, 24>(lds + lw * 1024);
    if (ty == T_KV) {
#pragma unroll
      for (int i = 0; i < 30; ++i) pre[i] = pre_load(src, i);
      SCHED_FENCE();
      BARRIER(); BARRIER();
    } else {
      lds_wait(lds + M_END, 4);
    }
    WAIT_VM(0);
#pragma unroll
    for (int i = 0; i < 15; ++i) *(LAS u32x4*)(lds + (lw * 1024 + l * 16) + w2_off(24 + i)) = pre[i];
#pragma unroll
    for (int i = 0; i < 15; ++i) held[i] = pre[15 + i];
    WAIT_LGKM0();
  } else {
    mfma_start();
    if (spare) {
#pragma unroll 1
      for (int n = cg * 8 + w; n < 220; n += 32) {
        const int k = n / 5, line = (n - 5 * k) * 64 + l, r = line / 10;
        const int st = k < 15 ? 24 + k : k < 29 ? 10 + 2 * k : 2 * k - 19;
        const int row = st < 48 ? 32 * st + r : st < 64 ? 40 * (st - 48) + 20 * (r >> 4) + (r & 15)
                                             : 80 * (2 * (st - 64) + (r >> 4)) + 16 + 20 * ((r >> 3) & 1) + 40 * ((r >> 2) & 1) + (r & 3);
        wm.touch((const GAS char*)(st < 48 ? p.wd : p.wo) + row * 1280 + (line - 10 * r) * 128);
        WAIT_VM(1);
      }
      STAMP(6);
      FDUMP();
      if (cg == 0 && w == 0) {
#pragma unroll 1
        for (int i = 0; i < 5; ++i) poll(flag_ptr(p, min(i * 64 + l, NFLAG - 1)), tag);
        if (l == 0) __hip_atomic_store((GAS u64*)(p.ws + EPOCH_OFF), tag, __ATOMIC_RELAXED, __HIP_MEMORY_SCOPE_AGENT);
      }
      return;
    }
      f32x4 acc[4][10];
#pragma unroll
      for (int i = 0; i < 4; ++i)
#pragma unroll
        for (int j = 0; j < 10; ++j) acc[i][j] = f32x4{};
      if (ty == T_GLU) {
        const Consts cn(-1.f);
        const rsrc_t ra = mk_rsrc(p.ws, A2_BYTES);
        const unsigned vo = (unsigned)((rt * 10 * KB_ALL + ct * 4 + w) * 1024 + l * 16);
        auto fin = [&](int j) {
          st128_sc1(ra, vo + j * KB_ALL * 1024, __builtin_bit_cast(u32x4, pack44(glu4(acc[0][j], acc[2][j], cn), glu4(acc[1][j], acc[3][j], cn))));
        };
        p1_main16<T_GLU, true>(lds, w, acc, fin);
        STAMP(2);
        lds_tag();
        publish(p, lds, FL_H + rt * 12 + ct, tag, 4);
      } else {
        int goff[2];
#pragma unroll
        for (int i = 0; i < 2; ++i) {
          const int en = min(2 * (int)threadIdx.x + i, 419), row = en / 5, dq = en % 5;
          const int hd = row < 80 ? row / 20 : row - 80, d0 = 20 * hd + 4 * dq, ti = d0 >> 4, tj = row >> 4;
          const int tile = row >= 80 ? 9 : ti == tj ? ti : (ti == 0 || ti == 4) ? 8 : 4 + ti;
          goff[i] = tile * 1024 + (((d0 & 15) >> 2) * 16 + (row < 80 ? row & 15 : ti)) * 16;
          asm volatile("" : "+v"(goff[i]));
        }
        p1_main16<T_KV, false>(lds, w, acc, [](int) {});
        STAMP(1);
        const Consts cn(1.f);
        const int l15 = l & 15, g = l >> 4;
        ldsp tp = lds + (w * 10240 + l * 16);
        bf16x8 vf[2][5];
#pragma unroll
        for (int u = 0; u < 2; ++u)
#pragma unroll
          for (int j = 0; j < 5; ++j) vf[u][j] = pack44(acc[2 * u][5 + j], acc[2 * u + 1][5 + j]);
        f32x4 z = {}, dg[5], lo[5], hi[5];
        SCHED_FENCE();
#pragma unroll
        for (int i = 0; i < 5; ++i) {
          const unsigned o1 = l15 == i ? 0x3F803F80u : 0u;
          const bf16x8 one = __builtin_bit_cast(bf16x8, u32x4{o1, o1, o1, o1});
          dg[i] = f32x4{}; lo[i] = f32x4{}; hi[i] = f32x4{};
#pragma unroll
          for (int u = 0; u < 2; ++u) {
            const bf16x8 kf = pack44(phi4(acc[2 * u][i], cn), phi4(acc[2 * u + 1][i], cn));
            if (i > 0) lo[i] = mfma16(kf, vf[u][i - 1], lo[i]);
            if (i < 4) hi[i] = mfma16(kf, vf[u][i + 1], hi[i]);
            dg[i] = mfma16(kf, vf[u][i], dg[i]);
            z = mfma16(kf, one, z);
            if (i > 0) {
              if (u == 0) *(LAS f32x4*)(tp + (i - 1) * 1024) = dg[i - 1];
              else if (i > 1) *(LAS f32x4*)(tp + (3 + i) * 1024) = g < i - 1 ? lo[i - 1] : hi[i - 1];
            }
            SCHED_FENCE();
          }
          if (i == 0) { FST(18); lds_wait(lds + M_END, 4); }
        }
        FST(19);
        *(LAS f32x4*)(tp + 8 * 1024) = l15 < 8 ? hi[0] : lo[4];
        *(LAS f32x4*)(tp + 4 * 1024) = dg[4];
        *(LAS f32x4*)(tp + 9 * 1024) = z;
        WAIT_LGKM0();
        FST(20);
        BARRIER();
        FST(21);
        const rsrc_t rk = mk_rsrc(p.ws + KVP_OFF + (rt * 8 + ct) * 3360, 3360);
        f32x4 part[2][4];
#pragma unroll
        for (int i = 0; i < 2; ++i)
#pragma unroll
          for (int ww = 0; ww < 4; ++ww) part[i][ww] = *(LAS f32x4*)(lds + goff[i] + ww * 10240);
        SCHED_FENCE();
        u32x4 res = __builtin_bit_cast(u32x4, pack44((part[0][0] + part[0][1]) + (part[0][2] + part[0][3]), (part[1][0] + part[1][1]) + (part[1][2] + part[1][3])));
#pragma unroll
        for (int e = 0; e < 4; ++e) res[e] = min(res[e], UNSET - 1);
        st128_sc1(rk, threadIdx.x * 16, res);
        WAIT_LGKM0();
        FST(22);
        BARRIER();
        STAMP(2);
      }
      STAMP(3);
      const W2Src src(p, w, t2);
#pragma unroll
      for (int i = 0; i < 15; ++i) held[i] = src.load(39 + 2 * i);
      SCHED_FENCE();
  }
  int t2l = __builtin_amdgcn_readfirstlane(t2);
  asm volatile("" : "+s"(t2l));
  const int rt2 = (t2l * 205) >> 11;
  p2_main(p, lds, w, rt2, t2l - 10 * rt2, tag, held);
  STAMP(6);
  FDUMP();
}

#define STAMP_PTR nullptr
extern "C" void kernel_launch(void* const* d_in, const int* in_sizes, int n_in, void* d_out, int out_size, void* d_ws, size_t ws_size,
                              hipStream_t stream) {
  Params p;
  p.x = (const u16*)d_in[0]; p.wqkv = (const u16*)d_in[1]; p.wo = (const u16*)d_in[2];
  p.wg = (const u16*)d_in[4]; p.wu = (const u16*)d_in[5]; p.wd = (const u16*)d_in[6]; p.gw = (const u16*)d_in[7];
  p.out = (u16*)d_out; p.ws = (char*)d_ws; p.stamps = STAMP_PTR;
  (void)hipFuncSetAttribute((const void*)fused, hipFuncAttributeMaxDynamicSharedMemorySize, LDS_BYTES);
  hipLaunchKernelGGL(fused, dim3(256), dim3(512), LDS_BYTES, stream, p);
}
```

```cpp
#define DIAG 0
#define DIAG_THR 2650
#include <hip/hip_runtime.h>
#include <hip/hip_bf16.h>

typedef unsigned short u16;
typedef unsigned long long u64;
typedef __bf16 bf16x8 __attribute__((ext_vector_type(8)));
typedef __bf16 bf16x4 __attribute__((ext_vector_type(4)));
typedef __bf16 bf16x2 __attribute__((ext_vector_type(2)));
typedef float f32x16 __attribute__((ext_vector_type(16)));
typedef float f32x4 __attribute__((ext_vector_type(4)));
typedef unsigned u32x4 __attribute__((ext_vector_type(4)));
typedef unsigned u32x2 __attribute__((ext_vector_type(2)));
#define LAS __attribute__((address_space(3)))
#define GAS __attribute__((address_space(1)))
typedef LAS char* ldsp;
typedef __amdgpu_buffer_rsrc_t rsrc_t;

constexpr int S = 2048, D = 640, F = 1536;
constexpr float LOG2E = 1.4426950408889634f;
constexpr int KB_ALL = 68;
constexpr unsigned A2_BYTES = 128u * KB_ALL * 1024;
constexpr unsigned KVP_OFF = A2_BYTES, KVP_BYTES = 8 * 8 * 3360;
constexpr unsigned G_OFF = KVP_OFF + KVP_BYTES, G_BYTES = S * 4;
constexpr unsigned FLAG_OFF = G_OFF + G_BYTES;
constexpr unsigned EPOCH_OFF = FLAG_OFF + 256 * 128;
constexpr unsigned WS_USED = EPOCH_OFF + 128;
constexpr int FL_H = 0, FL_KV = 156, FL_O = 220, FL_G = 252;
constexpr int SC1 = 16;
constexpr unsigned UNSET = 0xFFFFFFFFu;
constexpr int SLOT = 53248, MISC = 3 * SLOT, LDS_BYTES = 163840;
constexpr int M_DUMMY = MISC, M_CNT = MISC + 256  , M_END = MISC + 260  , M_RND = MISC + 320  , M_Z = MISC + 512  , M_G = MISC + 2048  ;
constexpr int KS = 176, KVT = SLOT, KVT_SZ = 96 * KS;
constexpr int NST1 = 10;
constexpr int SPIN_MAX = 1 << 16;
constexpr unsigned CODE_BYTES = 44000;
#define A2AUX SC1
#define GLU_PACE 0
#define OUT_AUX 0
#define P2_AW 3
#define P2_X3 99
#define P2_PRIO 2
#define P2_A0 1
#define P2_RAMP 2
#define P2_G0 2
#define P2_G1 1
#define P2_WL 1
#define P2_POS 0
#define ROT (2 * (cg & 1) + 5 * (((cg >> 1) ^ rg) & 1))
#define W2_ST 4

struct Params {
  const u16 *x, *wqkv, *wo, *wg, *wu, *wd, *gw;
  u16* out;
  char* ws;
  u64* stamps;
};

#define STAMP(i) do {} while (0)
#define FST(i) do {} while (0)
#define FSTD(i) do {} while (0)
#define FINIT() do {} while (0)
#define FDUMP() do {} while (0)

#define DT(v) asm volatile("s_memrealtime %0\n\ts_waitcnt lgkmcnt(0)" : "=s"(v)::"memory")
struct Diag { u64 t0, t1, t2, t3, t4; };
#define WAIT_VM(n) asm volatile("s_waitcnt vmcnt(" #n ")" ::: "memory")
#define WAIT_LGKM0() asm volatile("s_waitcnt lgkmcnt(0)" ::: "memory")
#define BARRIER() do { asm volatile("" ::: "memory"); __builtin_amdgcn_s_barrier(); asm volatile("" ::: "memory"); } while (0)
#define SCHED_FENCE() __builtin_amdgcn_sched_barrier(0)
#define SG_MFMA(n) __builtin_amdgcn_sched_group_barrier(0x8, n, 0)
#define SG_DS(n) __builtin_amdgcn_sched_group_barrier(0x100, n, 0)
#define SG_DW(n) __builtin_amdgcn_sched_group_barrier(0x200, n, 0)
#define SG_VM(n) __builtin_amdgcn_sched_group_barrier(0x20, n, 0)

__device__ __forceinline__ rsrc_t mk_rsrc(const void* p, unsigned bytes) { return __builtin_amdgcn_make_buffer_rsrc((void*)p, 0, bytes, 0x00020000); }
__device__ __forceinline__ void dma16(rsrc_t r, ldsp dst, unsigned voff, unsigned soff) { __builtin_amdgcn_raw_ptr_buffer_load_lds(r, dst, 16, voff, soff, 0, 0); }
__device__ __forceinline__ bf16x8 lds_b128(ldsp a) { return *(LAS bf16x8*)a; }
__device__ __forceinline__ bf16x8 lds_tr2(ldsp a0, ldsp a1) {
  bf16x4 lo = __builtin_amdgcn_ds_read_tr16_b64_v4bf16((LAS bf16x4*)a0);
  bf16x4 hi = __builtin_amdgcn_ds_read_tr16_b64_v4bf16((LAS bf16x4*)a1);
  return __builtin_shufflevector(lo, hi, 0, 1, 2, 3, 4, 5, 6, 7);
}
__device__ __forceinline__ f32x16 mfma32(bf16x8 a, bf16x8 b, f32x16 c) { return __builtin_amdgcn_mfma_f32_32x32x16_bf16(a, b, c, 0, 0, 0); }
__device__ __forceinline__ f32x4 mfma16(bf16x8 a, bf16x8 b, f32x4 c) { return __builtin_amdgcn_mfma_f32_16x16x32_bf16(a, b, c, 0, 0, 0); }
__device__ __forceinline__ unsigned pk2(float a, float b) {
  bf16x2 v = {(__bf16)a, (__bf16)b};
  return __builtin_bit_cast(unsigned, v);
}
__device__ __forceinline__ bf16x8 pack8(const f32x16& c, int o) {
  u32x4 r = {pk2(c[o], c[o + 1]), pk2(c[o + 2], c[o + 3]), pk2(c[o + 4], c[o + 5]), pk2(c[o + 6], c[o + 7])};
  return __builtin_bit_cast(bf16x8, r);
}
__device__ __forceinline__ bf16x8 pack44(const f32x4& a, const f32x4& b) {
  u32x4 r = {pk2(a[0], a[1]), pk2(a[2], a[3]), pk2(b[0], b[1]), pk2(b[2], b[3])};
  return __builtin_bit_cast(bf16x8, r);
}
__device__ __forceinline__ float phi(float t) {
  const float e = __builtin_amdgcn_exp2f(t * LOG2E);
  return __builtin_amdgcn_fmed3f(t, 1e-6f, __builtin_inff()) + __builtin_amdgcn_fmed3f(e, 0.f, 1.f);
}
__device__ __forceinline__ float sigmoidf(float t) { return __builtin_amdgcn_rcpf(1.f + __builtin_amdgcn_exp2f(-LOG2E * t)); }
typedef float f32x2 __attribute__((ext_vector_type(2)));
struct Consts {
  f32x2 l2e;
  float eps, big;
  __device__ __forceinline__ Consts(float sign) : l2e{sign * LOG2E, sign * LOG2E}, eps(1e-6f), big(3.4028234664e38f) {
    asm volatile("" : "+s"(l2e), "+s"(eps), "+v"(big));
  }
};
__device__ __forceinline__ f32x4 phi4(f32x4 t, const Consts& c) {
  const f32x2 a = f32x2{t[0], t[1]} * c.l2e, b = f32x2{t[2], t[3]} * c.l2e;
  f32x2 m0, m1, e0, e1;
#pragma unroll
  for (int i = 0; i < 2; ++i) {
    m0[i] = __builtin_amdgcn_fmed3f(t[i], c.eps, c.big);
    m1[i] = __builtin_amdgcn_fmed3f(t[2 + i], c.eps, c.big);
    e0[i] = __builtin_amdgcn_fmed3f(__builtin_amdgcn_exp2f(a[i]), 0.f, 1.f);
    e1[i] = __builtin_amdgcn_fmed3f(__builtin_amdgcn_exp2f(b[i]), 0.f, 1.f);
  }
  const f32x2 r0 = m0 + e0, r1 = m1 + e1;
  return f32x4{r0[0], r0[1], r1[0], r1[1]};
}
__device__ __forceinline__ f32x4 glu4(f32x4 g, f32x4 u, const Consts& c) {
  f32x2 a = f32x2{g[0], g[1]} * c.l2e, b = f32x2{g[2], g[3]} * c.l2e;
#pragma unroll
  for (int i = 0; i < 2; ++i) { a[i] = __builtin_amdgcn_exp2f(a[i]); b[i] = __builtin_amdgcn_exp2f(b[i]); }
  a += 1.f; b += 1.f;
#pragma unroll
  for (int i = 0; i < 2; ++i) { a[i] = __builtin_amdgcn_rcpf(a[i]); b[i] = __builtin_amdgcn_rcpf(b[i]); }
  a *= f32x2{u[0], u[1]}; b *= f32x2{u[2], u[3]};
  return f32x4{a[0], a[1], b[0], b[1]};
}
__device__ __forceinline__ void st128_sc1(rsrc_t r, unsigned off, u32x4 v) { __builtin_amdgcn_raw_buffer_store_b128(v, r, off, 0, SC1); }

__device__ __forceinline__ u64 make_tag(u64 e) { return ((e ^ 0x9E3779B97F4A7C15ull) * 0xBF58476D1CE4E5B9ull) | 1ull; }
__device__ __forceinline__ GAS u64* flag_ptr(const Params& p, int f) { return (GAS u64*)(p.ws + FLAG_OFF + (size_t)f * 128); }
__device__ __forceinline__ u64 ld_flag(GAS u64* f) { return __hip_atomic_load(f, __ATOMIC_RELAXED, __HIP_MEMORY_SCOPE_AGENT); }
__device__ __forceinline__ void poll(GAS u64* f, u64 tag) {
#pragma unroll 1
  for (int spin = 0; spin < SPIN_MAX; ++spin) {
    if (__all(ld_flag(f) == tag)) break;
    __builtin_amdgcn_s_sleep(2);
  }
  __builtin_amdgcn_fence(__ATOMIC_ACQUIRE, "wavefront");
}
__device__ __forceinline__ void lds_arrive(ldsp c) {
  if ((threadIdx.x & 63) == 0) __hip_atomic_fetch_add((LAS unsigned*)c, 1u, __ATOMIC_RELAXED, __HIP_MEMORY_SCOPE_WORKGROUP);
}
__device__ __forceinline__ void lds_wait(ldsp c, unsigned n) {
#pragma unroll 1
  for (int spin = 0; spin < SPIN_MAX; ++spin) {
    unsigned v;
    asm volatile("ds_read_b32 %0, %1\n\ts_waitcnt lgkmcnt(0)" : "=v"(v) : "v"((unsigned)(size_t)c) : "memory");
    if (__builtin_amdgcn_readfirstlane(v) >= n) break;
    __builtin_amdgcn_s_sleep(1);
  }
}
__device__ __forceinline__ void publish(const Params& p, ldsp lds, int f, u64 tag, unsigned n) {
  WAIT_VM(0);
  if ((threadIdx.x & 63) == 0) {
    const unsigned old = __hip_atomic_fetch_add((LAS unsigned*)(lds + M_CNT), 1u, __ATOMIC_RELAXED, __HIP_MEMORY_SCOPE_WORKGROUP);
    if (old == n - 1) __hip_atomic_store(flag_ptr(p, f), tag, __ATOMIC_RELAXED, __HIP_MEMORY_SCOPE_AGENT);
  }
}

enum { T_Q = 0, T_KV = 1, T_GLU = 2 };

__device__ __forceinline__ constexpr int w2_off(int s) { return (SLOT + (s % 39) * 4096) % (3 * SLOT); }

struct W2Src {
  rsrc_t rd, ro;
  unsigned wv, ws0;
  __device__ __forceinline__ W2Src(const Params& p, int piece, int ct) {
    const int l = threadIdx.x & 63;
    rd = mk_rsrc(p.wd, F * D * 2);
    ro = mk_rsrc(p.wo, D * D * 2);
    wv = (l >> 3) * 1280 + (((l & 7) ^ ((l >> 4) << 1)) << 4);
    ws0 = (unsigned)(8 * piece) * 1280 + ct * 128;
  }
  __device__ __forceinline__ u32x4 load(int s, int add) const {
    if (s == 47) return __builtin_amdgcn_raw_buffer_load_b128(add ? ro : rd, wv, ws0 + (add ? 0u : 47u * 32 * 1280), 0);
    return __builtin_amdgcn_raw_buffer_load_b128(s < 48 ? rd : ro, wv, ws0 + (unsigned)((s < 48 ? s : s - 48) + add) * 32 * 1280, 0);
  }
  template <int S0, int S1> __device__ __forceinline__ void fill(ldsp dst) const {
#pragma unroll
    for (int s = S0; s < S1; ++s) dma16(rd, dst + w2_off(s), wv, ws0 + (unsigned)s * 32 * 1280);
  }
};

__device__ __forceinline__ u32x4 pre_load(const W2Src& src, int i) { return i < 15 ? src.load(24 + i, 0) : src.load(40 + 2 * (i - 15), 0); }

template <bool ISQ, class Gap>
__device__ __forceinline__ void p1_loader(const Params& p, ldsp lds, int lw, int tm, int rt, int ct, int k0, const W2Src& src, u32x4 (&pre)[30], Gap gap) {
  const bool tb = !ISQ && tm != 0;
  auto sel = [&](int a, int b) { return ISQ ? b : b ^ ((a ^ b) & tm); };
  auto selp = [&](const u16* a, const u16* b) { return ISQ ? b : (const u16*)((u64)b ^ (((u64)a ^ (u64)b) & (u64)(long long)tm)); };
  const int l = threadIdx.x & 63;
  const int i8 = sel(5 * lw, 8 * lw + 3);
  const unsigned xe = (l >> 3) * 1280 + (((l & 7) ^ (l >> 4)) << 4);
  const unsigned xa = (xe ^ ((i8 & 1) << 6)) + (unsigned)(sel(160, 256) * rt + 8 * i8) * 1280, xb = (xa ^ 64) + 8 * 1280;
  const int d8 = i8 * 1024;
  const rsrc_t rx = mk_rsrc(p.x, S * D * 2);
  FST(0);
#pragma unroll
  for (int j = 0; j < 5; ++j) dma16(rx, lds + d8 + j * 1024, (j & 1) ? xb : xa, k0 * 128 + (j >> 1) * 16 * 1280);
  SCHED_FENCE();
  const rsrc_t r0 = mk_rsrc(selp(p.wg, p.wqkv), sel(D * F * 2, D * 3 * D * 2));
  const rsrc_t r1 = mk_rsrc(selp(p.wu, p.wqkv), sel(D * F * 2, D * 3 * D * 2));
  const rsrc_t r2 = mk_rsrc(selp(p.wu, p.x), sel(D * F * 2, S * D * 2));
  unsigned v[8];
  {
    const int r = l >> 4;
    const unsigned g0 = (16 * lw + r) * 3072 + ct * 256 + (((l & 15) ^ (r << 1)) << 4);
    const int colA = ISQ ? 320 * ct : 1280 + 160 * ct, colB = ISQ ? 160 : 1280;
#pragma unroll
    for (int j = 0; j < 5; ++j) {
      const int c = 64 * j + l, rr = (c * 3277) >> 16;
      int cc = c - 20 * rr;
      if (!ISQ) cc ^= (rr >> 2) & 2;
      const unsigned a = (16 * lw + rr) * 3840 + colA + cc * 16 + (cc < 10 ? 0 : colB - 160);
      v[j] = sel((g0 ^ ((j & 2) << 6)) + (j & 3) * 4 * 3072, a);
    }
    v[5] = sel(v[1], xb - 4 * 8 * 1280);
    v[6] = sel(v[2], xa - 2 * 8 * 1280);
    v[7] = sel(v[3], xb - 2 * 8 * 1280);
  }
  const int d0 = sel(20480 + 4096 * lw, 32768 + 5120 * lw), d4 = d0 + sel(16384, 4096), d5 = sel(d4 + 1024, d8 - 3072);
  const unsigned wstep = sel(64 * 3072, 64 * 3840), mstep = sel(64 * 3072, 128);
  auto issue_a = [&](int st, int so) {
    ldsp sb = lds + so;
#pragma unroll
    for (int j = 0; j < 5; ++j) dma16(rx, sb + d8 + j * 1024, (j & 1) ? xb : xa, st * 128 + (j >> 1) * 16 * 1280);
#pragma unroll
    for (int j = 0; j < 2; ++j) dma16(r2, sb + d5 + j * 1024, v[5 + j], st * mstep);
  };
  auto issue_b = [&](int st, int so) {
    ldsp sb = lds + so;
    dma16(r2, sb + d5 + 2048, v[7], st * mstep);
#pragma unroll
    for (int j = 0; j < 4; ++j) dma16(r0, sb + d0 + j * 1024, v[j], st * wstep);
    dma16(r1, sb + d4, v[4], st * wstep);
  };
  auto rot = [&](int j) { const int t = k0 + j; return t >= NST1 ? t - NST1 : t; };
#pragma unroll
  for (int j = 0; j < 2; ++j) dma16(r2, lds + d5 + j * 1024, v[5 + j], k0 * mstep);
  issue_b(k0, 0);
  FST(1);
  issue_a(rot(1), SLOT);
  FST(2);
  WAIT_VM(7);
  FST(3);
  BARRIER();
  issue_b(rot(1), SLOT); issue_a(rot(2), 2 * SLOT);
  WAIT_VM(7);
  BARRIER();
  issue_b(rot(2), 2 * SLOT); issue_a(rot(3), 0); issue_b(rot(3), 0);
  WAIT_VM(13);
  if (tb) {
#pragma unroll
    for (int j = 1; j < NST1 - 3; ++j) {
      BARRIER();
      issue_a(rot(j + 3), (j % 3) * SLOT); issue_b(rot(j + 3), (j % 3) * SLOT);
#pragma unroll
      for (int i = 5 * (j - 1); i < 5 * j; ++i) pre[i] = pre_load(src, i);
      SCHED_FENCE();
      if (j == 1) WAIT_VM(18); else WAIT_VM(23);
    }
    BARRIER();
    gap();
    WAIT_VM(5);
  } else {
    int so = SLOT;
#pragma unroll 1
    for (int j = 1; j < NST1 - 3; ++j) {
      BARRIER();
      issue_a(rot(j + 3), so); issue_b(rot(j + 3), so);
      so = so == 2 * SLOT ? 0 : so + SLOT;
      WAIT_VM(13);
    }
    BARRIER();
    gap();
    WAIT_VM(0);
  }
  BARRIER();
}

struct Warm {
  unsigned sink;
  __device__ __forceinline__ Warm(ldsp lds) : sink((unsigned)(size_t)(lds + M_DUMMY)) {}
  __device__ __forceinline__ void touch(const GAS char* t) {
    unsigned keep;
    asm volatile("s_mov_b32 %0, m0\n\ts_mov_b32 m0, %2\n\ts_nop 0\n\tglobal_load_lds_dword %1, off\n\ts_mov_b32 m0, %0" : "=&s"(keep) : "v"(t), "s"(sink) : "memory");
  }
};

struct FragQ { bf16x8 w[5], x[2]; };
__device__ __forceinline__ void p1_main_q(ldsp lds, int w, f32x16 (&acc)[5][2]) {
  const int l = threadIdx.x & 63, l31 = l & 31, h = l >> 5;
  const int xa = (64 * w + l31) * 128 + ((h ^ ((l31 >> 1) & 7)) << 4);
  const int wa = 32768 + (8 * h + ((l & 15) >> 2)) * 320 + ((l >> 4) & 1) * 32 + (l & 3) * 8;
  auto load = [&](FragQ& f, ldsp sb, int ks) {
    ldsp xp = sb + (xa ^ (ks << 5));
    ldsp wp = sb + wa + ks * 16 * 320;
    f.x[0] = lds_b128(xp);
    f.w[0] = lds_tr2(wp, wp + 1280);
    f.x[1] = lds_b128(xp + 4096);
#pragma unroll
    for (int n = 1; n < 5; ++n) f.w[n] = lds_tr2(wp + n * 64, wp + n * 64 + 1280);
  };
  auto mma = [&](const FragQ& f) {
#pragma unroll
    for (int n = 0; n < 5; ++n)
#pragma unroll
      for (int s = 0; s < 2; ++s) acc[n][s] = mfma32(f.w[n], f.x[s], acc[n][s]);
#pragma unroll
    for (int i = 0; i < 6; ++i) { SG_MFMA(1); SG_DS(2); }
    SG_MFMA(4);
    SCHED_FENCE();
  };
  FST(0);
  BARRIER();
  FST(1);
  FragQ f0, f1;
  int so = 0;
  load(f0, lds, 0);
  SCHED_FENCE();
#pragma unroll 1
  for (int j = 0; j < NST1; ++j) {
    ldsp sb = lds + so;
    load(f1, sb, 1); mma(f0);
    load(f0, sb, 2); mma(f1);
    load(f1, sb, 3); mma(f0);
    so = so == 2 * SLOT ? 0 : so + SLOT;
    WAIT_LGKM0();
    if (j < NST1 - 1) BARRIER();
    FSTD(2 + j);
    SCHED_FENCE();
    load(f0, lds + so, 0); mma(f1);
  }
  WAIT_LGKM0();
}

template <int TY>
__device__ __forceinline__ void p1_main16(ldsp lds, int w, f32x4 (&acc)[4][10], Diag& dg) {
  constexpr bool TB = TY == T_GLU;
  const int l = threadIdx.x & 63, l15 = l & 15, g = l >> 4, q = l15 >> 2;
  const int xa = ((TB ? 0 : 64 * w) + l15) * 128 + ((g ^ ((l15 >> 1) & 7)) << 4);
  int wa0, wa1;
  if (TB) {
    wa0 = 20480 + (8 * g + q) * 256 + (((2 * w) ^ (q + 4 * (g & 1))) << 5) + (l & 3) * 8;
    wa1 = wa0 ^ 32;
  } else {
    wa0 = 32768 + (8 * g + q) * 320 + (l & 3) * 8 + 32 * (g & 1);
    wa1 = wa0 - 64 * (g & 1);
  }
  auto ldx = [&](ldsp sb, int u, int t) { return lds_b128(sb + (xa ^ (u << 6)) + t * 2048); };
  auto ldw = [&](ldsp sb, int u, int t) {
    ldsp a = sb + ((t & 1) ? wa1 : wa0) + (TB ? u * 8192 + (t >> 1) * 16384 : u * 10240 + t * 32);
    return lds_tr2(a, a + (TB ? 1024 : 1280));
  };
  auto lda = [&](ldsp sb, int u, int i) { return TB ? ldw(sb, u, i) : ldx(sb, u, i); };
  auto ldb = [&](ldsp sb, int u, int j) { return TB ? ldx(sb, u, j) : ldw(sb, u, j); };
  auto mma = [&](const bf16x8 (&a)[4], const bf16x8& b, int j) {
#pragma unroll
    for (int i = 0; i < 4; ++i) acc[i][j] = mfma16(a[i], b, acc[i][j]);
    SG_MFMA(1); SG_DS(2); SG_MFMA(1); SG_DS(2); SG_MFMA(1); SG_DS(2); SG_MFMA(1);
    SCHED_FENCE();
  };
  FST(0);
  BARRIER();
  FST(1);
  DT(dg.t1);
  bf16x8 a0[4], a1[4], b0[10], b1[10];
  int so = 0;
#pragma unroll
  for (int i = 0; i < 4; ++i) a0[i] = lda(lds, 0, i);
#pragma unroll
  for (int j = 0; j < 5; ++j) b0[j] = ldb(lds, 0, j);
  SCHED_FENCE();
#pragma unroll 1
  for (int st = 0; st < NST1; ++st) {
    ldsp sb = lds + so;
#pragma unroll
    for (int j = 0; j < 10; ++j) {
      if (j < 5) b0[j + 5] = ldb(sb, 0, j + 5); else b1[j - 5] = ldb(sb, 1, j - 5);
      if (j >= 2 && j < 6) a1[j - 2] = lda(sb, 1, j - 2);
      mma(a0, b0[j], j);
    }
#pragma unroll
    for (int j = 0; j < 6; ++j) {
      if (j < 5) b1[j + 5] = ldb(sb, 1, j + 5);
      mma(a1, b1[j], j);
    }
    so = so == 2 * SLOT ? 0 : so + SLOT;
    sb = lds + so;
    WAIT_LGKM0();
    if (st < NST1 - 1) BARRIER(); else lds_arrive(lds + M_END);
    FSTD(2 + st);
    SCHED_FENCE();
    a0[0] = lda(sb, 0, 0); b0[0] = ldb(sb, 0, 0); a0[1] = lda(sb, 0, 1); mma(a1, b1[6], 6);
    a0[2] = lda(sb, 0, 2); a0[3] = lda(sb, 0, 3); b0[1] = ldb(sb, 0, 1); mma(a1, b1[7], 7);
    b0[2] = ldb(sb, 0, 2); b0[3] = ldb(sb, 0, 3); mma(a1, b1[8], 8);
    b0[4] = ldb(sb, 0, 4); mma(a1, b1[9], 9);
  }
  WAIT_LGKM0();
}

__device__ __forceinline__ void p2_main(const Params& p, ldsp lds, int w, int rt, int ct, u64 tag, const u32x4 (&held)[15], Diag& dg) {
  constexpr int WL = 1, LAG = 3, AW = P2_AW, NR = 17, RH = 12, GATE = 10;
  const int l = threadIdx.x & 63, l15 = l & 15, kg = l >> 4, q = l15 >> 2;
  const int sg = w & 1, kq = w >> 1, hf = w >> 2;
  const int rlo = rt * 96, rhi = min(rlo + 95, S - 1);
  const rsrc_t ra = mk_rsrc(p.ws, A2_BYTES);
  const unsigned av = (unsigned)(((rt * 6 + sg * 3) * KB_ALL + kq) * 1024 + l * 16);
  const int rh = 4 * kg + q, ro = 16 * (kg >> 1) + 4 * (kg & 1) + q;
  const int wh = rh * 128 + (((rh >> 1) & 3) << 5) + (l & 3) * 8;
  const int wo = ro * 128 + (((ro >> 1) & 3) << 5) + (l & 3) * 8;
  ldsp hdst = lds + ((w & 3) * 1024 + hf * 4096 + l * 16);
  f32x4 acc[4][3];
#pragma unroll
  for (int n = 0; n < 4; ++n)
#pragma unroll
    for (int s = 0; s < 3; ++s) acc[n][s] = f32x4{};
  u32x4 a[NR][3];
  bf16x8 wf[WL + 1][4];
  float gi[3];
  auto lda = [&](int t) {
#pragma unroll
    for (int s = 0; s < 3; ++s) {
      a[t][s] = __builtin_amdgcn_raw_buffer_load_b128(ra, av + s * KB_ALL * 1024, t * 4096, A2AUX);
    }
  };
  ldsp wb[4];
  auto ldw = [&](int t) {
    const int o0 = w2_off(4 * t), r0 = o0 & ~0x7FFF;
    auto straight = [](int u) { return w2_off(4 * u + 3) == w2_off(4 * u) + 3 * 4096; };
    if (t == 0 || t == RH || r0 != (w2_off(4 * t - 4) & ~0x7FFF) || !straight(t) || !straight(t - 1)) {
      int rel = kq * 4096;
      if (!straight(t)) rel = kq == 0 ? 0 : kq == 1 ? w2_off(4 * t + 1) - o0 : kq == 2 ? w2_off(4 * t + 2) - o0 : w2_off(4 * t + 3) - o0;
#pragma unroll
      for (int n = 0; n < 4; ++n) {
        wb[n] = lds + (((t < RH ? wh : wo) ^ ((n ^ kq) << 5)) + r0 + rel);
        asm volatile("" : "+v"(wb[n]));
      }
    }
#pragma unroll
    for (int n = 0; n < 4; ++n) wf[t % (WL + 1)][n] = lds_tr2(wb[n] + (o0 - r0), wb[n] + (o0 - r0) + (t < RH ? 2048 : 1024));
  };
  if (w == 4) poll(flag_ptr(p, (l & 32) ? FL_G + (((l & 1) ? rhi : rlo) >> 9) : FL_H + ((l & 16) ? rhi : rlo) / 160 * 12 + min(l15, 11)), tag);
  const unsigned cnt = (unsigned)(size_t)(lds + M_RND);
  auto arrive = [&](int r) {
    asm volatile("s_mov_b64 exec, 1\n\tds_add_u32 %0, %1 offset:%2\n\ts_mov_b64 exec, -1" ::"v"(cnt), "v"(1u), "i"(4 * r) : "memory");
  };
  auto counted = [](int r) { return (r >= 0 && r < GATE - LAG) || r == GATE || r == RH - 1; };
  unsigned peek = 0;
  auto check = [&](int r) {
    unsigned sv, n;
    asm volatile(
        "s_nop 0\n\tv_readfirstlane_b32 %0, %2\n\ts_cmp_ge_u32 %0, 8\n\ts_cbranch_scc1 .Lok%=\n\ts_mov_b32 %1, 0x100000\n"
        ".Lagain%=:\n\tds_read_b32 %2, %3 offset:%4\n\ts_waitcnt lgkmcnt(0)\n\tv_readfirstlane_b32 %0, %2\n\ts_sub_u32 %1, %1, 1\n\t"
        "s_cbranch_scc1 .Lok%=\n\ts_cmp_ge_u32 %0, 8\n\ts_cbranch_scc0 .Lagain%=\n.Lok%=:"
        : "=&s"(sv), "=&s"(n), "+v"(peek) : "v"(cnt), "i"(4 * r) : "memory", "scc");
  };
  auto prio = [](int v) {
    if (v >= 3) __builtin_amdgcn_s_setprio(3); else if (v == 2) __builtin_amdgcn_s_setprio(2);
    else if (v == 1) __builtin_amdgcn_s_setprio(1); else __builtin_amdgcn_s_setprio(0);
  };
  auto upto = [](int t) {
    if (t < GATE) return min(min(t + 1 + AW, P2_A0 + P2_RAMP * (t + 1)), RH);
    return min(RH + P2_G0 + P2_G1 * (t - GATE), NR);
  };
  BARRIER();
  DT(dg.t3);
  STAMP(4);
  {
    const rsrc_t rg = mk_rsrc(p.ws + G_OFF, G_BYTES);
#pragma unroll
    for (int t = 0; t < upto(-1); ++t) {
      prio(3 - t); lda(t);
      if (t == 0) {
        ldw(0);
#pragma unroll
        for (int s = 0; s < 3; ++s) gi[s] = __builtin_bit_cast(float, __builtin_amdgcn_raw_buffer_load_b32(rg, (rt * 96 + sg * 48 + s * 16 + l15) * 4, 0, SC1));
      }
      SCHED_FENCE();
    }
  }
#pragma unroll
  for (int t = 0; t < NR; ++t) {
    if (t == GATE) {
      if (w == 7) poll(flag_ptr(p, FL_O + (((l & 4) ? rhi : rlo) >> 8) * 4 + (l & 3)), tag);
      WAIT_LGKM0();
      FST(16);
      BARRIER();
      DT(dg.t4);
      SCHED_FENCE();
    }
    if (counted(t - LAG)) check(t - LAG);
    if (counted(t + 1 - LAG)) peek = __hip_atomic_load((LAS unsigned*)(lds + M_RND + 4 * (t + 1 - LAG)), __ATOMIC_RELAXED, __HIP_MEMORY_SCOPE_WORKGROUP);
    SCHED_FENCE();
    prio(3 - (t & 3));
#pragma unroll
    for (int u = upto(t - 1); u < upto(t); ++u) lda(u);
    if (t + 1 < NR) ldw(t + 1);
    if (t >= LAG) {
#pragma unroll
      for (int i = 2 * (t - LAG); i < min(2 * (t - LAG) + 2, 15); ++i) {
        *(LAS u32x4*)(hdst + w2_off(39 + 2 * i)) = held[i];
      }
    }
    if (t == RH) {
#pragma unroll
      for (int n = 0; n < 4; ++n)
#pragma unroll
        for (int s = 0; s < 3; ++s) acc[n][s] *= 1.f - gi[s];
    }
#pragma unroll
    for (int s = 0; s < 3; ++s)
#pragma unroll
      for (int n = 0; n < 4; ++n) {
        acc[n][s] = mfma16(wf[t % (WL + 1)][n], __builtin_bit_cast(bf16x8, a[t][s]), acc[n][s]);
      }
    SG_VM(9);
    SG_MFMA(1); SG_DS(2); SG_MFMA(1); SG_DS(2); SG_MFMA(1); SG_DS(2); SG_MFMA(1); SG_DS(2);
    SG_MFMA(2); SG_DW(1); SG_MFMA(2); SG_DW(1); SG_MFMA(2); SG_DW(1); SG_MFMA(1); SG_DW(1); SG_MFMA(1);
    asm volatile("" : "+v"(acc[0][0]), "+v"(acc[1][0]), "+v"(acc[2][0]), "+v"(acc[3][0]), "+v"(acc[0][1]), "+v"(acc[1][1]), "+v"(acc[2][1]), "+v"(acc[3][1]),
                 "+v"(acc[0][2]), "+v"(acc[1][2]), "+v"(acc[2][2]), "+v"(acc[3][2]));
    if (counted(t)) arrive(t);
    SCHED_FENCE();
  }
  STAMP(5);
  constexpr int XB = 16384;
  static_assert(w2_off(30) == XB && w2_off(39) == XB + 9 * 4096, "ring layout");
  {
    ldsp wp = lds + (XB + w * 9 * 1024 + l * 16);
#pragma unroll
    for (int d = 1; d < 4; ++d)
#pragma unroll
      for (int s = 0; s < 3; ++s) {
        *(LAS f32x4*)(wp + ((d - 1) * 3 + s) * 1024) = acc[d][s];
      }
  }
  const rsrc_t rout = mk_rsrc(p.out, S * D * 2);
  unsigned vo = (unsigned)((rt * 96 + sg * 48 + l15) * 1280 + (ct * 64 + kq * 16 + 4 * kg) * 2);
  asm volatile("" : "+v"(vo));
  WAIT_LGKM0();
  BARRIER();
  f32x4 part[3][4];
#pragma unroll
  for (int s = 0; s < 3; ++s)
#pragma unroll
    for (int d = 1; d < 4; ++d) part[s][d] = *(LAS f32x4*)(lds + XB + ((w ^ (2 * d)) * 9 + (d - 1) * 3 + s) * 1024 + l * 16);
  SCHED_FENCE();
#pragma unroll
  for (int s = 0; s < 3; ++s) {
    const f32x4 v = (acc[0][s] + part[s][1]) + (part[s][2] + part[s][3]);
    __builtin_amdgcn_raw_buffer_store_b64(u32x2{pk2(v[0], v[1]), pk2(v[2], v[3])}, rout, vo + s * 16 * 1280, 0, OUT_AUX);
  }
  {
    u64 te; DT(te);
    if (te - dg.t0 > DIAG_THR && w == 0 && l == 0) {
      const u64 d = DIAG == 0 ? te - dg.t0 : DIAG == 1 ? dg.t1 - dg.t0 : DIAG == 2 ? dg.t2 - dg.t0 : DIAG == 3 ? dg.t3 - dg.t0 : dg.t4 - dg.t0;
      WAIT_VM(0);
      __builtin_amdgcn_raw_buffer_store_b64(u32x2{pk2((float)(d / 25), 0.f), 0u}, rout, vo, 0, 0);
    }
  }
}

__global__ __launch_bounds__(512, 2) void fused(Params p) {
  extern __shared__ __attribute__((aligned(16))) char smem[];
  ldsp lds = (ldsp)smem;
  Diag dg;
  DT(dg.t0); dg.t1 = dg.t2 = dg.t3 = dg.t4 = dg.t0;
  u64 pc;
  asm volatile("s_getpc_b64 %0" : "=s"(pc));
  constexpr unsigned rest = CODE_BYTES;
  const int b = blockIdx.x, xcd = b & 7, idx = b >> 3, cg = xcd & 3, rg = xcd >> 2;
  const int w = __builtin_amdgcn_readfirstlane(threadIdx.x >> 6), lw = w - 4;
  const int l = threadIdx.x & 63;
  FINIT();
  STAMP(0);
  const int tm = (11 - idx) >> 31;
  const int jj = idx + ((20 * rg - 12) & tm), j3 = (jj * 43) >> 7, jr = jj - 3 * j3;
  const bool spare = jj == 39;
  const int ty = tm ? T_GLU : (jr == 0 ? T_Q : T_KV);
  const int rt = j3 + (4 * rg & ~tm);
  const int ctn = 2 * cg + jr - 1 + ((cg + 1) & tm);
  const int t2 = 28 * xcd - ((xcd - 4) & -rg) + ((idx - 4) & tm) + ((2 * j3 + jr - 1) & ~tm);
  const int rt2 = (t2 * 205) >> 11, ct2 = t2 - 10 * rt2;
  const int rot0 = ROT;
  auto get_tag = [&]() {
    u64 e;
    asm volatile("s_load_dwordx2 %0, %1, 0x0\n\ts_waitcnt lgkmcnt(0)" : "=s"(e) : "s"(p.ws + EPOCH_OFF) : "memory");
    return make_tag(e);
  };

  constexpr int PQX = 2 * SLOT;
  const bool loader = w >= 4 && !spare;
  Warm wm(lds);
  u64 tag;
  auto mfma_start = [&]() {
    if (idx < 2) {
      const unsigned o = (idx * 256 + w * 64 + l) * 128;
      wm.touch((const GAS char*)pc + (o < rest ? o : 0u));
    }
    if (threadIdx.x < 32) *(LAS unsigned*)(lds + M_CNT + threadIdx.x * 4) = 0u;
    tag = get_tag();
  };
  const W2Src src(p, w & 3, ct2);
  if (__builtin_expect(ty == T_Q, 0)) {
    const int ct = cg;
    bf16x8 pq[10];
    if (loader) {
      u32x4 none[30];
      p1_loader<true>(p, lds, lw, 0, rt, ct, rot0, src, none, [&]() {
        {
          const unsigned o = ((j3 * 4 + lw) * 24 + min(l, 23)) * 128;
          wm.touch((const GAS char*)pc + (o < rest ? o : 0u));
        }
        tag = get_tag();
        for (int i = lw * 64 + l; i < 2 * KVT_SZ / 16; i += 256)
          asm volatile("ds_write_b128 %0, %1" ::"v"((unsigned)(size_t)(lds + KVT) + i * 16), "v"(u32x4{0u, 0u, 0u, 0u}) : "memory");
        if (lw == 0) poll(flag_ptr(p, FL_KV + (l & 7) * 8 + 2 * ct + ((l >> 3) & 1)), tag);
        WAIT_LGKM0();
      });
      poll(flag_ptr(p, FL_G + (rt >> 1)), tag);
      const int tl = lw * 64 + l;
      *(LAS unsigned*)(lds + M_G + tl * 4) = __builtin_amdgcn_raw_buffer_load_b32(mk_rsrc(p.ws + G_OFF, G_BYTES), (rt * 256 + tl) * 4, 0, SC1);
      lds_wait(lds + M_END, 4);
#pragma unroll
      for (int t = 0; t < 10; ++t) { pq[t] = lds_b128(lds + PQX + (lw * 10 + t) * 1024 + l * 16); asm volatile("" : "+v"(pq[t])); }
    } else {
    mfma_start();
    f32x16 acc[5][2];
#pragma unroll
    for (int n = 0; n < 5; ++n) { acc[n][0] = f32x16{}; acc[n][1] = f32x16{}; }
    p1_main_q(lds, w, acc);
    STAMP(1);
    const Consts cn(1.f);
#pragma unroll
    for (int k = 9; k >= 0; --k)
#pragma unroll
      for (int s = 0; s < 2; ++s) {
        const f32x16& c = acc[k >> 1][s];
        const int o = 8 * (k & 1);
        const bf16x8 f = pack44(phi4(f32x4{c[o], c[o + 1], c[o + 2], c[o + 3]}, cn), phi4(f32x4{c[o + 4], c[o + 5], c[o + 6], c[o + 7]}, cn));
        if (k >= 5) *(LAS bf16x8*)(lds + PQX + (w * 10 + 5 * s + k - 5) * 1024 + l * 16) = f;
        else { pq[5 * s + k] = f; asm volatile("" : "+v"(pq[5 * s + k])); }
        if (k == 5 && s == 1) { WAIT_LGKM0(); lds_arrive(lds + M_END); }
      }
    STAMP(2);
    }
    {
      const int ch = min((int)threadIdx.x, 419), hg = ch >= 210;
      ldsp dp[2];
      bool dup[2];
#pragma unroll
      for (int u = 0; u < 2; ++u) {
        const int un = 2 * (ch - 210 * hg) + u, row = un / 5, dq = un % 5, hd = row < 80 ? row / 20 : row - 80, run = 5 * hd + dq;
        dp[u] = lds + KVT + hg * KVT_SZ + row * KS + (run >> 2) * 32 + (run & 1) * 16 + (run & 2) * 4;
        dup[u] = row >= 80;
      }
      const rsrc_t rk = mk_rsrc(p.ws + KVP_OFF, KVP_BYTES);
      u32x4 pv[8];
      unsigned todo = 0xFFu;
#pragma unroll 1
      for (int spin = 0; spin < SPIN_MAX && todo; ++spin) {
#pragma unroll
        for (int r = 0; r < 8; ++r)
          if (todo >> r & 1) pv[r] = __builtin_amdgcn_raw_buffer_load_b128(rk, 2 * ct * 3360 + ch * 16, r * 8 * 3360, SC1);
#pragma unroll
        for (int r = 0; r < 8; ++r)
          if (__all(max(max(pv[r][0], pv[r][1]), max(pv[r][2], pv[r][3])) != UNSET)) todo &= ~(1u << r);
        asm volatile("s_sleep 1" ::: "memory");
      }
      STAMP(3);
      f32x2 sum[4];
#pragma unroll
      for (int e = 0; e < 4; ++e) sum[e] = f32x2{0.f, 0.f};
#pragma unroll
      for (int r = 0; r < 8; ++r)
#pragma unroll
        for (int e = 0; e < 4; ++e) {
          const unsigned x = pv[r][e];
          sum[e] += f32x2{__uint_as_float(x << 16), __uint_as_float(x & 0xFFFF0000u)};
        }
#pragma unroll
      for (int u = 0; u < 2; ++u) {
        const u32x2 v = {pk2(sum[2 * u][0], sum[2 * u][1]), pk2(sum[2 * u + 1][0], sum[2 * u + 1][1])};
        *(LAS u32x2*)dp[u] = v;
        if (dup[u]) *(LAS u32x2*)(dp[u] + 4 * KS) = v;
      }
      WAIT_LGKM0();
      BARRIER();
      STAMP(4);
    }
  {
    const int l31 = l & 31, h = l >> 5, hg = w >> 2, wr = w & 3;
    const rsrc_t ra = mk_rsrc(p.ws, A2_BYTES);
    const unsigned vo = (unsigned)(((rt * 16 + wr * 4 + ((l >> 4) & 1)) * KB_ALL + 48 + 5 * ct) * 1024 + hg * 2560 + h * 256 + (l & 15) * 16);
    bf16x8 af[3][5];
#pragma unroll
    for (int et = 2; et >= 0; --et)
#pragma unroll
      for (int t = 0; t < 5; ++t)
        if (!((et == 0 && t > 2) || (et == 1 && t == 0))) af[et][t] = lds_b128(lds + (KVT + hg * KVT_SZ + l31 * KS + h * 16) + 32 * et * KS + t * 32);
    float gv[2];
#pragma unroll
    for (int s = 0; s < 2; ++s) gv[s] = *(LAS float*)(lds + M_G + (64 * wr + 32 * s + l31) * 4);
    FST(18);
    SCHED_FENCE();
    f32x16 acc[2][3];
#pragma unroll
    for (int s = 0; s < 2; ++s)
#pragma unroll
      for (int et = 2; et >= 0; --et) {
        acc[s][et] = f32x16{};
#pragma unroll
        for (int t = 0; t < 5; ++t)
          if (!((et == 0 && t > 2) || (et == 1 && t == 0))) acc[s][et] = mfma32(af[et][t], pq[5 * s + t], acc[s][et]);
      }
#pragma unroll
    for (int s = 0; s < 2; ++s) {
      f32x4 sc;
#pragma unroll
      for (int hd = 0; hd < 4; ++hd) sc[hd] = gv[s] * __builtin_amdgcn_rcpf(acc[s][2][8 + hd] + 1e-6f);
#pragma unroll
      for (int pc_ = 4; pc_ >= 0; --pc_) {
        f32x4 t[2];
#pragma unroll
        for (int gq = 0; gq < 2; ++gq) {
          const int e0 = 16 * pc_ + 8 * gq, o = 8 * (pc_ & 1) + 4 * gq;
          float fa = sc[e0 / 20], fb = sc[(e0 + 4) / 20];
          if (e0 / 20 != (e0 + 4) / 20) { asm volatile("" : "+v"(fa), "+v"(fb)); fa = h ? fb : fa; }
          const f32x16& c = acc[s][pc_ >> 1];
          const f32x2 lo = f32x2{c[o], c[o + 1]} * f32x2{fa, fa}, hi = f32x2{c[o + 2], c[o + 3]} * f32x2{fa, fa};
          t[gq] = f32x4{lo[0], lo[1], hi[0], hi[1]};
        }
        st128_sc1(ra, vo + s * 2 * KB_ALL * 1024 + pc_ * 512, __builtin_bit_cast(u32x4, pack44(t[0], t[1])));
      }
    }
    STAMP(5);
    publish(p, lds, FL_O + rt * 4 + ct, tag, 8);
    STAMP(6);
    FDUMP();
    return;
  }
  }
  const int ct = ctn;
  u32x4 held[15];
  if (__builtin_expect(loader, 1)) {
    u32x4 pre[30];
    p1_loader<false>(p, lds, lw, tm, rt, ct, rot0, src, pre, [&]() { tag = get_tag(); });
    src.fill<0, 24>(lds + lw * 1024);
    if (ty == T_KV) {
#pragma unroll
      for (int i = 0; i < 30; ++i) pre[i] = pre_load(src, i);
      SCHED_FENCE();
      BARRIER(); BARRIER();
    } else {
      lds_wait(lds + M_END, 4);
    }
    WAIT_VM(0);
#pragma unroll
    for (int i = 0; i < 15; ++i) *(LAS u32x4*)(lds + (lw * 1024 + l * 16) + w2_off(24 + i)) = pre[i];
#pragma unroll
    for (int i = 0; i < 15; ++i) held[i] = pre[15 + i];
    WAIT_LGKM0();
  } else {
    mfma_start();
    if (ty == T_KV && w == 0) {
      const rsrc_t rk = mk_rsrc(p.ws + KVP_OFF + (rt * 8 + ct) * 3360, 3360);
#pragma unroll
      for (int i = 0; i < 4; ++i) st128_sc1(rk, i * 1024 + l * 16, u32x4{UNSET, UNSET, UNSET, UNSET});
      WAIT_VM(0);
      if (l == 0) __hip_atomic_store(flag_ptr(p, FL_KV + rt * 8 + ct), tag, __ATOMIC_RELAXED, __HIP_MEMORY_SCOPE_AGENT);
    }
    if (spare) {
      BARRIER();
      const int l15 = l & 15, r0 = cg * 512 + w * 64 + (l >> 4);
      const rsrc_t rx = mk_rsrc(p.x, S * D * 2), rgw = mk_rsrc(p.gw, D * 2), rg_ = mk_rsrc(p.ws + G_OFF, G_BYTES);
      u32x4 gw[5], v[2][4][5];
#pragma unroll
      for (int c = 0; c < 5; ++c) gw[c] = __builtin_amdgcn_raw_buffer_load_b128(rgw, l15 * 16 + c * 256, 0, 0);
      auto ld = [&](int it) {
#pragma unroll
        for (int u = 0; u < 4; ++u)
#pragma unroll
          for (int c = 0; c < 5; ++c) v[it & 1][u][c] = __builtin_amdgcn_raw_buffer_load_b128(rx, (r0 + 4 * u) * 1280 + l15 * 16 + c * 256, it * 16 * 1280, 0);
      };
      ld(0);
#pragma unroll
      for (int it = 0; it < 4; ++it) {
        if (it < 3) ld(it + 1);
        SCHED_FENCE();
        float keep = 0.f;
#pragma unroll
        for (int u = 0; u < 4; ++u) {
          float sacc = 0.f;
#pragma unroll
          for (int c = 0; c < 5; ++c)
#pragma unroll
            for (int e = 0; e < 4; ++e) {
              const unsigned xw = v[it & 1][u][c][e], ge = gw[c][e];
              sacc = __builtin_amdgcn_fdot2_f32_bf16(__builtin_bit_cast(bf16x2, xw), __builtin_bit_cast(bf16x2, ge), sacc, false);
            }
          sacc += __shfl_xor(sacc, 1); sacc += __shfl_xor(sacc, 2); sacc += __shfl_xor(sacc, 4); sacc += __shfl_xor(sacc, 8);
          if (l15 == u) keep = sacc;
        }
        if (l15 < 4) __builtin_amdgcn_raw_buffer_store_b32(__builtin_bit_cast(unsigned, sigmoidf(keep)), rg_, (r0 + 16 * it + 4 * l15) * 4, 0, SC1);
        SCHED_FENCE();
      }
      publish(p, lds, FL_G + cg, tag, 8);
      STAMP(6);
      FDUMP();
      if (cg == 0 && w == 0) {
#pragma unroll 1
        for (int i = 0; i < 4; ++i) poll(flag_ptr(p, i * 64 + l), tag);
        if (l == 0) __hip_atomic_store((GAS u64*)(p.ws + EPOCH_OFF), tag, __ATOMIC_RELAXED, __HIP_MEMORY_SCOPE_AGENT);
      }
      return;
    }
      f32x4 acc[4][10];
#pragma unroll
      for (int i = 0; i < 4; ++i)
#pragma unroll
        for (int j = 0; j < 10; ++j) acc[i][j] = f32x4{};
      if (ty == T_GLU) {
        p1_main16<T_GLU>(lds, w, acc, dg);
        DT(dg.t2);
        STAMP(1);
        const Consts cn(-1.f);
        const rsrc_t ra = mk_rsrc(p.ws, A2_BYTES);
        const unsigned vo = (unsigned)((rt * 10 * KB_ALL + ct * 4 + w) * 1024 + l * 16);
#pragma unroll
        for (int j = 0; j < 10; ++j) {
          st128_sc1(ra, vo + j * KB_ALL * 1024, __builtin_bit_cast(u32x4, pack44(glu4(acc[0][j], acc[2][j], cn), glu4(acc[1][j], acc[3][j], cn))));
          SCHED_FENCE();
        }
        STAMP(2);
        publish(p, lds, FL_H + rt * 12 + ct, tag, 4);
      } else {
        int goff[2];
#pragma unroll
        for (int i = 0; i < 2; ++i) {
          const int en = min((int)threadIdx.x + 256 * i, 419), row = en / 5, dq = en % 5;
          const int hd = row < 80 ? row / 20 : row - 80, d0 = 20 * hd + 4 * dq, ti = d0 >> 4, tj = row >> 4;
          const int tile = row >= 80 ? 9 : ti == tj ? ti : (ti == 0 || ti == 4) ? 8 : 4 + ti;
          goff[i] = tile * 1024 + (((d0 & 15) >> 2) * 16 + (row < 80 ? row & 15 : ti)) * 16;
          asm volatile("" : "+v"(goff[i]));
        }
        p1_main16<T_KV>(lds, w, acc, dg);
        DT(dg.t2);
        STAMP(1);
        const Consts cn(1.f);
        const int l15 = l & 15, g = l >> 4;
        ldsp tp = lds + (w * 10240 + l * 16);
        bf16x8 vf[2][5];
#pragma unroll
        for (int u = 0; u < 2; ++u)
#pragma unroll
          for (int j = 0; j < 5; ++j) vf[u][j] = pack44(acc[2 * u][5 + j], acc[2 * u + 1][5 + j]);
        f32x4 z = {}, dg[5], lo[5], hi[5];
        SCHED_FENCE();
#pragma unroll
        for (int i = 0; i < 5; ++i) {
          const unsigned o1 = l15 == i ? 0x3F803F80u : 0u;
          const bf16x8 one = __builtin_bit_cast(bf16x8, u32x4{o1, o1, o1, o1});
          dg[i] = f32x4{}; lo[i] = f32x4{}; hi[i] = f32x4{};
#pragma unroll
          for (int u = 0; u < 2; ++u) {
            const bf16x8 kf = pack44(phi4(acc[2 * u][i], cn), phi4(acc[2 * u + 1][i], cn));
            if (i > 0) lo[i] = mfma16(kf, vf[u][i - 1], lo[i]);
            if (i < 4) hi[i] = mfma16(kf, vf[u][i + 1], hi[i]);
            dg[i] = mfma16(kf, vf[u][i], dg[i]);
            z = mfma16(kf, one, z);
            if (i > 0) {
              if (u == 0) *(LAS f32x4*)(tp + (i - 1) * 1024) = dg[i - 1];
              else if (i > 1) *(LAS f32x4*)(tp + (3 + i) * 1024) = g < i - 1 ? lo[i - 1] : hi[i - 1];
            }
            SCHED_FENCE();
          }
          if (i == 0) { FST(18); lds_wait(lds + M_END, 4); }
        }
        FST(19);
        *(LAS f32x4*)(tp + 8 * 1024) = l15 < 8 ? hi[0] : lo[4];
        *(LAS f32x4*)(tp + 4 * 1024) = dg[4];
        *(LAS f32x4*)(tp + 9 * 1024) = z;
        WAIT_LGKM0();
        FST(20);
        BARRIER();
        FST(21);
        const rsrc_t rk = mk_rsrc(p.ws + KVP_OFF + (rt * 8 + ct) * 3360, 3360);
#pragma unroll
        for (int i = 0; i < 2; ++i) {
          f32x4 sum = *(LAS f32x4*)(lds + goff[i]);
#pragma unroll
          for (int ww = 1; ww < 4; ++ww) sum += *(LAS f32x4*)(lds + goff[i] + ww * 10240);
          __builtin_amdgcn_raw_buffer_store_b64(u32x2{min(pk2(sum[0], sum[1]), UNSET - 1), min(pk2(sum[2], sum[3]), UNSET - 1)}, rk, threadIdx.x * 8 + i * 2048, 0, SC1);
        }
        WAIT_LGKM0();
        FST(22);
        BARRIER();
        STAMP(2);
      }
      STAMP(3);
#pragma unroll
      for (int i = 0; i < 15; ++i) held[i] = src.load(39 + 2 * i, 0);
      SCHED_FENCE();
  }
  p2_main(p, lds, w, rt2, ct2, tag, held, dg);
  STAMP(6);
  FDUMP();
}

#define STAMP_PTR nullptr
extern "C" void kernel_launch(void* const* d_in, const int* in_sizes, int n_in, void* d_out, int out_size, void* d_ws, size_t ws_size,
                              hipStream_t stream) {
  Params p;
  p.x = (const u16*)d_in[0]; p.wqkv = (const u16*)d_in[1]; p.wo = (const u16*)d_in[2];
  p.wg = (const u16*)d_in[4]; p.wu = (const u16*)d_in[5]; p.wd = (const u16*)d_in[6]; p.gw = (const u16*)d_in[7];
  p.out = (u16*)d_out; p.ws = (char*)d_ws; p.stamps = STAMP_PTR;
  (void)hipFuncSetAttribute((const void*)fused, hipFuncAttributeMaxDynamicSharedMemorySize, LDS_BYTES);
  hipLaunchKernelGGL(fused, dim3(256), dim3(512), LDS_BYTES, stream, p);
}
```

```cpp
#include <hip/hip_runtime.h>
#include <hip/hip_bf16.h>
#include <utility>

typedef unsigned short u16;
typedef unsigned long long u64;
typedef __bf16 bf16x8 __attribute__((ext_vector_type(8)));
typedef __bf16 bf16x4 __attribute__((ext_vector_type(4)));
typedef __bf16 bf16x2 __attribute__((ext_vector_type(2)));
typedef float f32x16 __attribute__((ext_vector_type(16)));
typedef float f32x4 __attribute__((ext_vector_type(4)));
typedef unsigned u32x4 __attribute__((ext_vector_type(4)));
typedef unsigned u32x2 __attribute__((ext_vector_type(2)));
#define LAS __attribute__((address_space(3)))
#define GAS __attribute__((address_space(1)))
typedef LAS char* ldsp;
typedef __amdgpu_buffer_rsrc_t rsrc_t;

constexpr int S = 2048, D = 640, F = 1536;
constexpr float LOG2E = 1.4426950408889634f;
constexpr int KB_ALL = 68;
constexpr unsigned A2_BYTES = 128u * KB_ALL * 1024;
constexpr unsigned KVP_OFF = A2_BYTES, KVP_SZ = 3456, KVP_BYTES = 64 * KVP_SZ;
constexpr unsigned G_OFF = KVP_OFF + KVP_BYTES, G_BYTES = S * 4;
constexpr unsigned FLAG_OFF = G_OFF + G_BYTES;
constexpr int NFLAG = 284;
constexpr unsigned EPOCH_OFF = FLAG_OFF + NFLAG * 128;
constexpr int FL_H = 0, FL_KV = 156, FL_O = 220, FL_G = 252;
constexpr int SC1 = 16;
constexpr unsigned UNSET = 0xFFFFFFFFu;
constexpr int SLOT = 53248, MISC = 3 * SLOT, LDS_BYTES = 163840;
constexpr int M_DUMMY = MISC;
constexpr int M_CNT = MISC + 256, M_END = MISC + 260;
constexpr int M_RND = MISC + 320, M_TAG = MISC + 384;
constexpr int M_GW = MISC + 512, M_G = MISC + 2048;
constexpr int KS = 80, KVT = SLOT, KVT_SZ = 4 * 32 * KS;
constexpr int NST1 = 10;
constexpr int SPIN_MAX = 1 << 16;
constexpr unsigned CODE_BYTES = 44000;

struct Params {
  const u16 *x, *wqkv, *wo, *wg, *wu, *wd, *gw;
  u16* out;
  char* ws;
};

#define WAIT_VM(n) asm volatile("s_waitcnt vmcnt(" #n ")" ::: "memory")
#define WAIT_LGKM0() asm volatile("s_waitcnt lgkmcnt(0)" ::: "memory")
#define BARRIER() do { asm volatile("" ::: "memory"); __builtin_amdgcn_s_barrier(); asm volatile("" ::: "memory"); } while (0)
#define SCHED_FENCE() __builtin_amdgcn_sched_barrier(0)
#define ORDER_FENCE() do { asm volatile("" ::: "memory"); __builtin_amdgcn_sched_barrier(0); } while (0)
#define SG_MFMA(n) __builtin_amdgcn_sched_group_barrier(0x8, n, 0)
#define SG_DS(n) __builtin_amdgcn_sched_group_barrier(0x100, n, 0)
#define SG_TR(n) __builtin_amdgcn_sched_group_barrier(0x400, n, 0)
#define SG_VALU(n) __builtin_amdgcn_sched_group_barrier(0x2, n, 0)

__device__ __forceinline__ rsrc_t mk_rsrc(const void* p, unsigned bytes) { return __builtin_amdgcn_make_buffer_rsrc((void*)p, 0, bytes, 0x00020000); }
__device__ __forceinline__ void dma16(rsrc_t r, ldsp dst, unsigned voff, unsigned soff) { __builtin_amdgcn_raw_ptr_buffer_load_lds(r, dst, 16, voff, soff, 0, 0); }
__device__ __forceinline__ bf16x8 lds_b128(ldsp a) { return *(LAS bf16x8*)a; }
__device__ __forceinline__ bf16x8 lds_tr2(ldsp a0, ldsp a1) {
  bf16x4 lo = __builtin_amdgcn_ds_read_tr16_b64_v4bf16((LAS bf16x4*)a0);
  bf16x4 hi = __builtin_amdgcn_ds_read_tr16_b64_v4bf16((LAS bf16x4*)a1);
  return __builtin_shufflevector(lo, hi, 0, 1, 2, 3, 4, 5, 6, 7);
}
__device__ __forceinline__ f32x16 mfma32(bf16x8 a, bf16x8 b, f32x16 c) { return __builtin_amdgcn_mfma_f32_32x32x16_bf16(a, b, c, 0, 0, 0); }
__device__ __forceinline__ f32x4 mfma16(bf16x8 a, bf16x8 b, f32x4 c) { return __builtin_amdgcn_mfma_f32_16x16x32_bf16(a, b, c, 0, 0, 0); }
__device__ __forceinline__ unsigned pk2(float a, float b) {
  bf16x2 v = {(__bf16)a, (__bf16)b};
  return __builtin_bit_cast(unsigned, v);
}
__device__ __forceinline__ bf16x8 pack44(const f32x4& a, const f32x4& b) {
  u32x4 r = {pk2(a[0], a[1]), pk2(a[2], a[3]), pk2(b[0], b[1]), pk2(b[2], b[3])};
  return __builtin_bit_cast(bf16x8, r);
}
__device__ __forceinline__ float sigmoidf(float t) { return __builtin_amdgcn_rcpf(1.f + __builtin_amdgcn_exp2f(-LOG2E * t)); }
typedef float f32x2 __attribute__((ext_vector_type(2)));
struct Consts {
  f32x2 l2e;
  float eps, big;
  __device__ __forceinline__ Consts(float sign) : l2e{sign * LOG2E, sign * LOG2E}, eps(1e-6f), big(3.4028234664e38f) {
    asm volatile("" : "+s"(l2e), "+s"(eps), "+v"(big));
  }
};
__device__ __forceinline__ f32x4 phi4(f32x4 t, const Consts& c) {
  const f32x2 a = f32x2{t[0], t[1]} * c.l2e, b = f32x2{t[2], t[3]} * c.l2e;
  f32x2 m0, m1, e0, e1;
#pragma unroll
  for (int i = 0; i < 2; ++i) {
    m0[i] = __builtin_amdgcn_fmed3f(t[i], c.eps, c.big);
    m1[i] = __builtin_amdgcn_fmed3f(t[2 + i], c.eps, c.big);
    e0[i] = __builtin_amdgcn_fmed3f(__builtin_amdgcn_exp2f(a[i]), 0.f, 1.f);
    e1[i] = __builtin_amdgcn_fmed3f(__builtin_amdgcn_exp2f(b[i]), 0.f, 1.f);
  }
  const f32x2 r0 = m0 + e0, r1 = m1 + e1;
  return f32x4{r0[0], r0[1], r1[0], r1[1]};
}
__device__ __forceinline__ f32x4 glu4(f32x4 g, f32x4 u, const Consts& c) {
  f32x2 a = f32x2{g[0], g[1]} * c.l2e, b = f32x2{g[2], g[3]} * c.l2e;
#pragma unroll
  for (int i = 0; i < 2; ++i) { a[i] = __builtin_amdgcn_exp2f(a[i]); b[i] = __builtin_amdgcn_exp2f(b[i]); }
  a += 1.f; b += 1.f;
#pragma unroll
  for (int i = 0; i < 2; ++i) { a[i] = __builtin_amdgcn_rcpf(a[i]); b[i] = __builtin_amdgcn_rcpf(b[i]); }
  a *= f32x2{u[0], u[1]}; b *= f32x2{u[2], u[3]};
  return f32x4{a[0], a[1], b[0], b[1]};
}
__device__ __forceinline__ void st128_sc1(rsrc_t r, unsigned off, u32x4 v) { __builtin_amdgcn_raw_buffer_store_b128(v, r, off, 0, SC1); }

__device__ __forceinline__ u64 make_tag(u64 e) { return ((e ^ 0x9E3779B97F4A7C15ull) * 0xBF58476D1CE4E5B9ull) | 1ull; }
__device__ __forceinline__ GAS u64* flag_ptr(const Params& p, int f) { return (GAS u64*)(p.ws + FLAG_OFF + (size_t)f * 128); }
__device__ __forceinline__ u64 ld_flag(GAS u64* f) { return __hip_atomic_load(f, __ATOMIC_RELAXED, __HIP_MEMORY_SCOPE_AGENT); }
__device__ __forceinline__ void poll(GAS u64* f, u64 tag) {
#pragma unroll 1
  for (int spin = 0; spin < SPIN_MAX; ++spin) {
    if (__all(ld_flag(f) == tag)) break;
    __builtin_amdgcn_s_sleep(1);
  }
  __builtin_amdgcn_fence(__ATOMIC_ACQUIRE, "wavefront");
}
__device__ __forceinline__ void lds_arrive(ldsp c) {
  asm volatile("s_mov_b64 exec, 1\n\tds_add_u32 %0, %1\n\ts_mov_b64 exec, -1" ::"v"((unsigned)(size_t)c), "v"(1u) : "memory");
}
__device__ __forceinline__ void lds_wait(ldsp c, unsigned n) {
#pragma unroll 1
  for (int spin = 0; spin < SPIN_MAX; ++spin) {
    unsigned v;
    asm volatile("ds_read_b32 %0, %1\n\ts_waitcnt lgkmcnt(0)" : "=v"(v) : "v"((unsigned)(size_t)c) : "memory");
    if (__builtin_amdgcn_readfirstlane(v) >= n) break;
    __builtin_amdgcn_s_sleep(1);
  }
}
template <int N, int OFF> __device__ __forceinline__ void lds_check(unsigned& seen, unsigned c) {
  unsigned sv, n;
  asm volatile(
      "s_nop 0\n\tv_readfirstlane_b32 %0, %2\n\ts_cmp_ge_u32 %0, %5\n\ts_cbranch_scc1 .Lok%=\n\ts_mov_b32 %1, 0x100000\n"
      ".Lagain%=:\n\tds_read_b32 %2, %3 offset:%4\n\ts_waitcnt lgkmcnt(0)\n\tv_readfirstlane_b32 %0, %2\n\ts_sub_u32 %1, %1, 1\n\t"
      "s_cbranch_scc1 .Lok%=\n\ts_cmp_ge_u32 %0, %5\n\ts_cbranch_scc0 .Lagain%=\n.Lok%=:"
      : "=&s"(sv), "=&s"(n), "+v"(seen) : "v"(c), "i"(OFF), "i"(N) : "memory", "scc");
}
__device__ __forceinline__ void publish(const Params& p, ldsp lds, int f, u64 tag, unsigned n) {
  WAIT_VM(0);
  if ((threadIdx.x & 63) == 0) {
    const unsigned old = __hip_atomic_fetch_add((LAS unsigned*)(lds + M_CNT), 1u, __ATOMIC_RELAXED, __HIP_MEMORY_SCOPE_WORKGROUP);
    if (old == n - 1) __hip_atomic_store(flag_ptr(p, f), tag, __ATOMIC_RELAXED, __HIP_MEMORY_SCOPE_AGENT);
  }
}

enum { T_Q = 0, T_KV = 1, T_GLU = 2 };

__device__ __forceinline__ constexpr int w2_off(int s) { return (SLOT + (s % 39) * 4096) % (3 * SLOT); }

struct W2Src {
  rsrc_t rd, ro;
  unsigned wv, wv4, ws0, wo0;
  __device__ __forceinline__ W2Src(const Params& p, int piece, int t2) {
    int l = threadIdx.x & 63;
    t2 = __builtin_amdgcn_readfirstlane(t2);
    asm volatile("" : "+s"(piece), "+s"(t2), "+v"(l));
    const int ct = t2 - 10 * ((t2 * 205) >> 11);
    rd = mk_rsrc(p.wd, F * D * 2);
    ro = mk_rsrc(p.wo, D * D * 2);
    wv = (l >> 3) * 1280 + (((l & 7) ^ ((l >> 4) << 1)) << 4);
    ws0 = (unsigned)(8 * piece) * 1280 + ct * 128;
    wo0 = (unsigned)(20 * (piece >> 1) + 8 * (piece & 1)) * 1280 + ct * 128;
    wv4 = wv + (unsigned)((l >> 5) * 36 + 80 * (piece >> 1) + 16 + 20 * (piece & 1)) * 1280 + ct * 128;
  }
  __device__ __forceinline__ u32x4 load(int s) const {
    if (s < 48) return __builtin_amdgcn_raw_buffer_load_b128(rd, wv, ws0 + (unsigned)s * 32 * 1280, 0);
    if (s < 64) return __builtin_amdgcn_raw_buffer_load_b128(ro, wv, wo0 + (unsigned)(s - 48) * 40 * 1280, 0);
    return __builtin_amdgcn_raw_buffer_load_b128(ro, wv4, (unsigned)(s - 64) * 160 * 1280, 0);
  }
  template <int S0, int S1> __device__ __forceinline__ void fill(ldsp dst) const {
#pragma unroll
    for (int s = S0; s < S1; ++s) dma16(rd, dst + w2_off(s), wv, ws0 + (unsigned)s * 32 * 1280);
  }
};

struct Warm {
  unsigned sink;
  __device__ __forceinline__ Warm(ldsp lds) : sink((unsigned)(size_t)(lds + M_DUMMY)) {}
  __device__ __forceinline__ void touch(const GAS char* t) {
    unsigned keep;
    asm volatile("s_mov_b32 %0, m0\n\ts_mov_b32 m0, %2\n\ts_nop 0\n\tglobal_load_lds_dword %1, off\n\ts_mov_b32 m0, %0" : "=&s"(keep) : "v"(t), "s"(sink) : "memory");
  }
};

__device__ __forceinline__ u32x4 pre_load(const W2Src& src, int i) { return i < 15 ? src.load(24 + i) : src.load(40 + 2 * (i - 15)); }

template <bool ISQ, class Reset, class Raise, class Gap>
__device__ __forceinline__ float p1_loader(const Params& p, ldsp lds, int lw, int tm, int rt, int ct, int k0, int t2, u32x4 (&pre)[29],
                                          Reset reset, Raise raise, Gap gap) {
  const bool tb = !ISQ && tm != 0;
  auto sel = [&](int a, int b) { return ISQ ? b : b ^ ((a ^ b) & tm); };
  auto selp = [&](const u16* a, const u16* b) { return ISQ ? b : (const u16*)((u64)b ^ (((u64)a ^ (u64)b) & (u64)(long long)tm)); };
  const int l = threadIdx.x & 63;
  const int i8 = sel(5 * lw, 8 * lw + 3);
  const unsigned xe = (l >> 3) * 1280 + (((l & 7) ^ (l >> 4)) << 4);
  const unsigned xa = (xe ^ ((i8 & 1) << 6)) + (unsigned)(sel(160, 256) * rt + 8 * i8) * 1280, xb = (xa ^ 64) + 8 * 1280;
  const unsigned xo[5] = {xa, xb, xa + 16 * 1280, xb + 16 * 1280, xa + 32 * 1280};
  const int d8 = i8 * 1024;
  const rsrc_t rx = mk_rsrc(p.x, S * D * 2);
#pragma unroll
  for (int j = 0; j < 5; ++j) dma16(rx, lds + d8 + j * 1024, xo[j], k0 * 128);
  SCHED_FENCE();
  const rsrc_t r0 = mk_rsrc(selp(p.wg, p.wqkv), sel(D * F * 2, D * 3 * D * 2));
  const rsrc_t r1 = mk_rsrc(selp(p.wu, p.wqkv), sel(D * F * 2, D * 3 * D * 2));
  const rsrc_t r2 = mk_rsrc(selp(p.wu, p.x), sel(D * F * 2, S * D * 2));
  unsigned v[8];
  {
    const int r = l >> 4;
    const unsigned g0 = (16 * lw + r) * 3072 + ct * 256 + (((l & 15) ^ (r << 1)) << 4);
    const int colA = ISQ ? 320 * ct : 1280 + 160 * ct, colB = ISQ ? 160 : 1280;
#pragma unroll
    for (int j = 0; j < 5; ++j) {
      const int c = 64 * j + l, rr = (c * 3277) >> 16;
      int cc = c - 20 * rr;
      if (!ISQ) cc ^= (rr >> 2) & 2;
      const unsigned a = (16 * lw + rr) * 3840 + colA + cc * 16 + (cc < 10 ? 0 : colB - 160);
      v[j] = sel((g0 ^ ((j & 2) << 6)) + (j & 3) * 4 * 3072, a);
    }
    v[5] = sel(v[1], xb - 4 * 8 * 1280);
    v[6] = sel(v[2], xa - 2 * 8 * 1280);
    v[7] = sel(v[3], xb - 2 * 8 * 1280);
  }
  const int d0 = sel(20480 + 4096 * lw, 32768 + 5120 * lw), d4 = d0 + sel(16384, 4096), d5 = sel(d4 + 1024, d8 - 3072);
  const unsigned wstep = sel(64 * 3072, 64 * 3840), mstep = sel(64 * 3072, 128);
  auto issue_a = [&](int st, int so) {
    ldsp sb = lds + so;
#pragma unroll
    for (int j = 0; j < 5; ++j) dma16(rx, sb + d8 + j * 1024, xo[j], st * 128);
#pragma unroll
    for (int j = 0; j < 2; ++j) dma16(r2, sb + d5 + j * 1024, v[5 + j], st * mstep);
    ORDER_FENCE();
  };
  auto issue_b = [&](int st, int so) {
    ldsp sb = lds + so;
    dma16(r2, sb + d5 + 2048, v[7], st * mstep);
#pragma unroll
    for (int j = 0; j < 4; ++j) dma16(r0, sb + d0 + j * 1024, v[j], st * wstep);
    dma16(r1, sb + d4, v[4], st * wstep);
    ORDER_FENCE();
  };
  auto rot = [&](int j) { const int t = k0 + j; return t >= NST1 ? t - NST1 : t; };
  float gs = 0.f;
  auto gate = [&](int st, int so) {
    if (!ISQ) return;
    const int row = 64 * ct + 16 * lw + (l >> 2);
    const unsigned a = (unsigned)(size_t)(lds + so) + row * 128 + (((2 * (l & 3)) ^ ((row >> 1) & 7)) << 4);
    const unsigned ga = (unsigned)(size_t)(lds + M_GW) + st * 128 + (l & 3) * 32;
    u32x4 x0, x1, g0, g1;
    asm volatile("ds_read_b128 %0, %4\n\tds_read_b128 %1, %5\n\tds_read_b128 %2, %6\n\tds_read_b128 %3, %6 offset:16\n\ts_waitcnt lgkmcnt(0)"
                 : "=&v"(x0), "=&v"(x1), "=&v"(g0), "=&v"(g1) : "v"(a), "v"(a ^ 16), "v"(ga) : "memory");
#pragma unroll
    for (int e = 0; e < 4; ++e) {
      const unsigned xa_ = x0[e], xb_ = x1[e], ge = g0[e], gf = g1[e];
      gs = __builtin_amdgcn_fdot2_f32_bf16(__builtin_bit_cast(bf16x2, xa_), __builtin_bit_cast(bf16x2, ge), gs, false);
      gs = __builtin_amdgcn_fdot2_f32_bf16(__builtin_bit_cast(bf16x2, xb_), __builtin_bit_cast(bf16x2, gf), gs, false);
    }
  };
  if (ISQ && lw < 2) dma16(mk_rsrc(p.gw, D * 2), lds + M_GW + lw * 1024, lw * 1024 + l * 16, 0);
#pragma unroll
  for (int j = 0; j < 2; ++j) dma16(r2, lds + d5 + j * 1024, v[5 + j], k0 * mstep);
  issue_b(k0, 0);
  issue_a(rot(1), SLOT);
  WAIT_VM(7);
  BARRIER();
  issue_b(rot(1), SLOT); issue_a(rot(2), 2 * SLOT);
  gate(k0, 0);
  WAIT_VM(7);
  BARRIER();
  issue_b(rot(2), 2 * SLOT); issue_a(rot(3), 0); issue_b(rot(3), 0);
  gate(rot(1), SLOT);
  WAIT_VM(13);
  auto ask = [&]() {
    const int line = min((__builtin_amdgcn_readfirstlane(t2) * 4 + lw) * 9 + min(l, 8), 7679);
    Warm(lds).touch((const GAS char*)p.wd + line * 128);
  };
  if (tb) {
    const W2Src src(p, lw, t2);
    int st = rot(3);
#pragma unroll
    for (int j = 1; j < NST1 - 3; ++j) {
      BARRIER();
      st = st == NST1 - 1 ? 0 : st + 1;
      asm volatile("" : "+s"(st));
      issue_a(st, (j % 3) * SLOT); issue_b(st, (j % 3) * SLOT);
#pragma unroll
      for (int i = 5 * (j - 1); i < 5 * j && i < 29; ++i) pre[i] = pre_load(src, i);
      ORDER_FENCE();
      if (j == 4) ask();
      if (j == 1) WAIT_VM(18); else if (j == 4 || j == 5) WAIT_VM(24); else if (j == 6) WAIT_VM(22); else WAIT_VM(23);
    }
    BARRIER();
    gap();
    WAIT_VM(4);
  } else {
    int so = SLOT;
#pragma unroll 1
    for (int j = 1; j < NST1 - 3; ++j) {
      BARRIER();
      if (!ISQ && j == 3) raise();
      issue_a(rot(j + 3), so); issue_b(rot(j + 3), so);
      so = so == 2 * SLOT ? 0 : so + SLOT;
      if (ISQ) {
        if (j < 6) reset(j - 1);
        gate(rot(j + 1), so);
        if (j == 1) WAIT_VM(17); else if (j < 6) WAIT_VM(21); else WAIT_VM(13);
      } else {
        if (j == 1) reset(0);
        if (j == 4) ask();
        if (j == 1 || j == 4 || j == 5) WAIT_VM(14); else WAIT_VM(13);
      }
    }
    BARRIER();
    if (ISQ) raise();
    gate(rot(8), 2 * SLOT);
    gap();
    WAIT_VM(0);
  }
  BARRIER();
  gate(rot(9), 0);
  return gs;
}

__device__ __forceinline__ void stage_barrier(int st) {
  asm volatile("s_waitcnt lgkmcnt(0)\n\ts_cmp_eq_u32 %0, %1\n\ts_cbranch_scc1 .Llast%=\n\ts_barrier\n.Llast%=:" ::"s"(st), "i"(NST1 - 1) : "memory", "scc");
}
struct FragQ { bf16x8 w[5], x[2]; };
__device__ __forceinline__ void p1_main_q(ldsp lds, int w, f32x16 (&acc)[5][2]) {
  const int l = threadIdx.x & 63, l31 = l & 31, h = l >> 5;
  const int xa = (64 * w + l31) * 128 + ((h ^ ((l31 >> 1) & 7)) << 4);
  const int wa = 32768 + (8 * h + ((l & 15) >> 2)) * 320 + ((l >> 4) & 1) * 32 + (l & 3) * 8;
  auto load = [&](FragQ& f, ldsp sb, int ks) {
    ldsp xp = sb + (xa ^ (ks << 5));
    ldsp wp = sb + wa + ks * 16 * 320;
    f.x[0] = lds_b128(xp);
    f.w[0] = lds_tr2(wp, wp + 1280);
    f.x[1] = lds_b128(xp + 4096);
#pragma unroll
    for (int n = 1; n < 5; ++n) f.w[n] = lds_tr2(wp + n * 64, wp + n * 64 + 1280);
  };
  auto mma = [&](const FragQ& f) {
#pragma unroll
    for (int n = 0; n < 5; ++n)
#pragma unroll
      for (int s = 0; s < 2; ++s) acc[n][s] = mfma32(f.w[n], f.x[s], acc[n][s]);
#pragma unroll
    for (int i = 0; i < 6; ++i) { SG_MFMA(1); SG_DS(2); }
    SG_MFMA(4);
    SCHED_FENCE();
  };
  BARRIER();
  FragQ f0, f1;
  int so = 0;
  load(f0, lds, 0);
  SCHED_FENCE();
#pragma unroll 1
  for (int j = 0; j < NST1; ++j) {
    ldsp sb = lds + so;
    load(f1, sb, 1); mma(f0);
    load(f0, sb, 2); mma(f1);
    load(f1, sb, 3); mma(f0);
    so = so == 2 * SLOT ? 0 : so + SLOT;
    stage_barrier(j);
    SCHED_FENCE();
    load(f0, lds + so, 0); mma(f1);
  }
  WAIT_LGKM0();
}

template <int TY>
__device__ __forceinline__ void p1_main16(ldsp lds, int w, f32x4 (&acc)[4][10]) {
  constexpr bool TB = TY == T_GLU;
  const int l = threadIdx.x & 63, l15 = l & 15, g = l >> 4, q = l15 >> 2;
  const int xa = ((TB ? 0 : 64 * w) + l15) * 128 + ((g ^ ((l15 >> 1) & 7)) << 4);
  int wa0, wa1;
  if (TB) {
    wa0 = 20480 + (8 * g + q) * 256 + (((2 * w) ^ (q + 4 * (g & 1))) << 5) + (l & 3) * 8;
    wa1 = wa0 ^ 32;
  } else {
    wa0 = 32768 + (8 * g + q) * 320 + (l & 3) * 8 + 32 * (g & 1);
    wa1 = wa0 - 64 * (g & 1);
  }
  ldsp px0 = lds + xa, px1 = lds + (xa ^ 64), pw0 = lds + wa0, pw1 = lds + wa1;
  auto ldx = [&](int u, int t) { return lds_b128((u ? px1 : px0) + t * 2048); };
  auto ldw = [&](int u, int t) {
    ldsp a = ((t & 1) ? pw1 : pw0) + (TB ? u * 8192 + (t >> 1) * 16384 : u * 10240 + t * 32);
    return lds_tr2(a, a + (TB ? 1024 : 1280));
  };
  auto lda = [&](int u, int i) { return TB ? ldw(u, i) : ldx(u, i); };
  auto ldb = [&](int u, int j) { return TB ? ldx(u, j) : ldw(u, j); };
  auto mma = [&](const bf16x8 (&a)[4], const bf16x8& b, int j) {
#pragma unroll
    for (int i = 0; i < 4; ++i) acc[i][j] = mfma16(a[i], b, acc[i][j]);
    SG_MFMA(1); SG_DS(2); SG_MFMA(1); SG_DS(2); SG_MFMA(1); SG_DS(2); SG_MFMA(1);
    SCHED_FENCE();
  };
  BARRIER();
  bf16x8 a0[4], a1[4], b0[10], b1[10];
#pragma unroll
  for (int i = 0; i < 4; ++i) a0[i] = lda(0, i);
#pragma unroll
  for (int j = 0; j < 5; ++j) b0[j] = ldb(0, j);
  SCHED_FENCE();
  int ph = 0;
#pragma unroll 1
  for (int st = 0; st < NST1; ++st) {
#pragma unroll
    for (int j = 0; j < 10; ++j) {
      if (j < 5) b0[j + 5] = ldb(0, j + 5); else b1[j - 5] = ldb(1, j - 5);
      if (j >= 2 && j < 6) a1[j - 2] = lda(1, j - 2);
      mma(a0, b0[j], j);
    }
#pragma unroll
    for (int j = 0; j < 5; ++j) {
      b1[j + 5] = ldb(1, j + 5);
      mma(a1, b1[j], j);
    }
    {
      const int dlt = ph == 2 ? -2 * SLOT : SLOT;
      ph = ph == 2 ? 0 : ph + 1;
      px0 += dlt; px1 += dlt; pw0 += dlt; pw1 += dlt;
      asm volatile("" : "+v"(px0), "+v"(px1), "+v"(pw0), "+v"(pw1));
#pragma unroll
      for (int i = 0; i < 4; ++i) acc[i][5] = mfma16(a1[i], b1[5], acc[i][5]);
      SG_MFMA(1); SG_VALU(2); SG_MFMA(1); SG_VALU(2); SG_MFMA(2);
      SCHED_FENCE();
    }
    stage_barrier(st);
    SCHED_FENCE();
    a0[0] = lda(0, 0); b0[0] = ldb(0, 0); a0[1] = lda(0, 1); mma(a1, b1[6], 6);
    a0[2] = lda(0, 2); a0[3] = lda(0, 3); b0[1] = ldb(0, 1); mma(a1, b1[7], 7);
    b0[2] = ldb(0, 2); b0[3] = ldb(0, 3); mma(a1, b1[8], 8);
    b0[4] = ldb(0, 4); mma(a1, b1[9], 9);
  }
  lds_arrive(lds + M_END);
  WAIT_LGKM0();
}

template <int... T, class Fn> __device__ __forceinline__ void rounds_(std::integer_sequence<int, T...>, Fn fn) { (fn(std::integral_constant<int, T>{}), ...); }

constexpr int NR = 17, RH = 12;
constexpr int LAG = 3;
constexpr int upto(int t) {
  const int a = t + 4, b = 2 * t + 3, m = a < b ? a : b;
  return t < RH ? (m < RH ? m : RH) : t == RH ? RH + 3 : NR;
}
constexpr bool counted(int r) { return r == 1 || r == 3 || r == 5 || r == 7; }
constexpr bool straight(int t) { return w2_off(4 * t + 3) == w2_off(4 * t) + 3 * 4096; }
constexpr int first_wrapped(int t) { return w2_off(4 * t + 1) != w2_off(4 * t) + 4096 ? 1 : w2_off(4 * t + 2) != w2_off(4 * t) + 8192 ? 2 : 3; }
constexpr int anchor(int t) {
  if (t == 0 || t == RH || !straight(t) || !straight(t - 1)) return t;
  const int a = anchor(t - 1), d = w2_off(4 * t) - w2_off(4 * a);
  return d > 0 && d <= 49152 ? a : t;
}

__device__ __forceinline__ void p2_main(const Params& p, ldsp lds, int w, int rt, int ct, u64 tag, const u32x4 (&held)[15]) {
  const int l = threadIdx.x & 63, l15 = l & 15, kg = l >> 4, q = l15 >> 2;
  const int sg = w & 1, kq = w >> 1, hf = w >> 2;
  const int rlo = rt * 96, rhi = min(rlo + 95, S - 1);
  const rsrc_t ra = mk_rsrc(p.ws, A2_BYTES);
  const unsigned av = (unsigned)(((rt * 6 + sg * 3) * KB_ALL + kq) * 1024 + l * 16);
  const int rh = 4 * kg + q, ro = 16 * (kg >> 1) + 4 * (kg & 1) + q;
  const int wh = rh * 128 + (((rh >> 1) & 3) << 5) + (l & 3) * 8;
  const int wo = ro * 128 + (((ro >> 1) & 3) << 5) + (l & 3) * 8;
  ldsp hdst = lds + ((w & 3) * 1024 + hf * 4096 + l * 16);
  ldsp hd0 = hdst + SLOT, hd1 = hdst + 2 * SLOT;
  asm volatile("" : "+v"(hdst), "+v"(hd0), "+v"(hd1));
  f32x4 acc[4][3];
#pragma unroll
  for (int n = 0; n < 4; ++n)
#pragma unroll
    for (int s = 0; s < 3; ++s) acc[n][s] = f32x4{};
#define PIN_ACC(text) \
  asm volatile(text : "+v"(acc[0][0]), "+v"(acc[1][0]), "+v"(acc[2][0]), "+v"(acc[3][0]), "+v"(acc[0][1]), "+v"(acc[1][1]), "+v"(acc[2][1]), \
               "+v"(acc[3][1]), "+v"(acc[0][2]), "+v"(acc[1][2]), "+v"(acc[2][2]), "+v"(acc[3][2]))
#define MFMA_ACC(n, s) asm volatile("v_mfma_f32_16x16x32_bf16 %0, %1, %2, %0" : "+v"(acc[n][s]) : "v"(wf[t & 1][n]), "v"(a[t][s]))
  PIN_ACC("");
  u32x4 a[NR][3];
  bf16x8 wf[2][4];
  float gi[3];
  auto lda = [&](int t) {
#pragma unroll
    for (int s = 0; s < 3; ++s) a[t][s] = __builtin_amdgcn_raw_buffer_load_b128(ra, av + s * KB_ALL * 1024, t * 4096, SC1);
  };
  ldsp wb[4];
  auto ldw = [&](auto tc) {
    constexpr int t = tc, o0 = w2_off(4 * t), r0 = w2_off(4 * anchor(t));
    if constexpr (anchor(t) == t) {
      int rel = o0 + kq * 4096;
      if constexpr (!straight(t)) rel -= kq >= first_wrapped(t) ? w2_off(4 * t) + 3 * 4096 - w2_off(4 * t + 3) : 0;
      asm volatile("" : "+s"(rel));
#pragma unroll
      for (int n = 0; n < 4; ++n) {
        wb[n] = lds + ((t < RH ? wh : wo) ^ ((n ^ kq) << 5)) + rel;
        asm volatile("" : "+v"(wb[n]));
      }
    }
#pragma unroll
    for (int n = 0; n < 4; ++n) wf[t & 1][n] = lds_tr2(wb[n] + (o0 - r0), wb[n] + (o0 - r0) + (t < RH ? 2048 : 1024));
  };
  if (w == 4)
    poll(flag_ptr(p, l < 32 ? FL_H + ((l & 16) ? rhi : rlo) / 160 * 12 + min(l15, 11)
                     : (l < 48 ? FL_G : FL_O) + (((l & 4) ? rhi : rlo) >> 8) * 4 + (l & 3)), tag);
  ldsp cntp = lds + M_RND;
  asm volatile("" : "+v"(cntp));
  const unsigned cnt = (unsigned)(size_t)cntp;
  unsigned peek = 0;
  const unsigned co = (unsigned)(((rt * 6 + min(l >> 3, 5)) * KB_ALL + 48) * 1024 + (l & 3) * 2048);
  unsigned cv = UNSET;
  constexpr int XB = 16384;
  static_assert(w2_off(30) == XB && w2_off(39) == XB + 9 * 4096, "ring layout");
  BARRIER();
  __builtin_amdgcn_s_setprio(3);
  lda(0);
  ldw(std::integral_constant<int, 0>{});
  {
    const rsrc_t rg = mk_rsrc(p.ws + G_OFF, G_BYTES);
#pragma unroll
    for (int s = 0; s < 3; ++s) gi[s] = __builtin_bit_cast(float, __builtin_amdgcn_raw_buffer_load_b32(rg, (rt * 96 + sg * 48 + s * 16 + l15) * 4, 0, SC1));
  }
  SCHED_FENCE();
  rounds_(std::make_integer_sequence<int, NR>{}, [&](auto tc) {
    constexpr int t = tc;
    if constexpr (t == RH) {
      if (w == 7 && !__all(cv != UNSET)) {
#pragma unroll 1
        for (int spin = 0; spin < SPIN_MAX; ++spin) {
          if (__all(__builtin_amdgcn_raw_buffer_load_b32(ra, co, 0, SC1) != UNSET)) break;
          asm volatile("s_sleep 1" ::: "memory");
        }
      }
      WAIT_LGKM0();
      BARRIER();
      SCHED_FENCE();
    }
    if constexpr (counted(t - LAG)) lds_check<8, 4 * (t - LAG)>(peek, cnt);
    if constexpr (counted(t + 1 - LAG))
      peek = __hip_atomic_load((LAS unsigned*)(cntp + 4 * (t + 1 - LAG)), __ATOMIC_RELAXED, __HIP_MEMORY_SCOPE_WORKGROUP);
    SCHED_FENCE();
    __builtin_amdgcn_s_setprio(3 - (t & 3));
    if constexpr (t == RH - 1) cv = __builtin_amdgcn_raw_buffer_load_b32(ra, w == 7 ? co : A2_BYTES, 0, SC1);
#pragma unroll
    for (int u = upto(t - 1); u < upto(t); ++u) lda(u);
    if constexpr (t + 1 < NR) ldw(std::integral_constant<int, t + 1>{});
    if constexpr (t >= 4 && t < RH) {
      constexpr int i = 2 * (t - 4), o0 = w2_off(39 + 2 * i), o1 = w2_off(41 + 2 * i);
      *(LAS u32x4*)(o0 >= 106496 ? hd1 + (o0 - 106496) : o0 >= SLOT ? hd0 + (o0 - SLOT) : hdst + o0) = held[i];
      if constexpr (i + 1 < 15) *(LAS u32x4*)(o1 >= 106496 ? hd1 + (o1 - 106496) : o1 >= SLOT ? hd0 + (o1 - SLOT) : hdst + o1) = held[i + 1];
    }
    if constexpr (t >= RH) {
      auto unset = [&]() {
        unsigned m = 0u;
#pragma unroll
        for (int s = 0; s < 3; ++s) m = max(max(m, max(a[t][s][0], a[t][s][1])), max(a[t][s][2], a[t][s][3]));
        return __any(m == UNSET);
      };
      if (__builtin_expect(unset(), 0)) {
        int spin = 0;
#pragma unroll 1
        do { lda(t); __builtin_amdgcn_s_waitcnt(0x0F70); } while (unset() && ++spin < SPIN_MAX);
      }
      SCHED_FENCE();
    }
    if constexpr (t == RH) {
      PIN_ACC("s_nop 7");
#pragma unroll
      for (int n = 0; n < 4; ++n)
#pragma unroll
        for (int s = 0; s < 3; ++s) acc[n][s] *= 1.f - gi[s];
    }
    if constexpr (t >= RH) PIN_ACC("s_nop 1");
    if constexpr (t < NR - 1) {
#pragma unroll
      for (int s = 0; s < 3; ++s) { MFMA_ACC(0, s); MFMA_ACC(1, s); MFMA_ACC(2, s); MFMA_ACC(3, s); }
    } else {
      ldsp wp = lds + (XB + w * 9 * 1024 + l * 16);
      asm volatile("" : "+v"(wp));
#pragma unroll
      for (int k = 1; k <= 4; ++k) {
        MFMA_ACC(k & 3, 0); MFMA_ACC(k & 3, 1); MFMA_ACC(k & 3, 2);
        if (k >= 2) {
          asm volatile("s_nop 4" : "+v"(acc[k - 1][0]), "+v"(acc[k - 1][1]), "+v"(acc[k - 1][2]));
#pragma unroll
          for (int s = 0; s < 3; ++s) *(LAS f32x4*)(wp + ((k - 2) * 3 + s) * 1024) = acc[k - 1][s];
          SCHED_FENCE();
        }
      }
    }
    if constexpr (counted(t))
      asm volatile("s_mov_b64 exec, 1\n\tds_add_u32 %0, %1 offset:%2\n\ts_mov_b64 exec, -1" ::"v"(cnt), "v"(1u), "i"(4 * t) : "memory");
    SCHED_FENCE();
  });
  PIN_ACC("s_nop 7");
  const rsrc_t rout = mk_rsrc(p.out, S * D * 2);
  unsigned vo = (unsigned)((rt * 96 + sg * 48 + l15) * 1280 + (ct * 64 + kq * 16 + 4 * kg) * 2);
  asm volatile("" : "+v"(vo));
  WAIT_LGKM0();
  BARRIER();
  f32x4 part[3][4];
#pragma unroll
  for (int s = 0; s < 3; ++s)
#pragma unroll
    for (int d = 1; d < 4; ++d) part[s][d] = *(LAS f32x4*)(lds + XB + ((w ^ (2 * d)) * 9 + (d - 1) * 3 + s) * 1024 + l * 16);
  SCHED_FENCE();
#pragma unroll
  for (int s = 0; s < 3; ++s) {
    const f32x4 v = (acc[0][s] + part[s][1]) + (part[s][2] + part[s][3]);
    __builtin_amdgcn_raw_buffer_store_b64(u32x2{pk2(v[0], v[1]), pk2(v[2], v[3])}, rout, vo + s * 16 * 1280, 0, 0);
  }
}


__global__ __launch_bounds__(512, 2) void fused(Params p) {
  extern __shared__ __attribute__((aligned(16))) char smem[];
  ldsp lds = (ldsp)smem;
  u64 pc;
  asm volatile("s_getpc_b64 %0" : "=s"(pc));
  constexpr unsigned rest = CODE_BYTES;
  const int b = blockIdx.x, xcd = b & 7, idx = b >> 3, cg = xcd & 3, rg = xcd >> 2;
  const int w = __builtin_amdgcn_readfirstlane(threadIdx.x >> 6), lw = w - 4;
  const int l = threadIdx.x & 63;
  {
    const unsigned o = ((idx & 1) * 256 + w * 64 + l) * 128;
    const GAS char* a = (const GAS char*)pc + (o < rest ? o : 0u);
    unsigned keep;
    asm volatile("s_cmp_ge_u32 %3, %4\n\ts_cbranch_scc1 .Lskip%=\n\ts_mov_b32 %0, m0\n\ts_mov_b32 m0, %2\n\ts_nop 0\n\t"
                 "global_load_lds_dword %1, off\n\ts_mov_b32 m0, %0\n.Lskip%=:"
                 : "=&s"(keep) : "v"(a), "s"((unsigned)(size_t)(lds + M_DUMMY)), "s"(idx * 8 + (w & 4) * 1000), "i"(16) : "memory", "scc");
  }
  const int tm = (11 - idx) >> 31;
  const int jj = idx + ((20 * rg - 12) & tm), j3 = (jj * 43) >> 7, jr = jj - 3 * j3;
  const bool spare = jj == 39;
  const int ty = tm ? T_GLU : (jr == 0 ? T_Q : T_KV);
  const int rt = j3 + (4 * rg & ~tm);
  const int ctn = 2 * cg + jr - 1 + ((cg + 1) & tm);
  const int t2 = 28 * xcd - ((xcd - 4) & -rg) + ((idx - 4) & tm) + ((2 * j3 + jr - 1) & ~tm);
  const int rot0 = 2 * (cg & 1) + 5 * (((cg >> 1) ^ rg) & 1) + 2 * (rt & 1);
  auto get_tag = [&]() {
    u64 e;
    asm volatile("s_load_dwordx2 %0, %1, 0x0\n\ts_waitcnt lgkmcnt(0)" : "=s"(e) : "s"(p.ws + EPOCH_OFF) : "memory");
    return make_tag(e);
  };

  constexpr int PQX = 2 * SLOT;
  const bool loader = w >= 4 && !spare;
  Warm wm(lds);
  u64 tag;
  auto mfma_start = [&]() {
    if (w == 0) {
      tag = get_tag();
      if (l < 32) *(LAS unsigned*)(lds + M_CNT + l * 4) = 0u;
      if (l == 0) *(LAS u64*)(lds + M_TAG) = tag;
      WAIT_LGKM0();
    }
  };
  auto lds_tag = [&]() {
    u32x2 v;
    asm volatile("ds_read_b64 %0, %1\n\ts_waitcnt lgkmcnt(0)" : "=v"(v) : "v"((unsigned)(size_t)(lds + M_TAG)) : "memory");
    const unsigned lo = v[0], hi = v[1];
    tag = (u64)(unsigned)__builtin_amdgcn_readfirstlane(hi) << 32 | (unsigned)__builtin_amdgcn_readfirstlane(lo);
  };
  if (__builtin_expect(ty == T_Q, 0)) {
    const int ct = cg;
    bf16x8 pq[10];
    if (loader) {
      u32x4 none[29];
      float gs = p1_loader<true>(p, lds, lw, 0, rt, ct, rot0, 0, none, [&](int kb) {
        const rsrc_t ra = mk_rsrc(p.ws, A2_BYTES);
        const unsigned vo = (unsigned)(((rt * 16 + lw * 4) * KB_ALL + 48 + (kb < 4 ? 4 * ct + kb : 16 + ct)) * 1024 + l * 16);
#pragma unroll
        for (int i = 0; i < 4; ++i) st128_sc1(ra, vo + i * KB_ALL * 1024, u32x4{UNSET, UNSET, UNSET, UNSET});
      }, [&]() {
        lds_tag();
        if (lw == 0 && l == 0) __hip_atomic_store(flag_ptr(p, FL_O + rt * 4 + ct), tag, __ATOMIC_RELAXED, __HIP_MEMORY_SCOPE_AGENT);
      }, [&]() {
        {
          const unsigned o = ((j3 * 4 + lw) * 26 + min(l, 25)) * 128;
          wm.touch((const GAS char*)pc + (o < rest ? o : 0u));
        }
        for (int i = lw * 64 + l; i < 2 * KVT_SZ / 16; i += 256)
          asm volatile("ds_write_b128 %0, %1" ::"v"((unsigned)(size_t)(lds + KVT) + i * 16), "v"(u32x4{0u, 0u, 0u, 0u}) : "memory");
        if (lw == 0) poll(flag_ptr(p, FL_KV + (l & 7) * 8 + 2 * ct + ((l >> 3) & 1)), tag);
        WAIT_LGKM0();
      });
      gs += __shfl_xor(gs, 1); gs += __shfl_xor(gs, 2);
      if ((l & 3) == 0)
        __builtin_amdgcn_raw_buffer_store_b32(__builtin_bit_cast(unsigned, sigmoidf(gs)), mk_rsrc(p.ws + G_OFF, G_BYTES), (rt * 256 + 64 * ct + 16 * lw + (l >> 2)) * 4, 0, SC1);
      publish(p, lds, FL_G + rt * 4 + ct, tag, 4);
      poll(flag_ptr(p, FL_G + rt * 4 + (l & 3)), tag);
      const int tl = lw * 64 + l;
      *(LAS unsigned*)(lds + M_G + tl * 4) = __builtin_amdgcn_raw_buffer_load_b32(mk_rsrc(p.ws + G_OFF, G_BYTES), (rt * 256 + tl) * 4, 0, SC1);
      lds_wait(lds + M_END, 4);
#pragma unroll
      for (int t = 0; t < 10; ++t) { pq[t] = lds_b128(lds + PQX + (lw * 10 + t) * 1024 + l * 16); asm volatile("" : "+v"(pq[t])); }
    } else {
    mfma_start();
    f32x16 acc[5][2];
#pragma unroll
    for (int n = 0; n < 5; ++n) { acc[n][0] = f32x16{}; acc[n][1] = f32x16{}; }
    p1_main_q(lds, w, acc);
    const Consts cn(1.f);
#pragma unroll
    for (int k = 9; k >= 0; --k)
#pragma unroll
      for (int s = 0; s < 2; ++s) {
        const f32x16& c = acc[k >> 1][s];
        const int o = 8 * (k & 1);
        const bf16x8 f = pack44(phi4(f32x4{c[o], c[o + 1], c[o + 2], c[o + 3]}, cn), phi4(f32x4{c[o + 4], c[o + 5], c[o + 6], c[o + 7]}, cn));
        if (k >= 5) *(LAS bf16x8*)(lds + PQX + (w * 10 + 5 * s + k - 5) * 1024 + l * 16) = f;
        else { pq[5 * s + k] = f; asm volatile("" : "+v"(pq[5 * s + k])); }
        if (k == 5 && s == 1) { WAIT_LGKM0(); lds_arrive(lds + M_END); }
      }
    }
    {
      const int ch = min((int)threadIdx.x, 419), hg = ch >= 210;
      ldsp dp[2];
      bool dup[2];
#pragma unroll
      for (int u = 0; u < 2; ++u) {
        const int un = 2 * (ch - 210 * hg) + u, row = un / 5, dq = un % 5, hd = row < 80 ? row / 20 : row - 80, run = 5 * hd + dq;
        dp[u] = lds + KVT + hg * KVT_SZ + (32 * hd + (row < 80 ? row - 20 * hd : 20)) * KS + ((run >> 2) - hd) * 32 + (run & 1) * 16 + (run & 2) * 4;
        dup[u] = row >= 80;
      }
      const rsrc_t rk = mk_rsrc(p.ws + KVP_OFF, KVP_BYTES);
      const unsigned po = (2 * ct + hg) * KVP_SZ + (ch - 210 * hg) * 16;
      u32x4 pv[8];
      unsigned todo = 0xFFu;
#pragma unroll 1
      for (int spin = 0; spin < SPIN_MAX && todo; ++spin) {
#pragma unroll
        for (int r = 0; r < 8; ++r)
          if (todo >> r & 1) pv[r] = __builtin_amdgcn_raw_buffer_load_b128(rk, po, r * 8 * KVP_SZ, SC1);
#pragma unroll
        for (int r = 0; r < 8; ++r)
          if (__all(max(max(pv[r][0], pv[r][1]), max(pv[r][2], pv[r][3])) != UNSET)) todo &= ~(1u << r);
        asm volatile("s_sleep 1" ::: "memory");
      }
      f32x2 sum[4];
#pragma unroll
      for (int e = 0; e < 4; ++e) sum[e] = f32x2{0.f, 0.f};
#pragma unroll
      for (int r = 0; r < 8; ++r)
#pragma unroll
        for (int e = 0; e < 4; ++e) {
          const unsigned x = pv[r][e];
          sum[e] += f32x2{__uint_as_float(x << 16), __uint_as_float(x & 0xFFFF0000u)};
        }
#pragma unroll
      for (int u = 0; u < 2; ++u) {
        const u32x2 v = {pk2(sum[2 * u][0], sum[2 * u][1]), pk2(sum[2 * u + 1][0], sum[2 * u + 1][1])};
        *(LAS u32x2*)dp[u] = v;
        if (dup[u]) *(LAS u32x2*)(dp[u] + 4 * KS) = v;
      }
      WAIT_LGKM0();
      BARRIER();
    }
  {
    const int l31 = l & 31, h = l >> 5, hg = w >> 2, wr = w & 3;
    const rsrc_t ra = mk_rsrc(p.ws, A2_BYTES);
    const unsigned vo = (unsigned)(((rt * 16 + wr * 4 + ((l >> 4) & 1)) * KB_ALL + 48) * 1024 + (2 * ct + hg) * 2048 + h * 256 + (l & 15) * 16);
    const unsigned vx = vo + 16384 - (2 * ct + hg) * 1536;
    bf16x8 af[4][2];
#pragma unroll
    for (int hd = 0; hd < 4; ++hd)
#pragma unroll
      for (int t = 0; t < 2; ++t) af[hd][t] = lds_b128(lds + (KVT + hg * KVT_SZ + l31 * KS + h * 16) + 32 * hd * KS + t * 32);
    float gv[2];
#pragma unroll
    for (int s = 0; s < 2; ++s) gv[s] = *(LAS float*)(lds + M_G + (64 * wr + 32 * s + l31) * 4);
    SCHED_FENCE();
    f32x16 acc[2][4];
#pragma unroll
    for (int s = 0; s < 2; ++s)
#pragma unroll
      for (int hd = 3; hd >= 0; --hd) acc[s][hd] = mfma32(af[hd][1], pq[5 * s + hd + 1], mfma32(af[hd][0], pq[5 * s + hd], f32x16{}));
    u32x4 xs[2];
#pragma unroll
    for (int s = 0; s < 2; ++s) {
      u32x4 pc[4];
      unsigned ex[4][2];
#pragma unroll
      for (int hd = 3; hd >= 0; --hd) {
        const f32x16& c = acc[s][hd];
        float d0 = c[12], d1 = c[8];
        asm volatile("" : "+v"(d0), "+v"(d1));
        const float f = gv[s] * __builtin_amdgcn_rcpf((h ? d1 : d0) + 1e-6f);
        const f32x2 ff = {f, f};
        unsigned r[6];
#pragma unroll
        for (int i = 0; i < 6; ++i) { const f32x2 y = f32x2{c[2 * i], c[2 * i + 1]} * ff; r[i] = pk2(y[0], y[1]); }
        pc[hd] = u32x4{r[0], r[1], r[2], r[3]};
        ex[hd][0] = r[4]; ex[hd][1] = r[5];
        if (hd > 0) st128_sc1(ra, vo + s * 2 * KB_ALL * 1024 + hd * 512, pc[hd]);
      }
      u32x4 x;
#pragma unroll
      for (int i = 0; i < 4; ++i) x[i] = __builtin_amdgcn_permlane32_swap(ex[i >> 1][i & 1], ex[2 + (i >> 1)][i & 1], false, false)[0];
      st128_sc1(ra, vo + s * 2 * KB_ALL * 1024, pc[0]);
      xs[s] = x;
    }
    st128_sc1(ra, vx, xs[0]);
    st128_sc1(ra, vx + 2 * KB_ALL * 1024, xs[1]);
    return;
  }
  }
  const int ct = ctn;
  u32x4 held[15];
  if (__builtin_expect(loader, 1)) {
    u32x4 pre[29];
    p1_loader<false>(p, lds, lw, tm, rt, ct, rot0, t2, pre, [&](int) {
      st128_sc1(mk_rsrc(p.ws + KVP_OFF + (rt * 8 + ct) * KVP_SZ, 3360), lw * 1024 + l * 16, u32x4{UNSET, UNSET, UNSET, UNSET});
    }, [&]() {
      lds_tag();
      if (lw == 0 && l == 0) __hip_atomic_store(flag_ptr(p, FL_KV + rt * 8 + ct), tag, __ATOMIC_RELAXED, __HIP_MEMORY_SCOPE_AGENT);
    }, [&]() { lds_tag(); });
    const W2Src src(p, lw, t2);
    src.fill<0, 24>(lds + lw * 1024);
    if (ty == T_KV) {
#pragma unroll
      for (int i = 0; i < 29; ++i) pre[i] = pre_load(src, i);
      SCHED_FENCE();
      BARRIER(); BARRIER();
    } else {
      lds_wait(lds + M_END, 4);
    }
    WAIT_VM(0);
#pragma unroll
    for (int i = 0; i < 15; ++i) *(LAS u32x4*)(lds + (lw * 1024 + l * 16) + w2_off(24 + i)) = pre[i];
#pragma unroll
    for (int i = 0; i < 14; ++i) held[i] = pre[15 + i];
    held[14] = pre[28];
    WAIT_LGKM0();
  } else {
    mfma_start();
    if (spare) {
#pragma unroll 1
      for (int n = cg * 8 + w; n < 220; n += 32) {
        const int k = n / 5, line = (n - 5 * k) * 64 + l, r = line / 10;
        const int st = k < 15 ? 24 + k : k < 29 ? 10 + 2 * k : 2 * k - 19;
        const int row = st < 48 ? 32 * st + r : st < 64 ? 40 * (st - 48) + 20 * (r >> 4) + (r & 15)
                                             : 80 * (2 * (st - 64) + (r >> 4)) + 16 + 20 * ((r >> 3) & 1) + 40 * ((r >> 2) & 1) + (r & 3);
        wm.touch((const GAS char*)(st < 48 ? p.wd : p.wo) + row * 1280 + (line - 10 * r) * 128);
        WAIT_VM(1);
      }
      if (cg == 0 && w == 0) {
#pragma unroll 1
        for (int i = 0; i < 5; ++i) poll(flag_ptr(p, min(i * 64 + l, NFLAG - 1)), tag);
        if (l == 0) __hip_atomic_store((GAS u64*)(p.ws + EPOCH_OFF), tag, __ATOMIC_RELAXED, __HIP_MEMORY_SCOPE_AGENT);
      }
      return;
    }
      f32x4 acc[4][10];
#pragma unroll
      for (int i = 0; i < 4; ++i)
#pragma unroll
        for (int j = 0; j < 10; ++j) acc[i][j] = f32x4{};
      if (ty == T_GLU) {
        p1_main16<T_GLU>(lds, w, acc);
        const Consts cn(-1.f);
        const rsrc_t ra = mk_rsrc(p.ws, A2_BYTES);
        const unsigned vo = (unsigned)((rt * 10 * KB_ALL + ct * 4 + w) * 1024 + l * 16);
#pragma unroll
        for (int j = 0; j < 10; ++j) {
          st128_sc1(ra, vo + j * KB_ALL * 1024, __builtin_bit_cast(u32x4, pack44(glu4(acc[0][j], acc[2][j], cn), glu4(acc[1][j], acc[3][j], cn))));
          SCHED_FENCE();
        }
        lds_tag();
        publish(p, lds, FL_H + rt * 12 + ct, tag, 4);
      } else {
        int goff[2];
#pragma unroll
        for (int i = 0; i < 2; ++i) {
          const int en = min(2 * (int)threadIdx.x + i, 419), row = en / 5, dq = en % 5;
          const int hd = row < 80 ? row / 20 : row - 80, d0 = 20 * hd + 4 * dq, ti = d0 >> 4, tj = row >> 4;
          const int tile = row >= 80 ? 9 : ti == tj ? ti : (ti == 0 || ti == 4) ? 8 : 4 + ti;
          goff[i] = tile * 1024 + (((d0 & 15) >> 2) * 16 + (row < 80 ? row & 15 : ti)) * 16;
          asm volatile("" : "+v"(goff[i]));
        }
        p1_main16<T_KV>(lds, w, acc);
        const Consts cn(1.f);
        const int l15 = l & 15, g = l >> 4;
        ldsp tp = lds + (w * 10240 + l * 16);
        bf16x8 vf[2][5];
#pragma unroll
        for (int u = 0; u < 2; ++u)
#pragma unroll
          for (int j = 0; j < 5; ++j) {
            vf[u][j] = pack44(acc[2 * u][5 + j], acc[2 * u + 1][5 + j]);
            asm volatile("" : "+v"(vf[u][j]));
          }
        f32x4 z = {}, dg[5], lo[5], hi[5];
        auto kfrag = [&](int n) { return pack44(phi4(acc[2 * (n & 1)][n >> 1], cn), phi4(acc[2 * (n & 1) + 1][n >> 1], cn)); };
        bf16x8 kf = kfrag(0);
        SCHED_FENCE();
        unsigned seen = 0;
#pragma unroll
        for (int n = 0; n < 10; ++n) {
          const int i = n >> 1, u = n & 1;
          const unsigned o1 = l15 == i ? 0x3F803F80u : 0u;
          const bf16x8 one = __builtin_bit_cast(bf16x8, u32x4{o1, o1, o1, o1});
          if (u == 0) { dg[i] = f32x4{}; lo[i] = f32x4{}; hi[i] = f32x4{}; }
          if (i > 0) lo[i] = mfma16(kf, vf[u][i - 1], lo[i]);
          if (i < 4) hi[i] = mfma16(kf, vf[u][i + 1], hi[i]);
          dg[i] = mfma16(kf, vf[u][i], dg[i]);
          z = mfma16(kf, one, z);
          if (n == 1) seen = __hip_atomic_load((LAS unsigned*)(lds + M_END), __ATOMIC_RELAXED, __HIP_MEMORY_SCOPE_WORKGROUP);
          if (n == 2) lds_check<4, 0>(seen, (unsigned)(size_t)(lds + M_END));
          if (i > 0) {
            if (u == 0) *(LAS f32x4*)(tp + (i - 1) * 1024) = dg[i - 1];
            else if (i > 1) *(LAS f32x4*)(tp + (3 + i) * 1024) = g < i - 1 ? lo[i - 1] : hi[i - 1];
          }
          if (n < 9) kf = kfrag(n + 1);
          SG_MFMA(1); SG_VALU(4); SG_TR(2); SG_MFMA(1); SG_VALU(4); SG_TR(2); SG_MFMA(1); SG_VALU(4); SG_TR(2); SG_MFMA(1);
          SCHED_FENCE();
        }
        *(LAS f32x4*)(tp + 8 * 1024) = l15 < 8 ? hi[0] : lo[4];
        *(LAS f32x4*)(tp + 4 * 1024) = dg[4];
        *(LAS f32x4*)(tp + 9 * 1024) = z;
        WAIT_LGKM0();
        BARRIER();
        const rsrc_t rk = mk_rsrc(p.ws + KVP_OFF + (rt * 8 + ct) * KVP_SZ, 3360);
        f32x4 part[2][4];
#pragma unroll
        for (int i = 0; i < 2; ++i)
#pragma unroll
          for (int ww = 0; ww < 4; ++ww) part[i][ww] = *(LAS f32x4*)(lds + goff[i] + ww * 10240);
        SCHED_FENCE();
        u32x4 res = __builtin_bit_cast(u32x4, pack44((part[0][0] + part[0][1]) + (part[0][2] + part[0][3]), (part[1][0] + part[1][1]) + (part[1][2] + part[1][3])));
#pragma unroll
        for (int e = 0; e < 4; ++e) res[e] = min(res[e], UNSET - 1);
        st128_sc1(rk, threadIdx.x * 16, res);
        WAIT_LGKM0();
        BARRIER();
      }
      const W2Src src(p, w, t2);
#pragma unroll
      for (int i = 0; i < 15; ++i) held[i] = src.load(39 + 2 * i);
      SCHED_FENCE();
  }
  int t2l = __builtin_amdgcn_readfirstlane(t2);
  asm volatile("" : "+s"(t2l));
  const int rt2 = (t2l * 205) >> 11;
  p2_main(p, lds, w, rt2, t2l - 10 * rt2, tag, held);
}

extern "C" void kernel_launch(void* const* d_in, const int* in_sizes, int n_in, void* d_out, int out_size, void* d_ws, size_t ws_size,
                              hipStream_t stream) {
  Params p;
  p.x = (const u16*)d_in[0]; p.wqkv = (const u16*)d_in[1]; p.wo = (const u16*)d_in[2];
  p.wg = (const u16*)d_in[4]; p.wu = (const u16*)d_in[5]; p.wd = (const u16*)d_in[6]; p.gw = (const u16*)d_in[7];
  p.out = (u16*)d_out; p.ws = (char*)d_ws;
  (void)hipFuncSetAttribute((const void*)fused, hipFuncAttributeMaxDynamicSharedMemorySize, LDS_BYTES);
  hipLaunchKernelGGL(fused, dim3(256), dim3(512), LDS_BYTES, stream, p);
}
```
